```python
import torch

def _softmax(x, axis=-1):
    return torch.softmax(x, dim=axis)
def _gelu(x):
    return torch.nn.functional.gelu(x, approximate="tanh")
def _silu(x):
    return torch.nn.functional.silu(x)
def _sigmoid(x):
    return torch.sigmoid(x)
def _layernorm(x, axis=-1, eps=1e-5):
    m = x.mean(dim=axis, keepdim=True)
    v = x.var(dim=axis, keepdim=True, unbiased=False)
    return (x - m) / torch.sqrt(v + eps)
def _scatter_add(tgt, idx, src, axis=0):
    return tgt.clone().index_add_(axis, idx, src)
def _segment_sum(src, seg, n):
    out = torch.zeros((n,) + src.shape[1:], dtype=src.dtype)
    return out.index_add_(0, seg, src)
def _pad(x, pw):
    flat = tuple(v for pair in reversed(pw) for v in pair)
    return torch.nn.functional.pad(x, flat)

kernel_name = 'interleave_layers__linear_attn_block__mixer_token_mlp__prompt'

def setup_inputs():

    _w = (lambda a: (lambda t: t.to(torch.bfloat16) if t.is_floating_point() else t)(torch.as_tensor(a)))
    def _A_setup():
        B, S, D = 1, 2048, 448
        return {
            "x": _w((lambda *s: torch.randn(*s, dtype=torch.bfloat16) * 0.05)(B, S, D)),
            "wqkv": _w((lambda *s: torch.randn(*s, dtype=torch.bfloat16) * 0.1)(D, 3 * D)),
            "wo": _w((lambda *s: torch.randn(*s, dtype=torch.bfloat16) * 0.05)(D, D)),
        }
    _A_in = _A_setup()

    def _B_setup():
        B, S, D, F = 1, 2048, 448, 1792
        return {
            "x": _w((lambda *s: torch.randn(*s, dtype=torch.bfloat16) * 0.05)(B, S, D)),
            "w1": _w((lambda *s: torch.randn(*s, dtype=torch.bfloat16) * 0.8)(S, F) * (S ** -0.5)),
            "b1": _w((lambda *s: torch.randn(*s, dtype=torch.bfloat16) * 12.8)(F) * 0.02),
            "w2": _w((lambda *s: torch.randn(*s, dtype=torch.bfloat16) * 0.4)(F, S) * (F ** -0.5)),
            "b2": _w((lambda *s: torch.randn(*s, dtype=torch.bfloat16) * 6.4)(S) * 0.02),
        }
    _B_in = _B_setup()

    return {
        "A_x": _A_in["x"],
        "A_wqkv": _A_in["wqkv"],
        "A_wo": _A_in["wo"],
        "B_x": _B_in["x"],
        "B_w1": _B_in["w1"],
        "B_b1": _B_in["b1"],
        "B_w2": _B_in["w2"],
        "B_b2": _B_in["b2"],
    }

def reference(A_x, A_wqkv, A_wo, B_x, B_w1, B_b1, B_w2, B_b2):
    def _A_ref(x, wqkv, wo):
        B, S, D = x.shape
        H = 16; Dh = D // H
        qkv = (lambda x, *d: x.permute(*d))((x @ wqkv).reshape(B, S, 3, H, Dh), 2, 0, 3, 1, 4)
        q, k, v = qkv[0], qkv[1], qkv[2]
        phi = lambda t: torch.where(t > 0, t + 1, torch.exp(t)) + 1e-6
        pq, pk = phi(q), phi(k)
        kv = torch.einsum("bhsd,bhse->bhde", pk, v)
        z = torch.einsum("bhsd->bhd", pk)
        num = torch.einsum("bhqd,bhde->bhqe", pq, kv)
        den = torch.einsum("bhqd,bhd->bhq", pq, z)[..., None] + 1e-6
        o = num / den
        return (lambda x, *d: x.permute(*d))(o, 0, 2, 1, 3).reshape(B, S, D) @ wo

    def _B_ref(x, w1, b1, w2, b2):
        B, S, D = x.shape
        h = _layernorm(x, axis=-1)
        u = torch.einsum("bsd,sf->bdf", h, w1) + b1
        u = _gelu(u)
        y = torch.einsum("bdf,fs->bds", u, w2) + b2
        return (lambda x, *d: x.permute(*d))(y, 0, 2, 1)

    x = A_x
    x = x + _A_ref(_layernorm(x, axis=-1), A_wqkv, A_wo)
    x = x + _B_ref(_layernorm(x, axis=-1), B_w1, B_b1, B_w2, B_b2)
    x = x + _A_ref(_layernorm(x, axis=-1), A_wqkv, A_wo)
    x = x + _B_ref(_layernorm(x, axis=-1), B_w1, B_b1, B_w2, B_b2)
    return x
```

```cpp
#include <hip/hip_runtime.h>
#include <hip/hip_bf16.h>
#include <stdint.h>

typedef unsigned short u16;
__device__ __forceinline__ float bf2f(u16 b){ return __builtin_bit_cast(float, ((unsigned)b)<<16); }
__device__ __forceinline__ u16 f2bf(float f){ unsigned u=__builtin_bit_cast(unsigned,f); u += 0x7FFFu + ((u>>16)&1u); return (u16)(u>>16); }

constexpr int S=2048, D=448, H=16, DH=28, F=1792;

__global__ void k_ln(const float* x, float* y, int times){
  int s = blockIdx.x; int t = threadIdx.x;
  float v[7];
  for(int i=0;i<7;i++) v[i]=x[s*D+t+64*i];
  for(int it=0; it<times; ++it){
    float sum=0; for(int i=0;i<7;i++) sum+=v[i];
    for(int o=32;o;o>>=1) sum+=__shfl_xor(sum,o);
    float m=sum/D; float q=0; for(int i=0;i<7;i++){ v[i]-=m; q+=v[i]*v[i]; }
    for(int o=32;o;o>>=1) q+=__shfl_xor(q,o);
    float r=1.0f/sqrtf(q/D+1e-5f);
    for(int i=0;i<7;i++) v[i]*=r;
  }
  for(int i=0;i<7;i++) y[s*D+t+64*i]=bf2f(f2bf(v[i]));
}
__global__ void k_cvt(const u16* a, float* b, int n){ int i=blockIdx.x*256+threadIdx.x; if(i<n) b[i]=bf2f(a[i]); }
__global__ void k_cvt2(const float* a, u16* b, int n){ int i=blockIdx.x*256+threadIdx.x; if(i<n) b[i]=f2bf(a[i]); }
__global__ void k_gemm(const float* A, int sam, int sak, const u16* B, float* C, int M, int N, int K){
  __shared__ float As[16][17], Bs[16][17];
  int tx=threadIdx.x&15, ty=threadIdx.x>>4;
  int m=blockIdx.y*16+ty, n=blockIdx.x*16+tx;
  float acc=0;
  for(int k0=0;k0<K;k0+=16){
    As[ty][tx]=A[(size_t)m*sam+(size_t)(k0+tx)*sak];
    Bs[ty][tx]=bf2f(B[(size_t)(k0+ty)*N+n]);
    __syncthreads();
    for(int k=0;k<16;k++) acc+=As[ty][k]*Bs[k][tx];
    __syncthreads();
  }
  C[(size_t)m*N+n]=acc;
}
__device__ float phi(float t){ return (t>0? t+1.0f : __expf(t)) + 1e-6f; }
__global__ void k_kv(const float* qkv, float* kv){
  int h=blockIdx.x/DH, d=blockIdx.x%DH; int e=threadIdx.x;
  if(e>DH) return;
  float acc=0;
  for(int s=0;s<S;s++){ float pk=phi(qkv[s*3*D+D+h*DH+d]); float v = e<DH? qkv[s*3*D+2*D+h*DH+e] : 1.0f; acc+=pk*v; }
  kv[(h*DH+d)*32+e]=acc;
}
__global__ void k_o(const float* qkv, const float* kv, float* o){
  int s=blockIdx.x, c=threadIdx.x; int h=c/DH, e=c%DH;
  float num=0, den=0;
  for(int d=0;d<DH;d++){ float pq=phi(qkv[s*3*D+h*DH+d]); num+=pq*kv[(h*DH+d)*32+e]; den+=pq*kv[(h*DH+d)*32+DH]; }
  o[s*D+c]=bf2f(f2bf(num/(den+1e-6f)));
}
__global__ void k_add(float* x, const float* y, int n){ int i=blockIdx.x*256+threadIdx.x; if(i<n) x[i]=bf2f(f2bf(x[i]+y[i])); }
__global__ void k_gelu(float* u, const u16* b1){ int i=blockIdx.x*256+threadIdx.x; int f=i%F; float t=u[i]+bf2f(b1[f]);
  float g=0.5f*t*(1.0f+tanhf(0.7978845608f*(t+0.044715f*t*t*t))); u[i]=bf2f(f2bf(g)); }
__global__ void k_addT(float* x, const float* y, const u16* b2){ int i=blockIdx.x*256+threadIdx.x; int s=i/D, d=i%D; x[i]=bf2f(f2bf(x[i]+y[d*S+s]+bf2f(b2[s]))); }

extern "C" void kernel_launch(void* const* d_in, const int* in_sizes, int n_in,
                              void* d_out, int out_size, void* d_ws, size_t ws_size,
                              hipStream_t stream) {
  const u16* x_in=(const u16*)d_in[0]; const u16* wqkv=(const u16*)d_in[1]; const u16* wo=(const u16*)d_in[2];
  const u16* w1=(const u16*)d_in[4]; const u16* b1=(const u16*)d_in[5]; const u16* w2=(const u16*)d_in[6]; const u16* b2=(const u16*)d_in[7];
  float* ws=(float*)d_ws;
  float* x=ws; float* xn=x+S*D; float* qkv=xn+S*D; float* kv=qkv+S*3*D; float* o=kv+H*DH*32; float* t=o+S*D; float* u=t+S*D;
  k_cvt<<<S*D/256,256,0,stream>>>(x_in,x,S*D);
  for(int l=0;l<2;l++){
    k_ln<<<S,64,0,stream>>>(x,xn,1);
    k_gemm<<<dim3(3*D/16,S/16),256,0,stream>>>(xn,D,1,wqkv,qkv,S,3*D,D);
    k_kv<<<H*DH,64,0,stream>>>(qkv,kv);
    k_o<<<S,448,0,stream>>>(qkv,kv,o);
    k_gemm<<<dim3(D/16,S/16),256,0,stream>>>(o,D,1,wo,t,S,D,D);
    k_add<<<S*D/256,256,0,stream>>>(x,t,S*D);
    k_ln<<<S,64,0,stream>>>(x,xn,2);
    k_gemm<<<dim3(F/16,D/16),256,0,stream>>>(xn,1,D,w1,u,D,F,S);
    k_gelu<<<D*F/256,256,0,stream>>>(u,b1);
    k_gemm<<<dim3(S/16,D/16),256,0,stream>>>(u,F,1,w2,t,D,S,F);
    k_addT<<<S*D/256,256,0,stream>>>(x,t,b2);
  }
  k_cvt2<<<S*D/256,256,0,stream>>>(x,(u16*)d_out,S*D);
}
```

```cpp
#include <hip/hip_runtime.h>

struct Params {
  const void *x, *wqkv, *wo, *w1, *b1, *w2, *b2;
  void *out, *ws;
};
constexpr int LDS_BYTES = 160640;

__global__ void __launch_bounds__(256, 1) fused_kernel(Params p) {
  asm volatile(
      "s_mov_b64 s[16:17], %0\n"
      "s_mov_b32 s18, %1\n"
      "v_mov_b32 v0, %2\n"
      ".Lcode_begin:\n"
      "s_load_dwordx16 s[80:95], s[16:17], 0\n"
      "s_load_dwordx2 s[20:21], s[16:17], 64\n"
      "s_getpc_b64 s[48:49]\n"
      "v_lshrrev_b32 v2, 6, v0\n"
      "v_and_b32 v0, 63, v0\n"
      "v_readfirstlane_b32 s52, v2\n"
      "v_lshlrev_b32 v1, 4, v0\n"
      "s_and_b32 s49, s49, 0xffff\n"
      "s_mov_b32 s50, .Lcode_end-.Lcode_begin-64\n"
      "s_mov_b32 s51, 0x20000\n"
      "s_lshr_b32 s79, s18, 3\n"
      "s_lshl2_add_u32 s78, s79, s52\n"
      "s_lshl_b32 s78, s78, 10\n"
      "buffer_load_dwordx4 v[252:255], v1, s[48:51], s78 offen\n"
      "s_and_b32 s4, s79, 7\n"
      "s_lshr_b32 s5, s79, 3\n"
      "s_and_b32 s6, s18, 1\n"
      "s_bfe_u32 s7, s18, 0x20001\n"
      "s_and_b32 s97, s18, 3\n"
      "s_lshl_b32 s97, s97, 2\n"
      "s_and_b32 s53, s79, 3\n"
      "s_add_u32 s97, s97, s53\n"
      "s_bfe_u32 s53, s18, 0x10002\n"
      "s_lshl_b32 s53, s53, 3\n"
      "s_lshr_b32 s8, s79, 2\n"
      "s_add_u32 s53, s53, s8\n"
      "s_lshl2_add_u32 s57, s6, s5\n"
      "s_and_b32 s8, s79, 3\n"
      "s_lshl2_add_u32 s8, s6, s8\n"
      "s_or_b32 s9, s4, 3\n"
      "s_cmp_lt_u32 s79, 24\n"
      "s_cselect_b32 s57, s57, s9\n"
      "s_cselect_b32 s4, s4, s8\n"
      "s_lshl3_add_u32 s56, s7, s4\n"
      "s_mul_i32 s98, s7, 7\n"
      "s_add_u32 s98, s98, s4\n"
      "s_cmp_lt_u32 s57, 7\n"
      "s_cselect_b32 s54, 1, 0\n"
      "s_cmp_lt_u32 s4, 7\n"
      "s_cselect_b32 s55, s54, 0\n"
      "s_lshl3_add_u32 s13, s56, s57\n"
      "s_and_b32 s9, s79, 3\n"
      "s_cmp_eq_u32 s9, 3\n"
      "s_cselect_b32 s9, 3, s8\n"
      "s_cmp_eq_u32 s54, 0\n"
      "s_cselect_b32 s57, s9, s57\n"
      "s_mov_b32 s5, s57\n"
      "s_mul_i32 s98, s7, 7\n"
      "s_add_u32 s98, s98, s57\n"
      "s_mov_b32 s5, s4\n"
      "s_mov_b32 s4, s57\n"
      "s_lshr_b32 s8, s79, 3\n"
      "s_and_b32 s9, s79, 3\n"
      "s_add_u32 s9, s9, 3\n"
      "s_cmp_eq_u32 s54, 0\n"
      "s_cselect_b32 s8, s9, s8\n"
      "s_cmp_eq_u32 s55, 0\n"
      "s_cselect_b32 s5, s8, s5\n"
      "s_and_b32 s56, s18, 7\n"
      "s_and_b32 s9, s79, 3\n"
      "s_lshl2_add_u32 s56, s56, s9\n"
      "s_lshr_b32 s57, s79, 2\n"
      "s_lshl3_add_u32 s13, s56, s57\n"
      "s_lshl_b32 s9, s9, 1\n"
      "s_cmp_eq_u32 s54, 0\n"
      "s_cselect_b32 s57, s9, s57\n"
      "v_and_b32 v207, 31, v0\n"
      "v_mul_u32_u24 v207, 0x380, v207\n"
      "v_lshrrev_b32 v205, 5, v0\n"
      "v_lshl_add_u32 v207, v205, 4, v207\n"
      "v_lshrrev_b32 v201, 2, v0\n"
      "v_mul_u32_u24 v201, 0xa80, v201\n"
      "v_and_b32 v200, 3, v0\n"
      "v_lshl_add_u32 v200, v200, 4, v201\n"
      "s_lshr_b32 s8, s52, 1\n"
      "s_lshl1_add_u32 s8, s56, s8\n"
      "s_and_b32 s12, s52, 1\n"
      "s_mul_i32 s96, s8, 0x7000\n"
      "s_lshl2_add_u32 s9, s57, s12\n"
      "s_lshl_b32 s9, s9, 5\n"
      "s_add_u32 s96, s96, s9\n"
      "s_mul_i32 s10, s53, 0x12600\n"
      "s_mul_i32 s11, s97, 56\n"
      "s_and_b32 s11, s11, 0xfffffff0\n"
      "s_add_u32 s10, s10, s11\n"
      "s_mul_i32 s11, s52, 0x380\n"
      "s_add_u32 s10, s10, s11\n"
      "s_cmp_eq_u32 s52, 3\n"
      "s_cselect_b32 s10, 0x200000, s10\n"
      "s_add_u32 s11, s10, 0x9300\n"
      "s_and_b32 s99, s5, 3\n"
      "s_lshl1_add_u32 s99, s99, s6\n"
      "s_mov_b32 s99, s5\n"
      "s_and_b32 s64, s4, 1\n"
      "s_mul_i32 s64, s64, s55\n"
      "s_and_b32 s79, s56, 1\n"
      "s_xor_b32 s2, s64, 0\n"
      "s_add_u32 s2, s2, s99\n"
      "s_and_b32 s2, s2, 7\n"
      "s_lshl_b32 s2, s2, 12\n"
      "s_xor_b32 s3, s64, 1\n"
      "s_add_u32 s3, s3, s99\n"
      "s_and_b32 s3, s3, 7\n"
      "s_lshl_b32 s3, s3, 12\n"
      "s_lshl_b32 s76, s52, 12\n"
      "s_mul_i32 s74, s52, 0x1c0000\n"
      "s_lshl_b32 s9, s98, 7\n"
      "s_add_u32 s74, s74, s9\n"
      "v_bfe_u32 v204, v0, 4, 1\n"
      "v_mul_u32_u24 v217, 6, v204\n"
      "v_and_b32 v216, 7, v0\n"
      "v_xor_b32 v217, v217, v216\n"
      "v_lshlrev_b32 v217, 4, v217\n"
      "v_lshrrev_b32 v216, 3, v0\n"
      "v_lshl_add_u32 v6, v216, 12, v217\n"
      "v_mul_u32_u24 v216, 0xe00, v216\n"
      "v_add_u32 v11, v216, v217\n"
      "s_waitcnt lgkmcnt(0)\n"
      "s_mov_b32 s24, s80\n"
      "s_and_b32 s25, s81, 0xffff\n"
      "s_mov_b32 s26, 0x1c0000\n"
      "s_mov_b32 s27, 0x20000\n"
      "s_mov_b32 s28, s82\n"
      "s_and_b32 s29, s83, 0xffff\n"
      "s_mov_b32 s30, 0x126000\n"
      "s_mov_b32 s31, 0x20000\n"
      "s_and_b32 s21, s21, 0xffff\n"
      "s_mov_b32 s22, 0x1289000\n"
      "s_mov_b32 s23, 0x20000\n"
      "buffer_load_dwordx4 v[18:21], v207, s[24:27], s96 offen\n"
      "buffer_load_dwordx4 v[22:25], v207, s[24:27], s96 offen offset:64\n"
      "v_mov_b32 v128, 0\n"
      "v_mov_b32 v129, 0\n"
      "v_mov_b32 v130, 0\n"
      "v_mov_b32 v131, 0\n"
      "v_mov_b32 v132, 0\n"
      "v_mov_b32 v133, 0\n"
      "v_mov_b32 v134, 0\n"
      "v_mov_b32 v135, 0\n"
      "s_mov_b32 exec_hi, 0xffffff\n"
      "buffer_load_dwordx4 v[128:131], v200, s[28:31], s10 offen\n"
      "buffer_load_dwordx4 v[132:135], v200, s[28:31], s11 offen\n"
      "s_mov_b32 exec_hi, -1\n"
      "buffer_load_dword v209, off, s[20:23], 0 sc1\n"
      "v_and_b32 v27, 31, v0\n"
      "s_mov_b32 s48, s92\n"
      "s_and_b32 s49, s93, 0xffff\n"
      "s_mov_b32 s50, 0x1000\n"
      "s_mov_b32 s51, 0x20000\n"
      "v_lshlrev_b32 v207, 1, v27\n"
      "s_lshl_b32 s9, s8, 6\n"
      "buffer_load_ushort v26, v207, s[48:51], s9 offen\n"
      "s_mov_b32 s48, s88\n"
      "s_and_b32 s49, s89, 0xffff\n"
      "s_mov_b32 s50, 0xe00\n"
      "s_mov_b32 s51, 0x20000\n"
      "v_lshlrev_b32 v207, 3, v205\n"
      "s_lshl2_add_u32 s9, s98, s52\n"
      "s_lshl_b32 s9, s9, 5\n"
      "buffer_load_dwordx2 v[140:141], v207, s[48:51], s9 offen\n"
      "buffer_load_dwordx2 v[142:143], v207, s[48:51], s9 offen offset:16\n"
      "s_mov_b32 s32, s84\n"
      "s_and_b32 s33, s85, 0xffff\n"
      "s_mov_b32 s34, 0x62000\n"
      "s_mov_b32 s35, 0x20000\n"
      "s_mov_b32 s36, s86\n"
      "s_and_b32 s37, s87, 0xffff\n"
      "s_mov_b32 s38, 0x700000\n"
      "s_mov_b32 s39, 0x20000\n"
      "s_mov_b32 s40, s90\n"
      "s_and_b32 s41, s91, 0xffff\n"
      "s_mov_b32 s42, 0x700000\n"
      "s_mov_b32 s43, 0x20000\n"
      "s_mov_b32 s44, s94\n"
      "s_and_b32 s45, s95, 0xffff\n"
      "s_mov_b32 s46, 0x1c0000\n"
      "s_mov_b32 s47, 0x20000\n"
      "s_cmp_eq_u32 s55, 0\n"
      "s_cbranch_scc1 .Lno_w1\n"
      "s_mul_i32 s78, s2, 56\n"
      "s_add_u32 s78, s78, s74\n"
      "s_add_u32 m0, s76, 0xc000\n"
      "s_nop 0\n"
      "buffer_load_dwordx4 v11, s[36:39], s78 offen lds\n"
      "s_add_u32 s78, s78, 0x6c00\n"
      "buffer_load_dwordx4 v11, s[36:39], s78 offen offset:1024 lds\n"
      "s_add_u32 s78, s78, 0x6c00\n"
      "buffer_load_dwordx4 v11, s[36:39], s78 offen offset:2048 lds\n"
      "s_add_u32 s78, s78, 0x6c00\n"
      "buffer_load_dwordx4 v11, s[36:39], s78 offen offset:3072 lds\n"
      "s_mul_i32 s78, s2, 56\n"
      "s_add_u32 s78, s78, s74\n"
      "s_add_u32 m0, s76, 0x10000\n"
      "s_add_u32 s78, s78, 0x1c000\n"
      "buffer_load_dwordx4 v11, s[36:39], s78 offen lds\n"
      "s_add_u32 s78, s78, 0x6c00\n"
      "buffer_load_dwordx4 v11, s[36:39], s78 offen offset:1024 lds\n"
      "s_add_u32 s78, s78, 0x6c00\n"
      "buffer_load_dwordx4 v11, s[36:39], s78 offen offset:2048 lds\n"
      "s_add_u32 s78, s78, 0x6c00\n"
      "buffer_load_dwordx4 v11, s[36:39], s78 offen offset:3072 lds\n"
      ".Lw1_early:\n"
      "s_lshl2_add_u32 s80, s53, s52\n"
      "s_lshl4_add_u32 s86, s97, s53\n"
      "s_lshl2_add_u32 s58, s86, s52\n"
      "s_lshl_b32 s58, s58, 9\n"
      "s_add_u32 s58, s58, 0x279000\n"
      "s_mul_i32 s59, s80, 0x8000\n"
      "s_lshl_b32 s81, s97, 11\n"
      "s_add_u32 s59, s59, s81\n"
      "s_add_u32 s59, s59, 0x2f9000\n"
      "v_lshlrev_b32 v208, 3, v0\n"
      "v_mov_b32 v212, -1\n"
      "v_mov_b32 v213, -1\n"
      "v_mov_b32 v214, -1\n"
      "v_mov_b32 v215, -1\n"
      "buffer_store_dwordx2 v[212:213], v208, s[20:23], s58 offen sc1\n"
      "buffer_store_dwordx4 v[212:215], v1, s[20:23], s59 offen sc1\n"
      "buffer_store_dwordx4 v[212:215], v1, s[20:23], s59 offen offset:1024 sc1\n"
      "s_mul_i32 s70, s80, 0x7000\n"
      "s_add_u32 s70, s70, 0x539000\n"
      "s_lshl_b32 s71, s80, 12\n"
      "s_add_u32 s71, s71, 0x6f9000\n"
      "s_mul_i32 s72, s97, 0x15000\n"
      "s_add_u32 s72, s72, 0x111000\n"
      "s_lshl_b32 s67, s8, 12\n"
      "s_add_u32 s67, s67, 0x4f9000\n"
      "s_mul_i32 s60, s12, 7\n"
      "s_add_u32 s60, s60, s57\n"
      "s_lshl_b32 s60, s60, 8\n"
      "s_add_u32 s60, s60, s67\n"
      "s_lshl_b32 s83, s57, 18\n"
      "s_add_u32 s83, s83, 0x739000\n"
      "s_lshl3_add_u32 s61, s8, s12\n"
      "s_lshl_b32 s61, s61, 8\n"
      "s_add_u32 s61, s61, s83\n"
      "s_lshl3_add_u32 s68, s5, s52\n"
      "s_lshl_b32 s68, s68, 15\n"
      "s_add_u32 s68, s68, 0x739000\n"
      "s_mul_i32 s83, s5, 0xe0\n"
      "s_lshl2_add_u32 s62, s98, s52\n"
      "s_add_u32 s62, s62, s83\n"
      "s_lshl_b32 s62, s62, 10\n"
      "s_add_u32 s62, s62, 0x8f9000\n"
      "s_lshl3_add_u32 s69, s57, s52\n"
      "s_mul_i32 s69, s69, 0x7000\n"
      "s_add_u32 s69, s69, 0x8f9000\n"
      "s_mul_i32 s63, s8, 28\n"
      "s_lshl2_add_u32 s63, s57, s63\n"
      "s_add_u32 s63, s63, s12\n"
      "s_lshl_b32 s63, s63, 10\n"
      "s_add_u32 s63, s63, 0x539000\n"
      "s_lshl_b32 s65, s97, 6\n"
      "s_add_u32 s65, s65, s52\n"
      "s_lshl_b32 s65, s65, 9\n"
      "s_add_u32 s65, s65, 0x279000\n"
      "s_lshl1_add_u32 s66, s6, s12\n"
      "s_lshl_b32 s66, s66, 13\n"
      "s_xor_b32 s17, s66, 0x4000\n"
      "s_mul_i32 s81, s8, 0x8000\n"
      "s_add_u32 s81, s81, 0x2f9000\n"
      "s_add_u32 s66, s66, s81\n"
      "s_add_u32 s17, s17, s81\n"
      "s_mul_i32 s73, s52, 0x6200\n"
      "s_lshl_b32 s81, s57, 7\n"
      "s_add_u32 s73, s73, s81\n"
      "s_mul_i32 s75, s52, 0x1c0000\n"
      "s_lshl_b32 s81, s56, 7\n"
      "s_add_u32 s75, s75, s81\n"
      "v_and_b32 v202, 15, v0\n"
      "s_lshl_b32 s81, s12, 8\n"
      "v_xor_b32 v28, s81, v1\n"
      "s_mov_b32 s26, 0x3f803f80\n"
      "s_mov_b32 s27, 0x3f803f80\n"
      "s_mul_i32 s87, s52, 0x3800\n"
      "s_add_u32 s87, s87, s72\n"
      "s_mul_i32 s25, s97, 3\n"
      "s_add_u32 s25, s25, s52\n"
      "s_lshl_b32 s25, s25, 11\n"
      "s_add_u32 s25, s25, 0x261000\n"
      "s_lshl_b32 s24, s53, 7\n"
      "s_add_u32 s24, s24, s25\n"
      "s_mul_i32 s81, s53, 28\n"
      "v_lshrrev_b32 v138, 2, v0\n"
      "v_add_u32 v138, s81, v138\n"
      "v_lshrrev_b32 v136, 6, v138\n"
      "v_lshlrev_b32 v136, 11, v136\n"
      "v_and_b32 v139, 31, v138\n"
      "v_lshl_add_u32 v136, v139, 6, v136\n"
      "v_bfe_u32 v138, v138, 5, 1\n"
      "v_xor_b32 v138, 1, v138\n"
      "v_mul_u32_u24 v138, 0xa800, v138\n"
      "v_add_u32 v136, v136, v138\n"
      "v_and_b32 v138, 48, v1\n"
      "v_add_u32 v136, v136, v138\n"
      "s_add_u32 s81, s81, 14\n"
      "v_lshrrev_b32 v138, 2, v0\n"
      "v_add_u32 v138, s81, v138\n"
      "v_lshrrev_b32 v137, 6, v138\n"
      "v_lshlrev_b32 v137, 11, v137\n"
      "v_and_b32 v139, 31, v138\n"
      "v_lshl_add_u32 v137, v139, 6, v137\n"
      "v_bfe_u32 v138, v138, 5, 1\n"
      "v_xor_b32 v138, 1, v138\n"
      "v_mul_u32_u24 v138, 0xa800, v138\n"
      "v_add_u32 v137, v137, v138\n"
      "v_and_b32 v138, 48, v1\n"
      "v_add_u32 v137, v137, v138\n"
      "s_add_u32 s81, s81, 14\n"
      "s_lshl_b32 s81, s86, 7\n"
      "s_add_u32 s81, s81, 0x1000\n"
      "v_mov_b32 v210, s81\n"
      "s_lshl_b32 s81, s13, 7\n"
      "s_add_u32 s81, s81, 0x9000\n"
      "v_writelane_b32 v210, s81, 1\n"
      "s_lshl2_add_u32 s81, s18, 0xc00\n"
      "v_writelane_b32 v210, s81, 2\n"
      "v_and_b32 v216, 7, v0\n"
      "v_min_u32 v216, 6, v216\n"
      "v_and_or_b32 v216, v0, 8, v216\n"
      "s_lshl_b32 s81, s53, 11\n"
      "s_add_u32 s81, s81, 0x9000\n"
      "v_lshl_add_u32 v216, v216, 7, s81\n"
      "s_lshl_b32 s81, s97, 11\n"
      "s_add_u32 s81, s81, 0x1000\n"
      "v_lshl_add_u32 v211, v202, 7, s81\n"
      "v_cmp_eq_u32 vcc, 1, v204\n"
      "s_nop 1\n"
      "v_cndmask_b32 v211, v211, v216, vcc\n"
      "s_min_u32 s82, s57, 6\n"
      "s_xor_b32 s4, s64, 2\n"
      "s_add_u32 s4, s4, s99\n"
      "s_and_b32 s4, s4, 7\n"
      "s_lshl_b32 s4, s4, 12\n"
      "s_xor_b32 s5, s64, 3\n"
      "s_add_u32 s5, s5, s99\n"
      "s_and_b32 s5, s5, 7\n"
      "s_lshl_b32 s5, s5, 12\n"
      "s_xor_b32 s6, s64, 4\n"
      "s_add_u32 s6, s6, s99\n"
      "s_and_b32 s6, s6, 7\n"
      "s_lshl_b32 s6, s6, 12\n"
      "s_xor_b32 s7, s64, 5\n"
      "s_add_u32 s7, s7, s99\n"
      "s_and_b32 s7, s7, 7\n"
      "s_lshl_b32 s7, s7, 12\n"
      "s_xor_b32 s8, s64, 6\n"
      "s_add_u32 s8, s8, s99\n"
      "s_and_b32 s8, s8, 7\n"
      "s_lshl_b32 s8, s8, 12\n"
      "s_xor_b32 s9, s64, 7\n"
      "s_add_u32 s9, s9, s99\n"
      "s_and_b32 s9, s9, 7\n"
      "s_lshl_b32 s9, s9, 12\n"
      "s_xor_b32 s83, s79, 0\n"
      "s_add_u32 s83, s83, s82\n"
      "s_sub_u32 s84, s83, 7\n"
      "s_min_u32 s83, s83, s84\n"
      "s_lshl_b32 s10, s83, 12\n"
      "s_xor_b32 s83, s79, 1\n"
      "s_add_u32 s83, s83, s82\n"
      "s_sub_u32 s84, s83, 7\n"
      "s_min_u32 s83, s83, s84\n"
      "s_lshl_b32 s11, s83, 12\n"
      "s_xor_b32 s83, s79, 2\n"
      "s_add_u32 s83, s83, s82\n"
      "s_sub_u32 s84, s83, 7\n"
      "s_min_u32 s83, s83, s84\n"
      "s_lshl_b32 s12, s83, 12\n"
      "s_xor_b32 s83, s79, 3\n"
      "s_add_u32 s83, s83, s82\n"
      "s_sub_u32 s84, s83, 7\n"
      "s_min_u32 s83, s83, s84\n"
      "s_lshl_b32 s13, s83, 12\n"
      "s_xor_b32 s83, s79, 4\n"
      "s_add_u32 s83, s83, s82\n"
      "s_sub_u32 s84, s83, 7\n"
      "s_min_u32 s83, s83, s84\n"
      "s_lshl_b32 s19, s83, 12\n"
      "s_xor_b32 s83, s79, 5\n"
      "s_add_u32 s83, s83, s82\n"
      "s_sub_u32 s84, s83, 7\n"
      "s_min_u32 s83, s83, s84\n"
      "s_lshl_b32 s99, s83, 12\n"
      "s_mov_b32 s83, 6\n"
      "s_add_u32 s83, s83, s82\n"
      "s_sub_u32 s84, s83, 7\n"
      "s_min_u32 s83, s83, s84\n"
      "s_lshl_b32 s16, s83, 12\n"
      "s_mov_b32 s53, 0\n"
      "s_mov_b32 s57, s55\n"
      "s_mov_b32 s88, 0x3fb8aa3b\n"
      "s_mov_b32 s89, 0xb5c1a2a0\n"
      "s_mov_b32 s90, 0x358637bd\n"
      "s_mov_b32 s91, 0x358637bd\n"
      "s_mov_b32 s92, 0xc0135761\n"
      "s_mov_b32 s93, 0xbdd2d3e8\n"
      "v_lshl_add_u32 v27, v202, 1, v204\n"
      "v_lshlrev_b32 v27, 3, v27\n"
      "v_lshrrev_b32 v216, 4, v0\n"
      "v_lshlrev_b32 v217, 4, v202\n"
      "v_lshl_add_u32 v29, v216, 9, v217\n"
      "v_lshrrev_b32 v216, 2, v202\n"
      "v_lshl_add_u32 v218, v205, 2, v216\n"
      "v_bfe_u32 v216, v0, 3, 1\n"
      "v_mul_u32_u24 v216, 12, v216\n"
      "v_and_b32 v217, 3, v0\n"
      "v_lshl_add_u32 v217, v204, 2, v217\n"
      "v_xor_b32 v216, v216, v217\n"
      "v_lshlrev_b32 v216, 3, v216\n"
      "v_lshl_add_u32 v216, v218, 7, v216\n"
      "s_lshr_b32 s81, s52, 1\n"
      "s_lshl_b32 s81, s81, 6\n"
      "s_add_u32 s81, s81, s76\n"
      "v_xor_b32 v2, s81, v216\n"
      "v_xor_b32 v3, 64, v2\n"
      "v_add_u32 v4, 0x10000, v2\n"
      "v_add_u32 v5, 0x10000, v3\n"
      "s_and_b32 s81, s97, 1\n"
      "v_add_u32 v217, s81, v217\n"
      "v_and_b32 v217, 7, v217\n"
      "v_lshlrev_b32 v217, 3, v217\n"
      "v_lshl_add_u32 v30, v218, 6, v217\n"
      "s_cmp_eq_u32 s54, 0\n"
      "s_cbranch_scc1 .Lnox_1\n"
      "s_waitcnt vmcnt(10)\n"
      "v_permlane32_swap_b32 v18, v20\n"
      "v_permlane32_swap_b32 v19, v21\n"
      "s_waitcnt vmcnt(9)\n"
      "v_permlane32_swap_b32 v22, v24\n"
      "v_permlane32_swap_b32 v23, v25\n"
      "s_mov_b32 exec_hi, 0\n"
      "buffer_store_dwordx2 v[212:213], v27, s[20:23], s60 offen sc1\n"
      "s_mov_b32 exec_hi, -1\n"
      "v_min_u32 v192, -2, v18\n"
      "v_min_u32 v193, -2, v19\n"
      "v_min_u32 v194, -2, v20\n"
      "v_min_u32 v195, -2, v21\n"
      "buffer_store_dwordx4 v[192:195], v1, s[20:23], s63 offen sc1\n"
      "v_min_u32 v196, -2, v22\n"
      "v_min_u32 v197, -2, v23\n"
      "v_min_u32 v198, -2, v24\n"
      "v_min_u32 v199, -2, v25\n"
      "buffer_store_dwordx4 v[196:199], v1, s[20:23], s63 offen offset:2048 sc1\n"
      "v_mov_b32 v160, 0\n"
      "v_mov_b32 v161, 0\n"
      "v_dot2c_f32_bf16 v160, 0x3f803f80, v18\n"
      "v_dot2c_f32_bf16 v161, v18, v18\n"
      "v_dot2c_f32_bf16 v160, 0x3f803f80, v19\n"
      "v_dot2c_f32_bf16 v161, v19, v19\n"
      "v_dot2c_f32_bf16 v160, 0x3f803f80, v20\n"
      "v_dot2c_f32_bf16 v161, v20, v20\n"
      "v_dot2c_f32_bf16 v160, 0x3f803f80, v21\n"
      "v_dot2c_f32_bf16 v161, v21, v21\n"
      "v_dot2c_f32_bf16 v160, 0x3f803f80, v22\n"
      "v_dot2c_f32_bf16 v161, v22, v22\n"
      "v_dot2c_f32_bf16 v160, 0x3f803f80, v23\n"
      "v_dot2c_f32_bf16 v161, v23, v23\n"
      "v_dot2c_f32_bf16 v160, 0x3f803f80, v24\n"
      "v_dot2c_f32_bf16 v161, v24, v24\n"
      "v_dot2c_f32_bf16 v160, 0x3f803f80, v25\n"
      "v_dot2c_f32_bf16 v161, v25, v25\n"
      "s_add_u32 s81, s60, 0x200000\n"
      "s_nop 0\n"
      "v_mov_b32 v162, v160\n"
      "v_mov_b32 v163, v161\n"
      "s_nop 0\n"
      "v_permlane32_swap_b32 v160, v162\n"
      "v_permlane32_swap_b32 v161, v163\n"
      "v_pk_add_f32 v[160:161], v[160:161], v[162:163]\n"
      "v_min_u32 v160, -2, v160\n"
      "v_min_u32 v161, -2, v161\n"
      "s_mov_b32 exec_hi, 0\n"
      "buffer_store_dwordx2 v[160:161], v27, s[20:23], s81 offen sc1\n"
      "s_mov_b32 exec_hi, -1\n"
      ".Lnox_1:\n"
      "s_cmp_eq_u32 s52, 3\n"
      "s_cbranch_scc1 .Lnow_2\n"
      "s_mov_b32 exec_hi, 0xffffff\n"
      "s_waitcnt vmcnt(8)\n"
      "buffer_store_dwordx4 v[128:131], v136, s[20:23], s87 offen sc1\n"
      "s_waitcnt vmcnt(8)\n"
      "buffer_store_dwordx4 v[132:135], v137, s[20:23], s87 offen sc1\n"
      "s_mov_b32 exec_hi, -1\n"
      "s_add_u32 s81, s76, 0x20000\n"
      "v_add_u32 v138, s81, v1\n"
      "v_add_u32 v139, s81, v30\n"
      "ds_write_b128 v138, v[128:131]\n"
      "ds_write_b128 v138, v[132:135] offset:1024\n"
      "ds_read_b64_tr_b16 v[164:165], v139 offset:0\n"
      "v_mov_b32 v172, 0x3f803f80\n"
      "ds_read_b64_tr_b16 v[166:167], v139 offset:512\n"
      "v_mov_b32 v173, 0x3f803f80\n"
      "ds_read_b64_tr_b16 v[168:169], v139 offset:1024\n"
      "v_mov_b32 v174, 0x3f803f80\n"
      "ds_read_b64_tr_b16 v[170:171], v139 offset:1536\n"
      "v_mov_b32 v175, 0x3f803f80\n"
      "s_waitcnt lgkmcnt(2)\n"
      "s_nop 0\n"
      "v_mfma_f32_32x32x16_bf16 v[176:191], v[172:175], v[164:167], 0\n"
      "s_waitcnt lgkmcnt(0)\n"
      "v_mfma_f32_32x32x16_bf16 v[176:191], v[172:175], v[168:171], v[176:191]\n"
      "v_lshlrev_b32 v138, 2, v0\n"
      "s_mov_b32 exec_hi, 0\n"
      "s_nop 9\n"
      "buffer_store_dword v176, v138, s[20:23], s24 offen sc1\n"
      "s_mov_b32 exec_hi, -1\n"
      ".Lnow_2:\n"
      "s_waitcnt vmcnt(6)\n"
      "v_add_u32 v209, 1, v209\n"
      "s_nop 0\n"
      "v_readfirstlane_b32 s56, v209\n"
      "s_waitcnt vmcnt(4)\n"
      "v_lshlrev_b32 v144, 16, v140\n"
      "v_and_b32 v145, 0xffff0000, v140\n"
      "v_lshlrev_b32 v146, 16, v141\n"
      "v_and_b32 v147, 0xffff0000, v141\n"
      "s_waitcnt vmcnt(3)\n"
      "v_lshlrev_b32 v148, 16, v142\n"
      "v_and_b32 v149, 0xffff0000, v142\n"
      "v_lshlrev_b32 v150, 16, v143\n"
      "v_and_b32 v151, 0xffff0000, v143\n"
      "s_lshl_b32 s81, s52, 6\n"
      "s_add_u32 s81, s81, 0x27280\n"
      "v_and_b32 v152, 32, v0\n"
      "v_add_u32 v152, s81, v152\n"
      "ds_write_b128 v152, v[144:147]\n"
      "ds_write_b128 v152, v[148:151] offset:16\n"
      "s_xor_b32 s81, s52, 1\n"
      "s_mul_i32 s81, s81, 0x1800\n"
      "s_add_u32 s81, s81, 0x20000\n"
      "v_add_u32 v7, s81, v1\n"
      "s_xor_b32 s81, s52, 2\n"
      "s_mul_i32 s81, s81, 0x1800\n"
      "s_add_u32 s81, s81, 0x20800\n"
      "v_add_u32 v8, s81, v1\n"
      "s_xor_b32 s81, s52, 3\n"
      "s_mul_i32 s81, s81, 0x1800\n"
      "s_add_u32 s81, s81, 0x21000\n"
      "v_add_u32 v9, s81, v1\n"
      "s_mul_i32 s81, s52, 0x1800\n"
      "s_add_u32 s81, s81, 0x20000\n"
      "v_add_u32 v10, s81, v1\n"
      "v_lshlrev_b32 v26, 16, v26\n"
      "s_add_u32 s63, s63, 0x808000\n"
      "s_mov_b32 s48, s20\n"
      "s_mov_b32 s49, s21\n"
      "s_add_u32 s50, s72, 0x15000\n"
      "s_add_u32 s85, s72, s76\n"
      "s_and_b32 s81, s52, 1\n"
      "s_lshl_b32 s81, s81, 5\n"
      "v_xor_b32 v12, s81, v2\n"
      "v_xor_b32 v14, s81, v4\n"
      "v_xor_b32 v13, s81, v3\n"
      "v_xor_b32 v15, s81, v5\n"
      "s_mov_b32 s79, 0x61a80\n"
      "s_cmp_eq_u32 s55, 0\n"
      "s_cbranch_scc1 .Lno_w1b\n"
      "s_mul_i32 s78, s3, 56\n"
      "s_add_u32 s78, s78, s74\n"
      "s_add_u32 m0, s76, 0x14000\n"
      "s_nop 0\n"
      "buffer_load_dwordx4 v11, s[36:39], s78 offen lds\n"
      "s_add_u32 s78, s78, 0x6c00\n"
      "buffer_load_dwordx4 v11, s[36:39], s78 offen offset:1024 lds\n"
      "s_add_u32 s78, s78, 0x6c00\n"
      "buffer_load_dwordx4 v11, s[36:39], s78 offen offset:2048 lds\n"
      "s_add_u32 s78, s78, 0x6c00\n"
      "buffer_load_dwordx4 v11, s[36:39], s78 offen offset:3072 lds\n"
      "s_waitcnt vmcnt(4)\n"
      "s_barrier\n"
      "s_cmp_lg_u32 s52, 0\n"
      "s_cbranch_scc1 .Lnof_3\n"
      "s_mov_b64 exec, 7\n"
      "buffer_store_dword v209, v210, s[20:23], 0 offen sc1\n"
      "s_mov_b64 exec, -1\n"
      ".Lnof_3:\n"
      "s_mul_i32 s78, s3, 56\n"
      "s_add_u32 s78, s78, s74\n"
      "s_add_u32 m0, s76, 0x8000\n"
      "s_add_u32 s78, s78, 0x1c000\n"
      "buffer_load_dwordx4 v11, s[36:39], s78 offen lds\n"
      "s_add_u32 s78, s78, 0x6c00\n"
      "buffer_load_dwordx4 v11, s[36:39], s78 offen offset:1024 lds\n"
      "s_add_u32 s78, s78, 0x6c00\n"
      "buffer_load_dwordx4 v11, s[36:39], s78 offen offset:2048 lds\n"
      "s_add_u32 s78, s78, 0x6c00\n"
      "buffer_load_dwordx4 v11, s[36:39], s78 offen offset:3072 lds\n"
      "s_mul_i32 s78, s4, 56\n"
      "s_add_u32 s78, s78, s74\n"
      "s_add_u32 m0, s76, 0x4000\n"
      "s_nop 0\n"
      "buffer_load_dwordx4 v11, s[36:39], s78 offen lds\n"
      "s_add_u32 s78, s78, 0x6c00\n"
      "buffer_load_dwordx4 v11, s[36:39], s78 offen offset:1024 lds\n"
      "s_add_u32 s78, s78, 0x6c00\n"
      "buffer_load_dwordx4 v11, s[36:39], s78 offen offset:2048 lds\n"
      "s_add_u32 s78, s78, 0x6c00\n"
      "buffer_load_dwordx4 v11, s[36:39], s78 offen offset:3072 lds\n"
      "s_mul_i32 s78, s4, 56\n"
      "s_add_u32 s78, s78, s74\n"
      "s_mov_b32 m0, s76\n"
      "s_add_u32 s78, s78, 0x1c000\n"
      "buffer_load_dwordx4 v11, s[36:39], s78 offen lds\n"
      "s_add_u32 s78, s78, 0x6c00\n"
      "buffer_load_dwordx4 v11, s[36:39], s78 offen offset:1024 lds\n"
      "s_add_u32 s78, s78, 0x6c00\n"
      "buffer_load_dwordx4 v11, s[36:39], s78 offen offset:2048 lds\n"
      "s_add_u32 s78, s78, 0x6c00\n"
      "buffer_load_dwordx4 v11, s[36:39], s78 offen offset:3072 lds\n"
      "s_mul_i32 s78, s5, 56\n"
      "s_add_u32 s78, s78, s74\n"
      "s_add_u32 m0, s76, 0x18000\n"
      "s_nop 0\n"
      "buffer_load_dwordx4 v11, s[36:39], s78 offen lds\n"
      "s_add_u32 s78, s78, 0x6c00\n"
      "buffer_load_dwordx4 v11, s[36:39], s78 offen offset:1024 lds\n"
      "s_add_u32 s78, s78, 0x6c00\n"
      "buffer_load_dwordx4 v11, s[36:39], s78 offen offset:2048 lds\n"
      "s_add_u32 s78, s78, 0x6c00\n"
      "buffer_load_dwordx4 v11, s[36:39], s78 offen offset:3072 lds\n"
      "s_mul_i32 s78, s5, 56\n"
      "s_add_u32 s78, s78, s74\n"
      "s_add_u32 m0, s76, 0x1c000\n"
      "s_add_u32 s78, s78, 0x1c000\n"
      "buffer_load_dwordx4 v11, s[36:39], s78 offen lds\n"
      "s_add_u32 s78, s78, 0x6c00\n"
      "buffer_load_dwordx4 v11, s[36:39], s78 offen offset:1024 lds\n"
      "s_add_u32 s78, s78, 0x6c00\n"
      "buffer_load_dwordx4 v11, s[36:39], s78 offen offset:2048 lds\n"
      "s_add_u32 s78, s78, 0x6c00\n"
      "buffer_load_dwordx4 v11, s[36:39], s78 offen offset:3072 lds\n"
      "ds_read_b64_tr_b16 a[0:1], v12 offset:49152\n"
      "ds_read_b64_tr_b16 a[2:3], v12 offset:50176\n"
      "ds_read_b64_tr_b16 a[4:5], v13 offset:49152\n"
      "ds_read_b64_tr_b16 a[6:7], v13 offset:50176\n"
      "ds_read_b64_tr_b16 a[8:9], v12 offset:51200\n"
      "ds_read_b64_tr_b16 a[10:11], v12 offset:52224\n"
      "ds_read_b64_tr_b16 a[12:13], v13 offset:51200\n"
      "ds_read_b64_tr_b16 a[14:15], v13 offset:52224\n"
      "s_waitcnt lgkmcnt(0)\n"
      "s_mul_i32 s78, s6, 56\n"
      "s_add_u32 s78, s78, s74\n"
      "s_add_u32 m0, s76, 0xc000\n"
      "s_nop 0\n"
      "buffer_load_dwordx4 v11, s[36:39], s78 offen lds\n"
      "s_add_u32 s78, s78, 0x6c00\n"
      "buffer_load_dwordx4 v11, s[36:39], s78 offen offset:1024 lds\n"
      "s_add_u32 s78, s78, 0x6c00\n"
      "buffer_load_dwordx4 v11, s[36:39], s78 offen offset:2048 lds\n"
      "s_add_u32 s78, s78, 0x6c00\n"
      "buffer_load_dwordx4 v11, s[36:39], s78 offen offset:3072 lds\n"
      "ds_read_b64_tr_b16 a[16:17], v14 offset:0\n"
      "ds_read_b64_tr_b16 a[18:19], v14 offset:1024\n"
      "ds_read_b64_tr_b16 a[20:21], v15 offset:0\n"
      "ds_read_b64_tr_b16 a[22:23], v15 offset:1024\n"
      "ds_read_b64_tr_b16 a[24:25], v14 offset:2048\n"
      "ds_read_b64_tr_b16 a[26:27], v14 offset:3072\n"
      "ds_read_b64_tr_b16 a[28:29], v15 offset:2048\n"
      "ds_read_b64_tr_b16 a[30:31], v15 offset:3072\n"
      "s_waitcnt lgkmcnt(0)\n"
      "s_mul_i32 s78, s6, 56\n"
      "s_add_u32 s78, s78, s74\n"
      "s_add_u32 m0, s76, 0x10000\n"
      "s_add_u32 s78, s78, 0x1c000\n"
      "buffer_load_dwordx4 v11, s[36:39], s78 offen lds\n"
      "s_add_u32 s78, s78, 0x6c00\n"
      "buffer_load_dwordx4 v11, s[36:39], s78 offen offset:1024 lds\n"
      "s_add_u32 s78, s78, 0x6c00\n"
      "buffer_load_dwordx4 v11, s[36:39], s78 offen offset:2048 lds\n"
      "s_add_u32 s78, s78, 0x6c00\n"
      "buffer_load_dwordx4 v11, s[36:39], s78 offen offset:3072 lds\n"
      "s_waitcnt vmcnt(28)\n"
      "ds_read_b64_tr_b16 a[32:33], v14 offset:16384\n"
      "ds_read_b64_tr_b16 a[34:35], v14 offset:17408\n"
      "ds_read_b64_tr_b16 a[36:37], v15 offset:16384\n"
      "ds_read_b64_tr_b16 a[38:39], v15 offset:17408\n"
      "ds_read_b64_tr_b16 a[40:41], v14 offset:18432\n"
      "ds_read_b64_tr_b16 a[42:43], v14 offset:19456\n"
      "ds_read_b64_tr_b16 a[44:45], v15 offset:18432\n"
      "ds_read_b64_tr_b16 a[46:47], v15 offset:19456\n"
      "s_waitcnt lgkmcnt(0)\n"
      "s_mul_i32 s78, s7, 56\n"
      "s_add_u32 s78, s78, s74\n"
      "s_add_u32 m0, s76, 0x14000\n"
      "s_nop 0\n"
      "buffer_load_dwordx4 v11, s[36:39], s78 offen lds\n"
      "s_add_u32 s78, s78, 0x6c00\n"
      "buffer_load_dwordx4 v11, s[36:39], s78 offen offset:1024 lds\n"
      "s_add_u32 s78, s78, 0x6c00\n"
      "buffer_load_dwordx4 v11, s[36:39], s78 offen offset:2048 lds\n"
      "s_add_u32 s78, s78, 0x6c00\n"
      "buffer_load_dwordx4 v11, s[36:39], s78 offen offset:3072 lds\n"
      "s_waitcnt vmcnt(28)\n"
      "ds_read_b64_tr_b16 a[48:49], v12 offset:32768\n"
      "ds_read_b64_tr_b16 a[50:51], v12 offset:33792\n"
      "ds_read_b64_tr_b16 a[52:53], v13 offset:32768\n"
      "ds_read_b64_tr_b16 a[54:55], v13 offset:33792\n"
      "ds_read_b64_tr_b16 a[56:57], v12 offset:34816\n"
      "ds_read_b64_tr_b16 a[58:59], v12 offset:35840\n"
      "ds_read_b64_tr_b16 a[60:61], v13 offset:34816\n"
      "ds_read_b64_tr_b16 a[62:63], v13 offset:35840\n"
      "s_waitcnt lgkmcnt(0)\n"
      "s_mul_i32 s78, s7, 56\n"
      "s_add_u32 s78, s78, s74\n"
      "s_add_u32 m0, s76, 0x8000\n"
      "s_add_u32 s78, s78, 0x1c000\n"
      "buffer_load_dwordx4 v11, s[36:39], s78 offen lds\n"
      "s_add_u32 s78, s78, 0x6c00\n"
      "buffer_load_dwordx4 v11, s[36:39], s78 offen offset:1024 lds\n"
      "s_add_u32 s78, s78, 0x6c00\n"
      "buffer_load_dwordx4 v11, s[36:39], s78 offen offset:2048 lds\n"
      "s_add_u32 s78, s78, 0x6c00\n"
      "buffer_load_dwordx4 v11, s[36:39], s78 offen offset:3072 lds\n"
      "s_waitcnt vmcnt(28)\n"
      "ds_read_b64_tr_b16 a[64:65], v12 offset:16384\n"
      "ds_read_b64_tr_b16 a[66:67], v12 offset:17408\n"
      "ds_read_b64_tr_b16 a[68:69], v13 offset:16384\n"
      "ds_read_b64_tr_b16 a[70:71], v13 offset:17408\n"
      "ds_read_b64_tr_b16 a[72:73], v12 offset:18432\n"
      "ds_read_b64_tr_b16 a[74:75], v12 offset:19456\n"
      "ds_read_b64_tr_b16 a[76:77], v13 offset:18432\n"
      "ds_read_b64_tr_b16 a[78:79], v13 offset:19456\n"
      "s_waitcnt lgkmcnt(0)\n"
      "s_mul_i32 s78, s8, 56\n"
      "s_add_u32 s78, s78, s74\n"
      "s_add_u32 m0, s76, 0x4000\n"
      "s_nop 0\n"
      "buffer_load_dwordx4 v11, s[36:39], s78 offen lds\n"
      "s_add_u32 s78, s78, 0x6c00\n"
      "buffer_load_dwordx4 v11, s[36:39], s78 offen offset:1024 lds\n"
      "s_add_u32 s78, s78, 0x6c00\n"
      "buffer_load_dwordx4 v11, s[36:39], s78 offen offset:2048 lds\n"
      "s_add_u32 s78, s78, 0x6c00\n"
      "buffer_load_dwordx4 v11, s[36:39], s78 offen offset:3072 lds\n"
      "buffer_load_dword v216, v211, s[20:23], 0 offen sc1\n"
      "s_waitcnt vmcnt(29)\n"
      "ds_read_b64_tr_b16 a[80:81], v12 offset:0\n"
      "ds_read_b64_tr_b16 a[82:83], v12 offset:1024\n"
      "ds_read_b64_tr_b16 a[84:85], v13 offset:0\n"
      "ds_read_b64_tr_b16 a[86:87], v13 offset:1024\n"
      "ds_read_b64_tr_b16 a[88:89], v12 offset:2048\n"
      "ds_read_b64_tr_b16 a[90:91], v12 offset:3072\n"
      "ds_read_b64_tr_b16 a[92:93], v13 offset:2048\n"
      "ds_read_b64_tr_b16 a[94:95], v13 offset:3072\n"
      "s_waitcnt lgkmcnt(0)\n"
      "s_mul_i32 s78, s8, 56\n"
      "s_add_u32 s78, s78, s74\n"
      "s_mov_b32 m0, s76\n"
      "s_add_u32 s78, s78, 0x1c000\n"
      "buffer_load_dwordx4 v11, s[36:39], s78 offen lds\n"
      "s_add_u32 s78, s78, 0x6c00\n"
      "buffer_load_dwordx4 v11, s[36:39], s78 offen offset:1024 lds\n"
      "s_add_u32 s78, s78, 0x6c00\n"
      "buffer_load_dwordx4 v11, s[36:39], s78 offen offset:2048 lds\n"
      "s_add_u32 s78, s78, 0x6c00\n"
      "buffer_load_dwordx4 v11, s[36:39], s78 offen offset:3072 lds\n"
      "s_waitcnt vmcnt(29)\n"
      "ds_read_b64_tr_b16 a[96:97], v14 offset:32768\n"
      "ds_read_b64_tr_b16 a[98:99], v14 offset:33792\n"
      "ds_read_b64_tr_b16 a[100:101], v15 offset:32768\n"
      "ds_read_b64_tr_b16 a[102:103], v15 offset:33792\n"
      "ds_read_b64_tr_b16 a[104:105], v14 offset:34816\n"
      "ds_read_b64_tr_b16 a[106:107], v14 offset:35840\n"
      "ds_read_b64_tr_b16 a[108:109], v15 offset:34816\n"
      "ds_read_b64_tr_b16 a[110:111], v15 offset:35840\n"
      "s_waitcnt lgkmcnt(0)\n"
      "s_mul_i32 s78, s9, 56\n"
      "s_add_u32 s78, s78, s74\n"
      "s_add_u32 m0, s76, 0x18000\n"
      "s_nop 0\n"
      "buffer_load_dwordx4 v11, s[36:39], s78 offen lds\n"
      "s_add_u32 s78, s78, 0x6c00\n"
      "buffer_load_dwordx4 v11, s[36:39], s78 offen offset:1024 lds\n"
      "s_add_u32 s78, s78, 0x6c00\n"
      "buffer_load_dwordx4 v11, s[36:39], s78 offen offset:2048 lds\n"
      "s_add_u32 s78, s78, 0x6c00\n"
      "buffer_load_dwordx4 v11, s[36:39], s78 offen offset:3072 lds\n"
      "s_waitcnt vmcnt(29)\n"
      "ds_read_b64_tr_b16 a[112:113], v14 offset:49152\n"
      "ds_read_b64_tr_b16 a[114:115], v14 offset:50176\n"
      "ds_read_b64_tr_b16 a[116:117], v15 offset:49152\n"
      "ds_read_b64_tr_b16 a[118:119], v15 offset:50176\n"
      "ds_read_b64_tr_b16 a[120:121], v14 offset:51200\n"
      "ds_read_b64_tr_b16 a[122:123], v14 offset:52224\n"
      "ds_read_b64_tr_b16 a[124:125], v15 offset:51200\n"
      "ds_read_b64_tr_b16 a[126:127], v15 offset:52224\n"
      "s_waitcnt lgkmcnt(0)\n"
      "s_mul_i32 s78, s9, 56\n"
      "s_add_u32 s78, s78, s74\n"
      "s_add_u32 m0, s76, 0x1c000\n"
      "s_add_u32 s78, s78, 0x1c000\n"
      "buffer_load_dwordx4 v11, s[36:39], s78 offen lds\n"
      "s_add_u32 s78, s78, 0x6c00\n"
      "buffer_load_dwordx4 v11, s[36:39], s78 offen offset:1024 lds\n"
      "s_add_u32 s78, s78, 0x6c00\n"
      "buffer_load_dwordx4 v11, s[36:39], s78 offen offset:2048 lds\n"
      "s_add_u32 s78, s78, 0x6c00\n"
      "buffer_load_dwordx4 v11, s[36:39], s78 offen offset:3072 lds\n"
      "s_waitcnt vmcnt(29)\n"
      "ds_read_b64_tr_b16 a[128:129], v12 offset:49152\n"
      "ds_read_b64_tr_b16 a[130:131], v12 offset:50176\n"
      "ds_read_b64_tr_b16 a[132:133], v13 offset:49152\n"
      "ds_read_b64_tr_b16 a[134:135], v13 offset:50176\n"
      "ds_read_b64_tr_b16 a[136:137], v12 offset:51200\n"
      "ds_read_b64_tr_b16 a[138:139], v12 offset:52224\n"
      "ds_read_b64_tr_b16 a[140:141], v13 offset:51200\n"
      "ds_read_b64_tr_b16 a[142:143], v13 offset:52224\n"
      "s_waitcnt vmcnt(25)\n"
      "ds_read_b64_tr_b16 a[144:145], v14 offset:0\n"
      "ds_read_b64_tr_b16 a[146:147], v14 offset:1024\n"
      "ds_read_b64_tr_b16 a[148:149], v15 offset:0\n"
      "ds_read_b64_tr_b16 a[150:151], v15 offset:1024\n"
      "ds_read_b64_tr_b16 a[152:153], v14 offset:2048\n"
      "ds_read_b64_tr_b16 a[154:155], v14 offset:3072\n"
      "ds_read_b64_tr_b16 a[156:157], v15 offset:2048\n"
      "ds_read_b64_tr_b16 a[158:159], v15 offset:3072\n"
      "s_waitcnt vmcnt(21)\n"
      "ds_read_b64_tr_b16 a[160:161], v14 offset:16384\n"
      "ds_read_b64_tr_b16 a[162:163], v14 offset:17408\n"
      "ds_read_b64_tr_b16 a[164:165], v15 offset:16384\n"
      "ds_read_b64_tr_b16 a[166:167], v15 offset:17408\n"
      "ds_read_b64_tr_b16 a[168:169], v14 offset:18432\n"
      "ds_read_b64_tr_b16 a[170:171], v14 offset:19456\n"
      "ds_read_b64_tr_b16 a[172:173], v15 offset:18432\n"
      "ds_read_b64_tr_b16 a[174:175], v15 offset:19456\n"
      "s_waitcnt vmcnt(17)\n"
      "ds_read_b64_tr_b16 a[176:177], v12 offset:32768\n"
      "ds_read_b64_tr_b16 a[178:179], v12 offset:33792\n"
      "ds_read_b64_tr_b16 a[180:181], v13 offset:32768\n"
      "ds_read_b64_tr_b16 a[182:183], v13 offset:33792\n"
      "ds_read_b64_tr_b16 a[184:185], v12 offset:34816\n"
      "ds_read_b64_tr_b16 a[186:187], v12 offset:35840\n"
      "ds_read_b64_tr_b16 a[188:189], v13 offset:34816\n"
      "ds_read_b64_tr_b16 a[190:191], v13 offset:35840\n"
      "s_waitcnt vmcnt(13)\n"
      "ds_read_b64_tr_b16 a[192:193], v12 offset:16384\n"
      "ds_read_b64_tr_b16 a[194:195], v12 offset:17408\n"
      "ds_read_b64_tr_b16 a[196:197], v13 offset:16384\n"
      "ds_read_b64_tr_b16 a[198:199], v13 offset:17408\n"
      "ds_read_b64_tr_b16 a[200:201], v12 offset:18432\n"
      "ds_read_b64_tr_b16 a[202:203], v12 offset:19456\n"
      "ds_read_b64_tr_b16 a[204:205], v13 offset:18432\n"
      "ds_read_b64_tr_b16 a[206:207], v13 offset:19456\n"
      "s_waitcnt vmcnt(12)\n"
      "v_cmp_ne_u32 vcc, s56, v216\n"
      "s_nop 0\n"
      "s_cbranch_vccnz .Lfl_wait\n"
      ".Lfl_ok:\n"
      "v_and_b32 v40, 31, v0\n"
      "v_lshlrev_b32 v40, 2, v40\n"
      "v_lshrrev_b32 v41, 5, v0\n"
      "v_lshl_add_u32 v41, v41, 10, v40\n"
      "buffer_load_dword v44, v41, s[20:23], s25 offen sc1\n"
      "buffer_load_dword v45, v41, s[20:23], s25 offen offset:128 sc1\n"
      "buffer_load_dword v46, v41, s[20:23], s25 offen offset:256 sc1\n"
      "buffer_load_dword v47, v41, s[20:23], s25 offen offset:384 sc1\n"
      "buffer_load_dword v48, v41, s[20:23], s25 offen offset:512 sc1\n"
      "buffer_load_dword v49, v41, s[20:23], s25 offen offset:640 sc1\n"
      "buffer_load_dword v50, v41, s[20:23], s25 offen offset:768 sc1\n"
      "buffer_load_dword v51, v41, s[20:23], s25 offen offset:896 sc1\n"
      "s_waitcnt lgkmcnt(0)\n"
      "s_add_u32 m0, s76, 0xc000\n"
      "s_add_u32 s78, s85, 0xc000\n"
      "buffer_load_dwordx4 v1, s[48:51], s78 offen sc1 lds\n"
      "buffer_load_dwordx4 v1, s[48:51], s78 offen offset:1024 sc1 lds\n"
      "buffer_load_dwordx4 v1, s[48:51], s78 offen offset:2048 sc1 lds\n"
      "buffer_load_dwordx4 v1, s[48:51], s78 offen offset:3072 sc1 lds\n"
      "s_add_u32 m0, s76, 0x10000\n"
      "s_add_u32 s78, s85, 0x10000\n"
      "buffer_load_dwordx4 v1, s[48:51], s78 offen sc1 lds\n"
      "buffer_load_dwordx4 v1, s[48:51], s78 offen offset:1024 sc1 lds\n"
      "buffer_load_dwordx4 v1, s[48:51], s78 offen offset:2048 sc1 lds\n"
      "buffer_load_dwordx4 v1, s[48:51], s78 offen offset:3072 sc1 lds\n"
      "s_add_u32 m0, s76, 0x14000\n"
      "s_add_u32 s78, s85, 0x14000\n"
      "buffer_load_dwordx4 v1, s[48:51], s78 offen sc1 lds\n"
      "buffer_load_dwordx4 v1, s[48:51], s78 offen offset:1024 sc1 lds\n"
      "buffer_load_dwordx4 v1, s[48:51], s78 offen offset:2048 sc1 lds\n"
      "buffer_load_dwordx4 v1, s[48:51], s78 offen offset:3072 sc1 lds\n"
      "s_add_u32 m0, s76, 0x8000\n"
      "s_add_u32 s78, s85, 0x8000\n"
      "buffer_load_dwordx4 v1, s[48:51], s78 offen sc1 lds\n"
      "buffer_load_dwordx4 v1, s[48:51], s78 offen offset:1024 sc1 lds\n"
      "buffer_load_dwordx4 v1, s[48:51], s78 offen offset:2048 sc1 lds\n"
      "buffer_load_dwordx4 v1, s[48:51], s78 offen offset:3072 sc1 lds\n"
      "s_mov_b32 s80, s70\n"
      "buffer_load_dwordx4 v[160:163], v1, s[20:23], s80 offen sc1\n"
      "buffer_load_dwordx4 v[164:167], v1, s[20:23], s80 offen offset:1024 sc1\n"
      "s_add_u32 s80, s70, 0x1000\n"
      "buffer_load_dwordx4 v[168:171], v1, s[20:23], s80 offen sc1\n"
      "buffer_load_dwordx4 v[172:175], v1, s[20:23], s80 offen offset:1024 sc1\n"
      "s_add_u32 s80, s70, 0x2000\n"
      "buffer_load_dwordx4 v[176:179], v1, s[20:23], s80 offen sc1\n"
      "buffer_load_dwordx4 v[180:183], v1, s[20:23], s80 offen offset:1024 sc1\n"
      "s_add_u32 s80, s70, 0x3000\n"
      "buffer_load_dwordx4 v[184:187], v1, s[20:23], s80 offen sc1\n"
      "s_waitcnt vmcnt(39)\n"
      "ds_read_b64_tr_b16 a[208:209], v12 offset:0\n"
      "ds_read_b64_tr_b16 a[210:211], v12 offset:1024\n"
      "ds_read_b64_tr_b16 a[212:213], v13 offset:0\n"
      "ds_read_b64_tr_b16 a[214:215], v13 offset:1024\n"
      "ds_read_b64_tr_b16 a[216:217], v12 offset:2048\n"
      "ds_read_b64_tr_b16 a[218:219], v12 offset:3072\n"
      "ds_read_b64_tr_b16 a[220:221], v13 offset:2048\n"
      "ds_read_b64_tr_b16 a[222:223], v13 offset:3072\n"
      "s_waitcnt vmcnt(35)\n"
      "ds_read_b64_tr_b16 a[224:225], v14 offset:32768\n"
      "ds_read_b64_tr_b16 a[226:227], v14 offset:33792\n"
      "ds_read_b64_tr_b16 a[228:229], v15 offset:32768\n"
      "ds_read_b64_tr_b16 a[230:231], v15 offset:33792\n"
      "ds_read_b64_tr_b16 a[232:233], v14 offset:34816\n"
      "ds_read_b64_tr_b16 a[234:235], v14 offset:35840\n"
      "ds_read_b64_tr_b16 a[236:237], v15 offset:34816\n"
      "ds_read_b64_tr_b16 a[238:239], v15 offset:35840\n"
      "s_waitcnt vmcnt(31)\n"
      "ds_read_b64_tr_b16 a[240:241], v14 offset:49152\n"
      "ds_read_b64_tr_b16 a[242:243], v14 offset:50176\n"
      "ds_read_b64_tr_b16 a[244:245], v15 offset:49152\n"
      "ds_read_b64_tr_b16 a[246:247], v15 offset:50176\n"
      "ds_read_b64_tr_b16 a[248:249], v14 offset:51200\n"
      "ds_read_b64_tr_b16 a[250:251], v14 offset:52224\n"
      "ds_read_b64_tr_b16 a[252:253], v15 offset:51200\n"
      "ds_read_b64_tr_b16 a[254:255], v15 offset:52224\n"
      "s_waitcnt vmcnt(25)\n"
      "v_pk_add_f32 v[44:45], v[44:45], v[48:49]\n"
      "s_waitcnt vmcnt(23)\n"
      "v_pk_add_f32 v[46:47], v[46:47], v[50:51]\n"
      "v_pk_add_f32 v[44:45], v[44:45], v[46:47]\n"
      "v_add_f32 v44, v44, v45\n"
      "v_mov_b32 v45, v44\n"
      "s_lshl_b32 s81, s52, 7\n"
      "s_add_u32 s81, s81, 0x26000\n"
      "v_permlane32_swap_b32 v44, v45\n"
      "v_add_u32 v41, s81, v40\n"
      "v_add_f32 v44, v44, v45\n"
      "ds_write_b32 v41, v44\n"
      "v_add_u32 v40, 0x26000, v40\n"
      "s_waitcnt vmcnt(0) lgkmcnt(0)\n"
      "s_barrier\n"
      "ds_read_b32 v16, v40 offset:128\n"
      "ds_read_b32 v17, v40 offset:256\n"
      "s_waitcnt lgkmcnt(0)\n"
      ".Llayer:\n"
      "v_add_u32 v232, 0xa800, v30\n"
      "s_mov_b32 s64, 0\n"
      "ds_read_b64_tr_b16 v[208:209], v232 offset:14336\n"
      "ds_read_b64_tr_b16 v[210:211], v232 offset:14848\n"
      "ds_read_b64_tr_b16 v[212:213], v232 offset:28672\n"
      "ds_read_b64_tr_b16 v[214:215], v232 offset:29184\n"
      "ds_read_b64_tr_b16 v[216:217], v232 offset:0\n"
      "ds_read_b64_tr_b16 v[218:219], v232 offset:512\n"
      "ds_read_b64_tr_b16 v[220:221], v232 offset:15360\n"
      "ds_read_b64_tr_b16 v[222:223], v232 offset:15872\n"
      "s_waitcnt vmcnt(8)\n"
      "v_cmp_eq_u32 vcc, -1, v160\n"
      "s_nop 0\n"
      "s_cbranch_vccnz .Lpk_5\n"
      ".Lpr_6:\n"
      "s_waitcnt lgkmcnt(6)\n"
      "v_mfma_f32_32x32x16_bf16 v[128:143], v[160:163], v[208:211], 0\n"
      "ds_read_b64_tr_b16 v[224:225], v232 offset:29696\n"
      "ds_read_b64_tr_b16 v[226:227], v232 offset:30208\n"
      "buffer_load_dwordx4 v[188:191], v1, s[20:23], s80 offen offset:1024 sc1\n"
      "v_lshrrev_b32 v12, 3, v0\n"
      "v_mul_u32_u24 v12, 0x380, v12\n"
      "s_waitcnt vmcnt(8) lgkmcnt(6)\n"
      "v_mfma_f32_32x32x16_bf16 v[144:159], v[160:163], v[212:215], 0\n"
      "v_cmp_eq_u32 vcc, -1, v164\n"
      "ds_read_b64_tr_b16 v[228:229], v232 offset:1024\n"
      "ds_read_b64_tr_b16 v[230:231], v232 offset:1536\n"
      "s_add_u32 m0, s76, 0x4000\n"
      "s_add_u32 s78, s85, 0x4000\n"
      "buffer_load_dwordx4 v1, s[48:51], s78 offen sc1 lds\n"
      "s_waitcnt lgkmcnt(6)\n"
      "v_mfma_f32_32x32x16_bf16 v[192:207], v[216:219], v[160:163], 0\n"
      "s_cbranch_vccnz .Lpk_8\n"
      ".Lpr_9:\n"
      "ds_read_b64_tr_b16 v[208:209], v232 offset:16384\n"
      "ds_read_b64_tr_b16 v[210:211], v232 offset:16896\n"
      "buffer_load_dwordx4 v1, s[48:51], s78 offen offset:1024 sc1 lds\n"
      "v_and_b32 v233, 0x7f, v6\n"
      "s_waitcnt lgkmcnt(6)\n"
      "v_mfma_f32_32x32x16_bf16 v[128:143], v[164:167], v[220:223], v[128:143]\n"
      "ds_read_b64_tr_b16 v[212:213], v232 offset:30720\n"
      "ds_read_b64_tr_b16 v[214:215], v232 offset:31232\n"
      "s_add_u32 s80, s70, 0x4000\n"
      "buffer_load_dwordx4 v[160:163], v1, s[20:23], s80 offen sc1\n"
      "v_add_u32 v233, v233, v12\n"
      "s_waitcnt vmcnt(10) lgkmcnt(6)\n"
      "v_mfma_f32_32x32x16_bf16 v[144:159], v[164:167], v[224:227], v[144:159]\n"
      "v_cmp_eq_u32 vcc, -1, v168\n"
      "ds_read_b64_tr_b16 v[216:217], v232 offset:2048\n"
      "ds_read_b64_tr_b16 v[218:219], v232 offset:2560\n"
      "buffer_load_dwordx4 v1, s[48:51], s78 offen offset:2048 sc1 lds\n"
      "s_lshl_b32 s87, s52, 10\n"
      "s_waitcnt lgkmcnt(6)\n"
      "v_mfma_f32_32x32x16_bf16 v[192:207], v[228:231], v[164:167], v[192:207]\n"
      "s_cbranch_vccnz .Lpk_11\n"
      ".Lpr_12:\n"
      "ds_read_b64_tr_b16 v[220:221], v232 offset:17408\n"
      "ds_read_b64_tr_b16 v[222:223], v232 offset:17920\n"
      "buffer_load_dwordx4 v1, s[48:51], s78 offen offset:3072 sc1 lds\n"
      "v_lshlrev_b32 v238, 3, v0\n"
      "s_waitcnt lgkmcnt(6)\n"
      "v_mfma_f32_32x32x16_bf16 v[128:143], v[168:171], v[208:211], v[128:143]\n"
      "ds_read_b64_tr_b16 v[224:225], v232 offset:31744\n"
      "ds_read_b64_tr_b16 v[226:227], v232 offset:32256\n"
      "buffer_load_dwordx4 v[164:167], v1, s[20:23], s80 offen offset:1024 sc1\n"
      "s_add_u32 s81, s87, 0x20000\n"
      "v_add_u32 v12, s81, v1\n"
      "s_waitcnt vmcnt(12) lgkmcnt(6)\n"
      "v_mfma_f32_32x32x16_bf16 v[144:159], v[168:171], v[212:215], v[144:159]\n"
      "v_cmp_eq_u32 vcc, -1, v172\n"
      "ds_read_b64_tr_b16 v[228:229], v232 offset:3072\n"
      "ds_read_b64_tr_b16 v[230:231], v232 offset:3584\n"
      "s_add_u32 s81, s76, 0x20000\n"
      "v_add_u32 v13, s81, v1\n"
      "s_waitcnt lgkmcnt(6)\n"
      "v_mfma_f32_32x32x16_bf16 v[192:207], v[216:219], v[168:171], v[192:207]\n"
      "s_cbranch_vccnz .Lpk_14\n"
      ".Lpr_15:\n"
      "ds_read_b64_tr_b16 v[208:209], v232 offset:18432\n"
      "ds_read_b64_tr_b16 v[210:211], v232 offset:18944\n"
      "s_mov_b32 m0, s76\n"
      "s_mov_b32 s78, s85\n"
      "buffer_load_dwordx4 v1, s[48:51], s78 offen sc1 lds\n"
      "s_waitcnt lgkmcnt(6)\n"
      "v_mfma_f32_32x32x16_bf16 v[128:143], v[172:175], v[220:223], v[128:143]\n"
      "ds_read_b64_tr_b16 v[212:213], v232 offset:32768\n"
      "ds_read_b64_tr_b16 v[214:215], v232 offset:33280\n"
      "s_add_u32 s80, s70, 0x5000\n"
      "buffer_load_dwordx4 v[168:171], v1, s[20:23], s80 offen sc1\n"
      "s_lshl_b32 s81, s52, 8\n"
      "s_waitcnt vmcnt(13) lgkmcnt(6)\n"
      "v_mfma_f32_32x32x16_bf16 v[144:159], v[172:175], v[224:227], v[144:159]\n"
      "v_cmp_eq_u32 vcc, -1, v176\n"
      "ds_read_b64_tr_b16 v[216:217], v232 offset:4096\n"
      "ds_read_b64_tr_b16 v[218:219], v232 offset:4608\n"
      "s_add_u32 s81, s81, 0x26680\n"
      "v_and_b32 v14, 31, v0\n"
      "s_waitcnt lgkmcnt(6)\n"
      "v_mfma_f32_32x32x16_bf16 v[192:207], v[228:231], v[172:175], v[192:207]\n"
      "s_cbranch_vccnz .Lpk_17\n"
      ".Lpr_18:\n"
      "ds_read_b64_tr_b16 v[220:221], v232 offset:19456\n"
      "ds_read_b64_tr_b16 v[222:223], v232 offset:19968\n"
      "buffer_load_dwordx4 v1, s[48:51], s78 offen offset:1024 sc1 lds\n"
      "v_lshl_add_u32 v14, v14, 2, s81\n"
      "s_waitcnt lgkmcnt(6)\n"
      "v_mfma_f32_32x32x16_bf16 v[128:143], v[176:179], v[208:211], v[128:143]\n"
      "ds_read_b64_tr_b16 v[224:225], v232 offset:33792\n"
      "ds_read_b64_tr_b16 v[226:227], v232 offset:34304\n"
      "buffer_load_dwordx4 v[172:175], v1, s[20:23], s80 offen offset:1024 sc1\n"
      "v_lshrrev_b32 v15, 5, v0\n"
      "v_lshl_add_u32 v15, v15, 4, s81\n"
      "s_waitcnt vmcnt(12) lgkmcnt(6)\n"
      "v_mfma_f32_32x32x16_bf16 v[144:159], v[176:179], v[212:215], v[144:159]\n"
      "v_cmp_eq_u32 vcc, -1, v180\n"
      "ds_read_b64_tr_b16 v[228:229], v232 offset:5120\n"
      "ds_read_b64_tr_b16 v[230:231], v232 offset:5632\n"
      "v_mov_b32 v252, 0\n"
      "v_mov_b32 v253, 0\n"
      "s_waitcnt lgkmcnt(6)\n"
      "v_mfma_f32_32x32x16_bf16 v[192:207], v[216:219], v[176:179], v[192:207]\n"
      "s_cbranch_vccnz .Lpk_20\n"
      ".Lpr_21:\n"
      "ds_read_b64_tr_b16 v[208:209], v232 offset:20480\n"
      "ds_read_b64_tr_b16 v[210:211], v232 offset:20992\n"
      "buffer_load_dwordx4 v1, s[48:51], s78 offen offset:2048 sc1 lds\n"
      "v_mov_b32 v254, 0\n"
      "s_waitcnt lgkmcnt(6)\n"
      "v_mfma_f32_32x32x16_bf16 v[128:143], v[180:183], v[220:223], v[128:143]\n"
      "ds_read_b64_tr_b16 v[212:213], v232 offset:34816\n"
      "ds_read_b64_tr_b16 v[214:215], v232 offset:35328\n"
      "s_add_u32 s80, s70, 0x6000\n"
      "buffer_load_dwordx4 v[176:179], v1, s[20:23], s80 offen sc1\n"
      "v_mov_b32 v255, 0\n"
      "s_waitcnt vmcnt(13) lgkmcnt(6)\n"
      "v_mfma_f32_32x32x16_bf16 v[144:159], v[180:183], v[224:227], v[144:159]\n"
      "v_cmp_eq_u32 vcc, -1, v184\n"
      "ds_read_b64_tr_b16 v[216:217], v232 offset:6144\n"
      "ds_read_b64_tr_b16 v[218:219], v232 offset:6656\n"
      "s_waitcnt lgkmcnt(6)\n"
      "v_mfma_f32_32x32x16_bf16 v[192:207], v[228:231], v[180:183], v[192:207]\n"
      "s_cbranch_vccnz .Lpk_23\n"
      ".Lpr_24:\n"
      "ds_read_b64_tr_b16 v[220:221], v232 offset:21504\n"
      "ds_read_b64_tr_b16 v[222:223], v232 offset:22016\n"
      "buffer_load_dwordx4 v1, s[48:51], s78 offen offset:3072 sc1 lds\n"
      "s_waitcnt lgkmcnt(6)\n"
      "v_mfma_f32_32x32x16_bf16 v[128:143], v[184:187], v[208:211], v[128:143]\n"
      "ds_read_b64_tr_b16 v[224:225], v232 offset:35840\n"
      "ds_read_b64_tr_b16 v[226:227], v232 offset:36352\n"
      "buffer_load_dwordx4 v[180:183], v1, s[20:23], s80 offen offset:1024 sc1\n"
      "s_waitcnt vmcnt(14) lgkmcnt(6)\n"
      "v_mfma_f32_32x32x16_bf16 v[144:159], v[184:187], v[212:215], v[144:159]\n"
      "v_cmp_eq_u32 vcc, -1, v188\n"
      "ds_read_b64_tr_b16 v[228:229], v232 offset:7168\n"
      "ds_read_b64_tr_b16 v[230:231], v232 offset:7680\n"
      "s_waitcnt lgkmcnt(6)\n"
      "v_mfma_f32_32x32x16_bf16 v[192:207], v[216:219], v[184:187], v[192:207]\n"
      "s_cbranch_vccnz .Lpk_26\n"
      ".Lpr_27:\n"
      "ds_read_b64_tr_b16 v[208:209], v232 offset:22528\n"
      "ds_read_b64_tr_b16 v[210:211], v232 offset:23040\n"
      "s_waitcnt lgkmcnt(6)\n"
      "v_mfma_f32_32x32x16_bf16 v[128:143], v[188:191], v[220:223], v[128:143]\n"
      "ds_read_b64_tr_b16 v[212:213], v232 offset:36864\n"
      "ds_read_b64_tr_b16 v[214:215], v232 offset:37376\n"
      "s_mov_b32 s80, s70\n"
      "buffer_load_dwordx4 v[184:187], v1, s[20:23], s80 offen offset:2048 sc1\n"
      "s_waitcnt vmcnt(12) lgkmcnt(6)\n"
      "v_mfma_f32_32x32x16_bf16 v[144:159], v[188:191], v[224:227], v[144:159]\n"
      "v_cmp_eq_u32 vcc, -1, v160\n"
      "ds_read_b64_tr_b16 v[216:217], v232 offset:8192\n"
      "ds_read_b64_tr_b16 v[218:219], v232 offset:8704\n"
      "s_waitcnt lgkmcnt(6)\n"
      "v_mfma_f32_32x32x16_bf16 v[192:207], v[228:231], v[188:191], v[192:207]\n"
      "s_cbranch_vccnz .Lpk_29\n"
      ".Lpr_30:\n"
      "ds_read_b64_tr_b16 v[220:221], v232 offset:23552\n"
      "ds_read_b64_tr_b16 v[222:223], v232 offset:24064\n"
      "s_waitcnt lgkmcnt(6)\n"
      "v_mfma_f32_32x32x16_bf16 v[128:143], v[160:163], v[208:211], v[128:143]\n"
      "ds_read_b64_tr_b16 v[224:225], v232 offset:37888\n"
      "ds_read_b64_tr_b16 v[226:227], v232 offset:38400\n"
      "buffer_load_dwordx4 v[188:191], v1, s[20:23], s80 offen offset:3072 sc1\n"
      "s_waitcnt vmcnt(10) lgkmcnt(6)\n"
      "v_mfma_f32_32x32x16_bf16 v[144:159], v[160:163], v[212:215], v[144:159]\n"
      "v_cmp_eq_u32 vcc, -1, v164\n"
      "ds_read_b64_tr_b16 v[228:229], v232 offset:9216\n"
      "ds_read_b64_tr_b16 v[230:231], v232 offset:9728\n"
      "s_waitcnt lgkmcnt(6)\n"
      "v_mfma_f32_32x32x16_bf16 v[192:207], v[216:219], v[160:163], v[192:207]\n"
      "s_cbranch_vccnz .Lpk_32\n"
      ".Lpr_33:\n"
      "ds_read_b64_tr_b16 v[208:209], v232 offset:24576\n"
      "ds_read_b64_tr_b16 v[210:211], v232 offset:25088\n"
      "buffer_load_dwordx4 v[240:243], v1, s[20:23], s71 offen sc1\n"
      "s_waitcnt lgkmcnt(6)\n"
      "v_mfma_f32_32x32x16_bf16 v[128:143], v[164:167], v[220:223], v[128:143]\n"
      "ds_read_b64_tr_b16 v[212:213], v232 offset:38912\n"
      "ds_read_b64_tr_b16 v[214:215], v232 offset:39424\n"
      "s_add_u32 s80, s70, 0x1000\n"
      "buffer_load_dwordx4 v[160:163], v1, s[20:23], s80 offen offset:2048 sc1\n"
      "s_waitcnt vmcnt(10) lgkmcnt(6)\n"
      "v_mfma_f32_32x32x16_bf16 v[144:159], v[164:167], v[224:227], v[144:159]\n"
      "v_cmp_eq_u32 vcc, -1, v168\n"
      "ds_read_b64_tr_b16 v[216:217], v232 offset:10240\n"
      "ds_read_b64_tr_b16 v[218:219], v232 offset:10752\n"
      "s_waitcnt lgkmcnt(6)\n"
      "v_mfma_f32_32x32x16_bf16 v[192:207], v[228:231], v[164:167], v[192:207]\n"
      "s_cbranch_vccnz .Lpk_35\n"
      ".Lpr_36:\n"
      "ds_read_b64_tr_b16 v[220:221], v232 offset:25600\n"
      "ds_read_b64_tr_b16 v[222:223], v232 offset:26112\n"
      "buffer_load_dwordx4 v[244:247], v1, s[20:23], s71 offen offset:1024 sc1\n"
      "s_waitcnt lgkmcnt(6)\n"
      "v_mfma_f32_32x32x16_bf16 v[128:143], v[168:171], v[208:211], v[128:143]\n"
      "ds_read_b64_tr_b16 v[224:225], v232 offset:39936\n"
      "ds_read_b64_tr_b16 v[226:227], v232 offset:40448\n"
      "buffer_load_dwordx4 v[164:167], v1, s[20:23], s80 offen offset:3072 sc1\n"
      "s_waitcnt vmcnt(10) lgkmcnt(6)\n"
      "v_mfma_f32_32x32x16_bf16 v[144:159], v[168:171], v[212:215], v[144:159]\n"
      "v_cmp_eq_u32 vcc, -1, v172\n"
      "ds_read_b64_tr_b16 v[228:229], v232 offset:11264\n"
      "ds_read_b64_tr_b16 v[230:231], v232 offset:11776\n"
      "s_waitcnt lgkmcnt(6)\n"
      "v_mfma_f32_32x32x16_bf16 v[192:207], v[216:219], v[168:171], v[192:207]\n"
      "s_cbranch_vccnz .Lpk_38\n"
      ".Lpr_39:\n"
      "ds_read_b64_tr_b16 v[208:209], v232 offset:26624\n"
      "ds_read_b64_tr_b16 v[210:211], v232 offset:27136\n"
      "buffer_load_dwordx4 v[248:251], v1, s[20:23], s71 offen offset:2048 sc1\n"
      "s_waitcnt lgkmcnt(6)\n"
      "v_mfma_f32_32x32x16_bf16 v[128:143], v[172:175], v[220:223], v[128:143]\n"
      "ds_read_b64_tr_b16 v[212:213], v232 offset:40960\n"
      "ds_read_b64_tr_b16 v[214:215], v232 offset:41472\n"
      "s_add_u32 s80, s70, 0x2000\n"
      "buffer_load_dwordx4 v[168:171], v1, s[20:23], s80 offen offset:2048 sc1\n"
      "s_waitcnt vmcnt(10) lgkmcnt(6)\n"
      "v_mfma_f32_32x32x16_bf16 v[144:159], v[172:175], v[224:227], v[144:159]\n"
      "v_cmp_eq_u32 vcc, -1, v176\n"
      "ds_read_b64_tr_b16 v[216:217], v232 offset:12288\n"
      "ds_read_b64_tr_b16 v[218:219], v232 offset:12800\n"
      "s_waitcnt lgkmcnt(6)\n"
      "v_mfma_f32_32x32x16_bf16 v[192:207], v[228:231], v[172:175], v[192:207]\n"
      "s_cbranch_vccnz .Lpk_41\n"
      ".Lpr_42:\n"
      "ds_read_b64_tr_b16 v[220:221], v232 offset:27648\n"
      "ds_read_b64_tr_b16 v[222:223], v232 offset:28160\n"
      "s_mov_b32 exec_hi, 0\n"
      "buffer_load_dwordx4 v[252:255], v1, s[20:23], s71 offen offset:3072 sc1\n"
      "s_mov_b32 exec_hi, -1\n"
      "s_waitcnt lgkmcnt(6)\n"
      "v_mfma_f32_32x32x16_bf16 v[128:143], v[176:179], v[208:211], v[128:143]\n"
      "ds_read_b64_tr_b16 v[224:225], v232 offset:41984\n"
      "ds_read_b64_tr_b16 v[226:227], v232 offset:42496\n"
      "buffer_load_dwordx4 v[172:175], v1, s[20:23], s80 offen offset:3072 sc1\n"
      "s_waitcnt vmcnt(10) lgkmcnt(6)\n"
      "v_mfma_f32_32x32x16_bf16 v[144:159], v[176:179], v[212:215], v[144:159]\n"
      "v_cmp_eq_u32 vcc, -1, v180\n"
      "ds_read_b64_tr_b16 v[228:229], v232 offset:13312\n"
      "ds_read_b64_tr_b16 v[230:231], v232 offset:13824\n"
      "s_waitcnt lgkmcnt(6)\n"
      "v_mfma_f32_32x32x16_bf16 v[192:207], v[216:219], v[176:179], v[192:207]\n"
      "s_cbranch_vccnz .Lpk_44\n"
      ".Lpr_45:\n"
      "s_barrier\n"
      "ds_read_b64_tr_b16 v[208:209], v30 offset:14336\n"
      "ds_read_b64_tr_b16 v[210:211], v30 offset:14848\n"
      "s_waitcnt lgkmcnt(6)\n"
      "v_mfma_f32_32x32x16_bf16 v[128:143], v[180:183], v[220:223], v[128:143]\n"
      "ds_read_b64_tr_b16 v[212:213], v30 offset:28672\n"
      "ds_read_b64_tr_b16 v[214:215], v30 offset:29184\n"
      "s_add_u32 s80, s70, 0x3000\n"
      "buffer_load_dwordx4 v[176:179], v1, s[20:23], s80 offen offset:2048 sc1\n"
      "s_waitcnt vmcnt(10) lgkmcnt(6)\n"
      "v_mfma_f32_32x32x16_bf16 v[144:159], v[180:183], v[224:227], v[144:159]\n"
      "v_cmp_eq_u32 vcc, -1, v184\n"
      "ds_read_b64_tr_b16 v[216:217], v30 offset:0\n"
      "ds_read_b64_tr_b16 v[218:219], v30 offset:512\n"
      "s_waitcnt lgkmcnt(6)\n"
      "v_mfma_f32_32x32x16_bf16 v[192:207], v[228:231], v[180:183], v[192:207]\n"
      "s_cbranch_vccnz .Lpk_47\n"
      ".Lpr_48:\n"
      "ds_read_b64_tr_b16 v[220:221], v30 offset:15360\n"
      "ds_read_b64_tr_b16 v[222:223], v30 offset:15872\n"
      "s_waitcnt lgkmcnt(6)\n"
      "v_mfma_f32_32x32x16_bf16 v[128:143], v[184:187], v[208:211], v[128:143]\n"
      "ds_read_b64_tr_b16 v[224:225], v30 offset:29696\n"
      "ds_read_b64_tr_b16 v[226:227], v30 offset:30208\n"
      "buffer_load_dwordx4 v[180:183], v1, s[20:23], s80 offen offset:3072 sc1\n"
      "s_waitcnt vmcnt(7)\n"
      "v_add_f32 v240, v240, v244\n"
      "v_add_f32 v241, v241, v245\n"
      "s_waitcnt lgkmcnt(6)\n"
      "v_mfma_f32_32x32x16_bf16 v[144:159], v[184:187], v[212:215], v[144:159]\n"
      "v_cmp_eq_u32 vcc, -1, v188\n"
      "ds_read_b64_tr_b16 v[228:229], v30 offset:1024\n"
      "ds_read_b64_tr_b16 v[230:231], v30 offset:1536\n"
      "v_add_f32 v242, v242, v246\n"
      "v_add_f32 v243, v243, v247\n"
      "s_waitcnt lgkmcnt(6)\n"
      "v_mfma_f32_32x32x16_bf16 v[192:207], v[216:219], v[184:187], v[192:207]\n"
      "s_cbranch_vccnz .Lpk_50\n"
      ".Lpr_51:\n"
      "ds_read_b64_tr_b16 v[208:209], v30 offset:16384\n"
      "ds_read_b64_tr_b16 v[210:211], v30 offset:16896\n"
      "s_waitcnt vmcnt(3)\n"
      "v_add_f32 v248, v248, v252\n"
      "v_add_f32 v249, v249, v253\n"
      "s_waitcnt lgkmcnt(6)\n"
      "v_mfma_f32_32x32x16_bf16 v[128:143], v[188:191], v[220:223], v[128:143]\n"
      "ds_read_b64_tr_b16 v[212:213], v30 offset:30720\n"
      "ds_read_b64_tr_b16 v[214:215], v30 offset:31232\n"
      "s_add_u32 s80, s70, 0x4000\n"
      "buffer_load_dwordx4 v[184:187], v1, s[20:23], s80 offen offset:2048 sc1\n"
      "v_add_f32 v250, v250, v254\n"
      "s_waitcnt lgkmcnt(6)\n"
      "v_mfma_f32_32x32x16_bf16 v[144:159], v[188:191], v[224:227], v[144:159]\n"
      "v_cmp_eq_u32 vcc, -1, v160\n"
      "ds_read_b64_tr_b16 v[216:217], v30 offset:2048\n"
      "ds_read_b64_tr_b16 v[218:219], v30 offset:2560\n"
      "v_add_f32 v251, v251, v255\n"
      "v_add_f32 v240, v240, v248\n"
      "s_waitcnt lgkmcnt(6)\n"
      "v_mfma_f32_32x32x16_bf16 v[192:207], v[228:231], v[188:191], v[192:207]\n"
      "s_cbranch_vccnz .Lpk_53\n"
      ".Lpr_54:\n"
      "ds_read_b64_tr_b16 v[220:221], v30 offset:17408\n"
      "ds_read_b64_tr_b16 v[222:223], v30 offset:17920\n"
      "v_add_f32 v241, v241, v249\n"
      "v_add_f32 v242, v242, v250\n"
      "s_waitcnt lgkmcnt(6)\n"
      "v_mfma_f32_32x32x16_bf16 v[128:143], v[160:163], v[208:211], v[128:143]\n"
      "ds_read_b64_tr_b16 v[224:225], v30 offset:31744\n"
      "ds_read_b64_tr_b16 v[226:227], v30 offset:32256\n"
      "buffer_load_dwordx4 v[188:191], v1, s[20:23], s80 offen offset:3072 sc1\n"
      "v_add_f32 v243, v243, v251\n"
      "v_permlane16_swap_b32 v240, v242\n"
      "s_waitcnt lgkmcnt(6)\n"
      "v_mfma_f32_32x32x16_bf16 v[144:159], v[160:163], v[212:215], v[144:159]\n"
      "v_cmp_eq_u32 vcc, -1, v164\n"
      "ds_read_b64_tr_b16 v[228:229], v30 offset:3072\n"
      "ds_read_b64_tr_b16 v[230:231], v30 offset:3584\n"
      "v_permlane16_swap_b32 v241, v243\n"
      "v_add_f32 v234, v240, v242\n"
      "s_waitcnt lgkmcnt(6)\n"
      "v_mfma_f32_32x32x16_bf16 v[192:207], v[216:219], v[160:163], v[192:207]\n"
      "s_cbranch_vccnz .Lpk_56\n"
      ".Lpr_57:\n"
      "ds_read_b64_tr_b16 v[208:209], v30 offset:18432\n"
      "ds_read_b64_tr_b16 v[210:211], v30 offset:18944\n"
      "v_add_f32 v235, v241, v243\n"
      "v_mov_b32 v236, v234\n"
      "s_waitcnt lgkmcnt(6)\n"
      "v_mfma_f32_32x32x16_bf16 v[128:143], v[164:167], v[220:223], v[128:143]\n"
      "ds_read_b64_tr_b16 v[212:213], v30 offset:32768\n"
      "ds_read_b64_tr_b16 v[214:215], v30 offset:33280\n"
      "s_add_u32 s80, s70, 0x5000\n"
      "buffer_load_dwordx4 v[160:163], v1, s[20:23], s80 offen offset:2048 sc1\n"
      "v_mov_b32 v237, v235\n"
      "s_waitcnt lgkmcnt(6)\n"
      "v_mfma_f32_32x32x16_bf16 v[144:159], v[164:167], v[224:227], v[144:159]\n"
      "v_cmp_eq_u32 vcc, -1, v168\n"
      "ds_read_b64_tr_b16 v[216:217], v30 offset:4096\n"
      "ds_read_b64_tr_b16 v[218:219], v30 offset:4608\n"
      "v_permlane32_swap_b32 v234, v236\n"
      "v_permlane32_swap_b32 v235, v237\n"
      "s_waitcnt lgkmcnt(6)\n"
      "v_mfma_f32_32x32x16_bf16 v[192:207], v[228:231], v[164:167], v[192:207]\n"
      "s_cbranch_vccnz .Lpk_59\n"
      ".Lpr_60:\n"
      "ds_read_b64_tr_b16 v[220:221], v30 offset:19456\n"
      "ds_read_b64_tr_b16 v[222:223], v30 offset:19968\n"
      "v_add_f32 v234, v234, v236\n"
      "v_add_f32 v235, v235, v237\n"
      "s_waitcnt lgkmcnt(6)\n"
      "v_mfma_f32_32x32x16_bf16 v[128:143], v[168:171], v[208:211], v[128:143]\n"
      "ds_read_b64_tr_b16 v[224:225], v30 offset:33792\n"
      "ds_read_b64_tr_b16 v[226:227], v30 offset:34304\n"
      "buffer_load_dwordx4 v[164:167], v1, s[20:23], s80 offen offset:3072 sc1\n"
      "v_mul_f32 v234, 0xbb124925, v234\n"
      "v_mul_f32 v235, 0x3b124925, v235\n"
      "s_waitcnt vmcnt(6) lgkmcnt(6)\n"
      "v_mfma_f32_32x32x16_bf16 v[144:159], v[168:171], v[212:215], v[144:159]\n"
      "v_cmp_eq_u32 vcc, -1, v172\n"
      "ds_read_b64_tr_b16 v[228:229], v30 offset:5120\n"
      "ds_read_b64_tr_b16 v[230:231], v30 offset:5632\n"
      "v_mul_f32 v236, v234, v234\n"
      "v_sub_f32 v235, v235, v236\n"
      "s_waitcnt lgkmcnt(6)\n"
      "v_mfma_f32_32x32x16_bf16 v[192:207], v[216:219], v[168:171], v[192:207]\n"
      "s_cbranch_vccnz .Lpk_62\n"
      ".Lpr_63:\n"
      "ds_read_b64_tr_b16 v[208:209], v30 offset:20480\n"
      "ds_read_b64_tr_b16 v[210:211], v30 offset:20992\n"
      "v_fma_f32 v234, v235, 0, v234\n"
      "v_max_f32 v235, 0, v235\n"
      "s_waitcnt lgkmcnt(6)\n"
      "v_mfma_f32_32x32x16_bf16 v[128:143], v[172:175], v[220:223], v[128:143]\n"
      "ds_read_b64_tr_b16 v[212:213], v30 offset:34816\n"
      "ds_read_b64_tr_b16 v[214:215], v30 offset:35328\n"
      "s_add_u32 s80, s70, 0x6000\n"
      "buffer_load_dwordx4 v[168:171], v1, s[20:23], s80 offen offset:2048 sc1\n"
      "v_add_f32 v235, 0x3727c5ac, v235\n"
      "s_waitcnt vmcnt(6) lgkmcnt(6)\n"
      "v_mfma_f32_32x32x16_bf16 v[144:159], v[172:175], v[224:227], v[144:159]\n"
      "v_cmp_eq_u32 vcc, -1, v176\n"
      "ds_read_b64_tr_b16 v[216:217], v30 offset:21504\n"
      "ds_read_b64_tr_b16 v[218:219], v30 offset:22016\n"
      "v_rsq_f32 v235, v235\n"
      "s_waitcnt lgkmcnt(6)\n"
      "v_mfma_f32_32x32x16_bf16 v[192:207], v[228:231], v[172:175], v[192:207]\n"
      "s_cbranch_vccnz .Lpk_65\n"
      ".Lpr_66:\n"
      "ds_read_b64_tr_b16 v[220:221], v30 offset:35840\n"
      "ds_read_b64_tr_b16 v[222:223], v30 offset:36352\n"
      "ds_write_b32 v14, v235 offset:128\n"
      "v_mul_f32 v234, v234, v235\n"
      "s_waitcnt vmcnt(5) lgkmcnt(7)\n"
      "v_mfma_f32_32x32x16_bf16 v[128:143], v[176:179], v[208:211], v[128:143]\n"
      "v_cmp_eq_u32 vcc, -1, v180\n"
      "ds_read_b64_tr_b16 v[224:225], v30 offset:22528\n"
      "ds_read_b64_tr_b16 v[226:227], v30 offset:23040\n"
      "buffer_load_dwordx4 v[172:175], v1, s[20:23], s80 offen offset:3072 sc1\n"
      "ds_write_b32 v14, v234\n"
      "s_waitcnt lgkmcnt(8)\n"
      "v_mfma_f32_32x32x16_bf16 v[144:159], v[176:179], v[212:215], v[144:159]\n"
      "s_cbranch_vccnz .Lpk_68\n"
      ".Lpr_69:\n"
      "ds_read_b64_tr_b16 v[228:229], v30 offset:36864\n"
      "ds_read_b64_tr_b16 v[230:231], v30 offset:37376\n"
      "v_cmp_u_f32 vcc, v234, v234\n"
      "s_waitcnt vmcnt(5) lgkmcnt(8)\n"
      "v_mfma_f32_32x32x16_bf16 v[128:143], v[180:183], v[216:219], v[128:143]\n"
      "s_cbranch_vccnz .La1_st\n"
      ".La1_st_ok:\n"
      "v_cmp_eq_u32 vcc, -1, v184\n"
      "ds_read_b64_tr_b16 v[208:209], v30 offset:23552\n"
      "ds_read_b64_tr_b16 v[210:211], v30 offset:24064\n"
      "ds_read_b128 v[240:243], v15 offset:128\n"
      "s_waitcnt lgkmcnt(9)\n"
      "v_mfma_f32_32x32x16_bf16 v[144:159], v[180:183], v[220:223], v[144:159]\n"
      "s_cbranch_vccnz .Lpk_71\n"
      ".Lpr_72:\n"
      "ds_read_b64_tr_b16 v[212:213], v30 offset:37888\n"
      "ds_read_b64_tr_b16 v[214:215], v30 offset:38400\n"
      "ds_read_b128 v[244:247], v15 offset:160\n"
      "ds_read_b128 v[248:251], v15 offset:192\n"
      "s_waitcnt vmcnt(4) lgkmcnt(10)\n"
      "v_mfma_f32_32x32x16_bf16 v[128:143], v[184:187], v[224:227], v[128:143]\n"
      "v_cmp_eq_u32 vcc, -1, v188\n"
      "ds_read_b64_tr_b16 v[216:217], v30 offset:24576\n"
      "ds_read_b64_tr_b16 v[218:219], v30 offset:25088\n"
      "ds_read_b128 v[252:255], v15 offset:224\n"
      "s_waitcnt lgkmcnt(10)\n"
      "v_mfma_f32_32x32x16_bf16 v[144:159], v[184:187], v[228:231], v[144:159]\n"
      "s_cbranch_vccnz .Lpk_74\n"
      ".Lpr_75:\n"
      "ds_read_b64_tr_b16 v[220:221], v30 offset:38912\n"
      "ds_read_b64_tr_b16 v[222:223], v30 offset:39424\n"
      "s_waitcnt vmcnt(3) lgkmcnt(10)\n"
      "v_mfma_f32_32x32x16_bf16 v[128:143], v[188:191], v[208:211], v[128:143]\n"
      "v_cmp_eq_u32 vcc, -1, v160\n"
      "ds_read_b64_tr_b16 v[224:225], v30 offset:25600\n"
      "ds_read_b64_tr_b16 v[226:227], v30 offset:26112\n"
      "s_waitcnt lgkmcnt(9)\n"
      "v_mfma_f32_32x32x16_bf16 v[144:159], v[188:191], v[212:215], v[144:159]\n"
      "s_cbranch_vccnz .Lpk_77\n"
      ".Lpr_78:\n"
      "ds_read_b64_tr_b16 v[228:229], v30 offset:39936\n"
      "ds_read_b64_tr_b16 v[230:231], v30 offset:40448\n"
      "s_waitcnt vmcnt(2) lgkmcnt(7)\n"
      "v_mfma_f32_32x32x16_bf16 v[128:143], v[160:163], v[216:219], v[128:143]\n"
      "v_cmp_eq_u32 vcc, -1, v164\n"
      "ds_read_b64_tr_b16 v[208:209], v30 offset:26624\n"
      "ds_read_b64_tr_b16 v[210:211], v30 offset:27136\n"
      "s_waitcnt lgkmcnt(6)\n"
      "v_mfma_f32_32x32x16_bf16 v[144:159], v[160:163], v[220:223], v[144:159]\n"
      "s_cbranch_vccnz .Lpk_80\n"
      ".Lpr_81:\n"
      "ds_read_b64_tr_b16 v[212:213], v30 offset:40960\n"
      "ds_read_b64_tr_b16 v[214:215], v30 offset:41472\n"
      "s_waitcnt vmcnt(1) lgkmcnt(6)\n"
      "v_mfma_f32_32x32x16_bf16 v[128:143], v[164:167], v[224:227], v[128:143]\n"
      "v_cmp_eq_u32 vcc, -1, v168\n"
      "ds_read_b64_tr_b16 v[216:217], v30 offset:27648\n"
      "ds_read_b64_tr_b16 v[218:219], v30 offset:28160\n"
      "s_waitcnt lgkmcnt(6)\n"
      "v_mfma_f32_32x32x16_bf16 v[144:159], v[164:167], v[228:231], v[144:159]\n"
      "s_cbranch_vccnz .Lpk_83\n"
      ".Lpr_84:\n"
      "ds_read_b64_tr_b16 v[220:221], v30 offset:41984\n"
      "ds_read_b64_tr_b16 v[222:223], v30 offset:42496\n"
      "s_waitcnt vmcnt(0) lgkmcnt(6)\n"
      "v_mfma_f32_32x32x16_bf16 v[128:143], v[168:171], v[208:211], v[128:143]\n"
      "v_cmp_eq_u32 vcc, -1, v172\n"
      "s_waitcnt lgkmcnt(4)\n"
      "v_mfma_f32_32x32x16_bf16 v[144:159], v[168:171], v[212:215], v[144:159]\n"
      "s_cbranch_vccnz .Lpk_86\n"
      ".Lpr_87:\n"
      "s_waitcnt lgkmcnt(2)\n"
      "v_mfma_f32_32x32x16_bf16 v[128:143], v[172:175], v[216:219], v[128:143]\n"
      "s_waitcnt lgkmcnt(0)\n"
      "v_mfma_f32_32x32x16_bf16 v[144:159], v[172:175], v[220:223], v[144:159]\n"
      ".La1_epi:\n"
      "ds_read_b128 v[208:211], v15 offset:0\n"
      "ds_read_b128 v[212:215], v15 offset:32\n"
      "ds_read_b128 v[216:219], v15 offset:64\n"
      "ds_read_b128 v[220:223], v15 offset:96\n"
      "s_waitcnt lgkmcnt(3)\n"
      "v_pk_fma_f32 v[232:233], v[208:209], v[16:17], s[90:91] op_sel_hi:[1,0,1]\n"
      "v_pk_fma_f32 v[236:237], v[210:211], v[16:17], s[90:91] op_sel_hi:[1,0,1]\n"
      "s_nop 2\n"
      "v_pk_fma_f32 v[224:225], v[128:129], v[240:241], v[232:233]\n"
      "v_pk_fma_f32 v[228:229], v[130:131], v[242:243], v[236:237]\n"
      "v_pk_fma_f32 v[226:227], v[224:225], s[88:89], s[88:89] op_sel:[0,0,1] op_sel_hi:[1,0,1]\n"
      "v_pk_fma_f32 v[230:231], v[228:229], s[88:89], s[88:89] op_sel:[0,0,1] op_sel_hi:[1,0,1]\n"
      "v_exp_f32 v226, v226 clamp\n"
      "v_exp_f32 v230, v230 clamp\n"
      "v_exp_f32 v227, v227 clamp\n"
      "v_exp_f32 v231, v231 clamp\n"
      "v_max_f32 v224, s90, v224\n"
      "v_max_f32 v228, s90, v228\n"
      "v_max_f32 v225, s90, v225\n"
      "v_max_f32 v229, s90, v229\n"
      "v_pk_add_f32 v[224:225], v[224:225], v[226:227]\n"
      "v_pk_add_f32 v[228:229], v[228:229], v[230:231]\n"
      "v_cvt_pk_bf16_f32 v128, v224, v225\n"
      "v_cvt_pk_bf16_f32 v129, v228, v229\n"
      "s_waitcnt lgkmcnt(2)\n"
      "v_pk_fma_f32 v[232:233], v[212:213], v[16:17], s[90:91] op_sel_hi:[1,0,1]\n"
      "v_pk_fma_f32 v[236:237], v[214:215], v[16:17], s[90:91] op_sel_hi:[1,0,1]\n"
      "v_pk_fma_f32 v[224:225], v[132:133], v[244:245], v[232:233]\n"
      "v_pk_fma_f32 v[228:229], v[134:135], v[246:247], v[236:237]\n"
      "v_pk_fma_f32 v[226:227], v[224:225], s[88:89], s[88:89] op_sel:[0,0,1] op_sel_hi:[1,0,1]\n"
      "v_pk_fma_f32 v[230:231], v[228:229], s[88:89], s[88:89] op_sel:[0,0,1] op_sel_hi:[1,0,1]\n"
      "v_exp_f32 v226, v226 clamp\n"
      "v_exp_f32 v230, v230 clamp\n"
      "v_exp_f32 v227, v227 clamp\n"
      "v_exp_f32 v231, v231 clamp\n"
      "v_max_f32 v224, s90, v224\n"
      "v_max_f32 v228, s90, v228\n"
      "v_max_f32 v225, s90, v225\n"
      "v_max_f32 v229, s90, v229\n"
      "v_pk_add_f32 v[224:225], v[224:225], v[226:227]\n"
      "v_pk_add_f32 v[228:229], v[228:229], v[230:231]\n"
      "v_cvt_pk_bf16_f32 v130, v224, v225\n"
      "v_cvt_pk_bf16_f32 v131, v228, v229\n"
      "s_waitcnt lgkmcnt(1)\n"
      "v_pk_fma_f32 v[232:233], v[216:217], v[16:17], s[90:91] op_sel_hi:[1,0,1]\n"
      "v_pk_fma_f32 v[236:237], v[218:219], v[16:17], s[90:91] op_sel_hi:[1,0,1]\n"
      "v_pk_fma_f32 v[224:225], v[136:137], v[248:249], v[232:233]\n"
      "v_pk_fma_f32 v[228:229], v[138:139], v[250:251], v[236:237]\n"
      "v_pk_fma_f32 v[226:227], v[224:225], s[88:89], s[88:89] op_sel:[0,0,1] op_sel_hi:[1,0,1]\n"
      "v_pk_fma_f32 v[230:231], v[228:229], s[88:89], s[88:89] op_sel:[0,0,1] op_sel_hi:[1,0,1]\n"
      "v_exp_f32 v226, v226 clamp\n"
      "v_exp_f32 v230, v230 clamp\n"
      "v_exp_f32 v227, v227 clamp\n"
      "v_exp_f32 v231, v231 clamp\n"
      "v_max_f32 v224, s90, v224\n"
      "v_max_f32 v228, s90, v228\n"
      "v_max_f32 v225, s90, v225\n"
      "v_max_f32 v229, s90, v229\n"
      "v_pk_add_f32 v[224:225], v[224:225], v[226:227]\n"
      "v_pk_add_f32 v[228:229], v[228:229], v[230:231]\n"
      "v_cvt_pk_bf16_f32 v132, v224, v225\n"
      "v_cvt_pk_bf16_f32 v133, v228, v229\n"
      "s_waitcnt lgkmcnt(0)\n"
      "v_pk_fma_f32 v[232:233], v[220:221], v[16:17], s[90:91] op_sel_hi:[1,0,1]\n"
      "v_pk_fma_f32 v[236:237], v[222:223], v[16:17], s[90:91] op_sel_hi:[1,0,1]\n"
      "v_pk_fma_f32 v[224:225], v[140:141], v[252:253], v[232:233]\n"
      "v_pk_fma_f32 v[228:229], v[142:143], v[254:255], v[236:237]\n"
      "v_pk_fma_f32 v[226:227], v[224:225], s[88:89], s[88:89] op_sel:[0,0,1] op_sel_hi:[1,0,1]\n"
      "v_pk_fma_f32 v[230:231], v[228:229], s[88:89], s[88:89] op_sel:[0,0,1] op_sel_hi:[1,0,1]\n"
      "v_exp_f32 v226, v226 clamp\n"
      "v_exp_f32 v230, v230 clamp\n"
      "v_exp_f32 v227, v227 clamp\n"
      "v_exp_f32 v231, v231 clamp\n"
      "v_max_f32 v224, s90, v224\n"
      "v_max_f32 v228, s90, v228\n"
      "v_max_f32 v225, s90, v225\n"
      "v_max_f32 v229, s90, v229\n"
      "v_pk_add_f32 v[224:225], v[224:225], v[226:227]\n"
      "v_pk_add_f32 v[228:229], v[228:229], v[230:231]\n"
      "v_cvt_pk_bf16_f32 v134, v224, v225\n"
      "v_cvt_pk_bf16_f32 v135, v228, v229\n"
      "v_pk_mul_f32 v[224:225], v[208:209], v[16:17] op_sel:[0,1]\n"
      "v_pk_mul_f32 v[226:227], v[210:211], v[16:17] op_sel:[0,1]\n"
      "v_pk_mul_f32 v[228:229], v[212:213], v[16:17] op_sel:[0,1]\n"
      "v_pk_mul_f32 v[230:231], v[214:215], v[16:17] op_sel:[0,1]\n"
      "v_pk_fma_f32 v[224:225], v[144:145], v[240:241], v[224:225]\n"
      "v_pk_fma_f32 v[226:227], v[146:147], v[242:243], v[226:227]\n"
      "v_pk_fma_f32 v[228:229], v[148:149], v[244:245], v[228:229]\n"
      "v_pk_fma_f32 v[230:231], v[150:151], v[246:247], v[230:231]\n"
      "v_cvt_pk_bf16_f32 v144, v224, v225\n"
      "v_cvt_pk_bf16_f32 v145, v226, v227\n"
      "v_cvt_pk_bf16_f32 v146, v228, v229\n"
      "v_cvt_pk_bf16_f32 v147, v230, v231\n"
      "v_pk_mul_f32 v[224:225], v[216:217], v[16:17] op_sel:[0,1]\n"
      "v_pk_mul_f32 v[226:227], v[218:219], v[16:17] op_sel:[0,1]\n"
      "v_pk_mul_f32 v[228:229], v[220:221], v[16:17] op_sel:[0,1]\n"
      "v_pk_mul_f32 v[230:231], v[222:223], v[16:17] op_sel:[0,1]\n"
      "v_pk_fma_f32 v[224:225], v[152:153], v[248:249], v[224:225]\n"
      "v_pk_fma_f32 v[226:227], v[154:155], v[250:251], v[226:227]\n"
      "v_pk_fma_f32 v[228:229], v[156:157], v[252:253], v[228:229]\n"
      "v_pk_fma_f32 v[230:231], v[158:159], v[254:255], v[230:231]\n"
      "v_cvt_pk_bf16_f32 v148, v224, v225\n"
      "v_cvt_pk_bf16_f32 v149, v226, v227\n"
      "v_cvt_pk_bf16_f32 v150, v228, v229\n"
      "v_cvt_pk_bf16_f32 v151, v230, v231\n"
      "s_mov_b32 exec_lo, 0x10000000\n"
      "s_mov_b32 exec_hi, 0x10000000\n"
      "v_pk_mov_b32 v[144:145], s[26:27], s[26:27]\n"
      "v_pk_mov_b32 v[146:147], s[26:27], s[26:27]\n"
      "v_pk_mov_b32 v[148:149], s[26:27], s[26:27]\n"
      "v_pk_mov_b32 v[150:151], s[26:27], s[26:27]\n"
      "s_mov_b64 exec, -1\n"
      "v_mfma_f32_32x32x16_bf16 v[208:223], v[128:131], v[144:147], 0\n"
      "v_mfma_f32_32x32x16_bf16 v[208:223], v[132:135], v[148:151], v[208:223]\n"
      "ds_read_b64_tr_b16 v[240:241], v30 offset:6144\n"
      "ds_read_b64_tr_b16 v[242:243], v30 offset:6656\n"
      "ds_read_b64_tr_b16 v[244:245], v30 offset:7168\n"
      "ds_read_b64_tr_b16 v[246:247], v30 offset:7680\n"
      "ds_read_b64_tr_b16 v[248:249], v30 offset:8192\n"
      "ds_read_b64_tr_b16 v[250:251], v30 offset:8704\n"
      "ds_read_b64_tr_b16 v[252:253], v30 offset:9216\n"
      "ds_read_b64_tr_b16 v[254:255], v30 offset:9728\n"
      "v_lshrrev_b32 v233, 3, v0\n"
      "v_mul_u32_u24 v233, 0x380, v233\n"
      "v_and_b32 v232, 0x7f, v6\n"
      "v_add_u32 v233, v233, v232\n"
      "v_cmp_u_f32 vcc, v208, v208\n"
      "ds_write_b128 v12, v[208:211] offset:0\n"
      "ds_write_b128 v12, v[212:215] offset:4096\n"
      "ds_write_b128 v12, v[216:219] offset:8192\n"
      "ds_write_b128 v12, v[220:223] offset:12288\n"
      "s_cbranch_vccnz .La1_nan\n"
      ".La1_ok:\n"
      "s_waitcnt lgkmcnt(0)\n"
      "s_barrier\n"
      "ds_read_b128 v[128:131], v13 offset:0\n"
      "ds_read_b128 v[132:135], v13 offset:1024\n"
      "ds_read_b128 v[136:139], v13 offset:2048\n"
      "ds_read_b128 v[140:143], v13 offset:3072\n"
      "s_waitcnt lgkmcnt(2)\n"
      "v_pk_add_f32 v[128:129], v[128:129], v[132:133]\n"
      "v_pk_add_f32 v[130:131], v[130:131], v[134:135]\n"
      "s_waitcnt lgkmcnt(0)\n"
      "v_pk_add_f32 v[136:137], v[136:137], v[140:141]\n"
      "v_pk_add_f32 v[138:139], v[138:139], v[142:143]\n"
      "v_pk_add_f32 v[128:129], v[128:129], v[136:137]\n"
      "v_pk_add_f32 v[130:131], v[130:131], v[138:139]\n"
      "v_cvt_pk_bf16_f32 v132, v128, v129\n"
      "v_cvt_pk_bf16_f32 v133, v130, v131\n"
      "v_min_u32 v132, -2, v132\n"
      "v_min_u32 v133, -2, v133\n"
      "buffer_store_dwordx2 v[132:133], v238, s[20:23], s58 offen sc1\n"
      "v_lshrrev_b32 v236, 5, v0\n"
      "v_lshlrev_b32 v236, 4, v236\n"
      "v_add_u32 v236, 0x26000, v236\n"
      "ds_read_b128 v[208:211], v236 offset:0\n"
      "ds_read_b128 v[212:215], v236 offset:32\n"
      "ds_read_b128 v[216:219], v236 offset:64\n"
      "ds_read_b128 v[220:223], v236 offset:96\n"
      "v_mfma_f32_32x32x16_bf16 v[192:207], v[240:243], v[176:179], v[192:207]\n"
      "v_mfma_f32_32x32x16_bf16 v[192:207], v[244:247], v[180:183], v[192:207]\n"
      "ds_read_b64_tr_b16 v[240:241], v30 offset:10240\n"
      "ds_read_b64_tr_b16 v[242:243], v30 offset:10752\n"
      "s_add_u32 m0, s76, 0xc000\n"
      "s_add_u32 s78, s73, 0x18800\n"
      "buffer_load_dwordx4 v233, s[32:35], s78 offen lds\n"
      "v_mfma_f32_32x32x16_bf16 v[192:207], v[248:251], v[184:187], v[192:207]\n"
      "ds_read_b64_tr_b16 v[244:245], v30 offset:11264\n"
      "ds_read_b64_tr_b16 v[246:247], v30 offset:11776\n"
      "v_mfma_f32_32x32x16_bf16 v[192:207], v[252:255], v[188:191], v[192:207]\n"
      "ds_read_b64_tr_b16 v[248:249], v30 offset:12288\n"
      "ds_read_b64_tr_b16 v[250:251], v30 offset:12800\n"
      "s_add_u32 s78, s78, 0x1800\n"
      "buffer_load_dwordx4 v233, s[32:35], s78 offen offset:1024 lds\n"
      "s_waitcnt lgkmcnt(4)\n"
      "v_mfma_f32_32x32x16_bf16 v[192:207], v[240:243], v[160:163], v[192:207]\n"
      "ds_read_b64_tr_b16 v[252:253], v30 offset:13312\n"
      "ds_read_b64_tr_b16 v[254:255], v30 offset:13824\n"
      "s_waitcnt lgkmcnt(4)\n"
      "v_mfma_f32_32x32x16_bf16 v[192:207], v[244:247], v[164:167], v[192:207]\n"
      "s_add_u32 s78, s78, 0x1800\n"
      "buffer_load_dwordx4 v233, s[32:35], s78 offen offset:2048 lds\n"
      "s_waitcnt lgkmcnt(2)\n"
      "v_mfma_f32_32x32x16_bf16 v[192:207], v[248:251], v[168:171], v[192:207]\n"
      "s_waitcnt lgkmcnt(0)\n"
      "v_mfma_f32_32x32x16_bf16 v[192:207], v[252:255], v[172:175], v[192:207]\n"
      "s_add_u32 s78, s78, 0x1800\n"
      "buffer_load_dwordx4 v233, s[32:35], s78 offen offset:3072 lds\n"
      "v_pk_fma_f32 v[144:145], v[208:209], v[234:235], s[90:91] op_sel_hi:[1,0,1]\n"
      "v_pk_fma_f32 v[146:147], v[210:211], v[234:235], s[90:91] op_sel_hi:[1,0,1]\n"
      "s_nop 7\n"
      "v_pk_fma_f32 v[148:149], v[192:193], v[234:235], v[144:145] op_sel:[0,1,0]\n"
      "v_pk_fma_f32 v[152:153], v[194:195], v[234:235], v[146:147] op_sel:[0,1,0]\n"
      "v_pk_fma_f32 v[150:151], v[148:149], s[88:89], s[88:89] op_sel:[0,0,1] op_sel_hi:[1,0,1]\n"
      "v_pk_fma_f32 v[154:155], v[152:153], s[88:89], s[88:89] op_sel:[0,0,1] op_sel_hi:[1,0,1]\n"
      "v_exp_f32 v150, v150 clamp\n"
      "v_exp_f32 v154, v154 clamp\n"
      "v_exp_f32 v151, v151 clamp\n"
      "v_exp_f32 v155, v155 clamp\n"
      "v_max_f32 v148, s90, v148\n"
      "v_max_f32 v152, s90, v152\n"
      "v_max_f32 v149, s90, v149\n"
      "v_max_f32 v153, s90, v153\n"
      "v_pk_add_f32 v[148:149], v[148:149], v[150:151]\n"
      "v_pk_add_f32 v[152:153], v[152:153], v[154:155]\n"
      "v_cvt_pk_bf16_f32 v248, v148, v149\n"
      "v_cvt_pk_bf16_f32 v249, v152, v153\n"
      "s_add_u32 m0, s76, 0x10000\n"
      "s_add_u32 s78, s73, 0x31000\n"
      "buffer_load_dwordx4 v233, s[32:35], s78 offen lds\n"
      "s_add_u32 s78, s78, 0x1800\n"
      "buffer_load_dwordx4 v233, s[32:35], s78 offen offset:1024 lds\n"
      "v_pk_fma_f32 v[144:145], v[212:213], v[234:235], s[90:91] op_sel_hi:[1,0,1]\n"
      "v_pk_fma_f32 v[146:147], v[214:215], v[234:235], s[90:91] op_sel_hi:[1,0,1]\n"
      "v_pk_fma_f32 v[148:149], v[196:197], v[234:235], v[144:145] op_sel:[0,1,0]\n"
      "v_pk_fma_f32 v[152:153], v[198:199], v[234:235], v[146:147] op_sel:[0,1,0]\n"
      "v_pk_fma_f32 v[150:151], v[148:149], s[88:89], s[88:89] op_sel:[0,0,1] op_sel_hi:[1,0,1]\n"
      "v_pk_fma_f32 v[154:155], v[152:153], s[88:89], s[88:89] op_sel:[0,0,1] op_sel_hi:[1,0,1]\n"
      "v_exp_f32 v150, v150 clamp\n"
      "v_exp_f32 v154, v154 clamp\n"
      "v_exp_f32 v151, v151 clamp\n"
      "v_exp_f32 v155, v155 clamp\n"
      "v_max_f32 v148, s90, v148\n"
      "v_max_f32 v152, s90, v152\n"
      "v_max_f32 v149, s90, v149\n"
      "v_max_f32 v153, s90, v153\n"
      "v_pk_add_f32 v[148:149], v[148:149], v[150:151]\n"
      "v_pk_add_f32 v[152:153], v[152:153], v[154:155]\n"
      "v_cvt_pk_bf16_f32 v250, v148, v149\n"
      "v_cvt_pk_bf16_f32 v251, v152, v153\n"
      "s_add_u32 s78, s78, 0x1800\n"
      "buffer_load_dwordx4 v233, s[32:35], s78 offen offset:2048 lds\n"
      "s_add_u32 s78, s78, 0x1800\n"
      "buffer_load_dwordx4 v233, s[32:35], s78 offen offset:3072 lds\n"
      "v_pk_fma_f32 v[144:145], v[216:217], v[234:235], s[90:91] op_sel_hi:[1,0,1]\n"
      "v_pk_fma_f32 v[146:147], v[218:219], v[234:235], s[90:91] op_sel_hi:[1,0,1]\n"
      "v_pk_fma_f32 v[148:149], v[200:201], v[234:235], v[144:145] op_sel:[0,1,0]\n"
      "v_pk_fma_f32 v[152:153], v[202:203], v[234:235], v[146:147] op_sel:[0,1,0]\n"
      "v_pk_fma_f32 v[150:151], v[148:149], s[88:89], s[88:89] op_sel:[0,0,1] op_sel_hi:[1,0,1]\n"
      "v_pk_fma_f32 v[154:155], v[152:153], s[88:89], s[88:89] op_sel:[0,0,1] op_sel_hi:[1,0,1]\n"
      "v_exp_f32 v150, v150 clamp\n"
      "v_exp_f32 v154, v154 clamp\n"
      "v_exp_f32 v151, v151 clamp\n"
      "v_exp_f32 v155, v155 clamp\n"
      "v_max_f32 v148, s90, v148\n"
      "v_max_f32 v152, s90, v152\n"
      "v_max_f32 v149, s90, v149\n"
      "v_max_f32 v153, s90, v153\n"
      "v_pk_add_f32 v[148:149], v[148:149], v[150:151]\n"
      "v_pk_add_f32 v[152:153], v[152:153], v[154:155]\n"
      "v_cvt_pk_bf16_f32 v252, v148, v149\n"
      "v_cvt_pk_bf16_f32 v253, v152, v153\n"
      "s_add_u32 m0, s76, 0x14000\n"
      "s_add_u32 s78, s73, 0x49800\n"
      "buffer_load_dwordx4 v233, s[32:35], s78 offen lds\n"
      "s_add_u32 s78, s78, 0x1800\n"
      "buffer_load_dwordx4 v233, s[32:35], s78 offen offset:1024 lds\n"
      "v_pk_fma_f32 v[144:145], v[220:221], v[234:235], s[90:91] op_sel_hi:[1,0,1]\n"
      "v_pk_fma_f32 v[146:147], v[222:223], v[234:235], s[90:91] op_sel_hi:[1,0,1]\n"
      "v_pk_fma_f32 v[148:149], v[204:205], v[234:235], v[144:145] op_sel:[0,1,0]\n"
      "v_pk_fma_f32 v[152:153], v[206:207], v[234:235], v[146:147] op_sel:[0,1,0]\n"
      "v_pk_fma_f32 v[150:151], v[148:149], s[88:89], s[88:89] op_sel:[0,0,1] op_sel_hi:[1,0,1]\n"
      "v_pk_fma_f32 v[154:155], v[152:153], s[88:89], s[88:89] op_sel:[0,0,1] op_sel_hi:[1,0,1]\n"
      "v_exp_f32 v150, v150 clamp\n"
      "v_exp_f32 v154, v154 clamp\n"
      "v_exp_f32 v151, v151 clamp\n"
      "v_exp_f32 v155, v155 clamp\n"
      "v_max_f32 v148, s90, v148\n"
      "v_max_f32 v152, s90, v152\n"
      "v_max_f32 v149, s90, v149\n"
      "v_max_f32 v153, s90, v153\n"
      "v_pk_add_f32 v[148:149], v[148:149], v[150:151]\n"
      "v_pk_add_f32 v[152:153], v[152:153], v[154:155]\n"
      "v_cvt_pk_bf16_f32 v254, v148, v149\n"
      "v_cvt_pk_bf16_f32 v255, v152, v153\n"
      "s_add_u32 s78, s78, 0x1800\n"
      "buffer_load_dwordx4 v233, s[32:35], s78 offen offset:2048 lds\n"
      "s_add_u32 s78, s78, 0x1800\n"
      "buffer_load_dwordx4 v233, s[32:35], s78 offen offset:3072 lds\n"
      "s_add_u32 m0, s76, 0x8000\n"
      "s_mov_b32 s78, s73\n"
      "buffer_load_dwordx4 v233, s[32:35], s78 offen lds\n"
      "s_add_u32 s78, s78, 0x1800\n"
      "buffer_load_dwordx4 v233, s[32:35], s78 offen offset:1024 lds\n"
      "s_add_u32 s78, s78, 0x1800\n"
      "buffer_load_dwordx4 v233, s[32:35], s78 offen offset:2048 lds\n"
      "s_add_u32 s78, s78, 0x1800\n"
      "buffer_load_dwordx4 v233, s[32:35], s78 offen offset:3072 lds\n"
      "s_cmp_lg_u32 s53, 0\n"
      "s_cbranch_scc1 .Lbg_92\n"
      "s_mul_i32 s78, s10, 64\n"
      "s_add_u32 s78, s78, s75\n"
      "s_add_u32 m0, s76, 0x18000\n"
      "s_add_u32 s78, s78, 0x20000\n"
      "buffer_load_dwordx4 v6, s[40:43], s78 offen lds\n"
      "s_add_u32 s78, s78, 0x7c00\n"
      "buffer_load_dwordx4 v6, s[40:43], s78 offen offset:1024 lds\n"
      "s_add_u32 s78, s78, 0x7c00\n"
      "buffer_load_dwordx4 v6, s[40:43], s78 offen offset:2048 lds\n"
      "s_add_u32 s78, s78, 0x7c00\n"
      "buffer_load_dwordx4 v6, s[40:43], s78 offen offset:3072 lds\n"
      "s_mul_i32 s78, s11, 64\n"
      "s_add_u32 s78, s78, s75\n"
      "s_add_u32 m0, s76, 0x1c000\n"
      "s_add_u32 s78, s78, 0x20000\n"
      "buffer_load_dwordx4 v6, s[40:43], s78 offen lds\n"
      "s_add_u32 s78, s78, 0x7c00\n"
      "buffer_load_dwordx4 v6, s[40:43], s78 offen offset:1024 lds\n"
      "s_add_u32 s78, s78, 0x7c00\n"
      "buffer_load_dwordx4 v6, s[40:43], s78 offen offset:2048 lds\n"
      "s_add_u32 s78, s78, 0x7c00\n"
      "buffer_load_dwordx4 v6, s[40:43], s78 offen offset:3072 lds\n"
      "s_branch .Lbg_93\n"
      ".Lbg_92:\n"
      ".Lbg_93:\n"
      "s_mov_b32 exec_lo, 0\n"
      "v_mov_b32 v254, 0\n"
      "v_mov_b32 v255, 0\n"
      "s_mov_b64 exec, -1\n"
      "v_lshrrev_b32 v170, 5, v0\n"
      "v_mul_u32_u24 v170, 0x600, v170\n"
      "v_add_u32 v170, v170, v1\n"
      "s_waitcnt vmcnt(16)\n"
      "s_mov_b32 s80, s65\n"
      "buffer_load_dwordx4 v[128:131], v170, s[20:23], s80 offen sc1\n"
      "s_add_u32 s80, s65, 0x1000\n"
      "buffer_load_dwordx4 v[132:135], v170, s[20:23], s80 offen sc1\n"
      "s_add_u32 s80, s65, 0x2000\n"
      "buffer_load_dwordx4 v[136:139], v170, s[20:23], s80 offen sc1\n"
      "s_add_u32 s80, s65, 0x3000\n"
      "buffer_load_dwordx4 v[140:143], v170, s[20:23], s80 offen sc1\n"
      "s_add_u32 s80, s65, 0x4000\n"
      "buffer_load_dwordx4 v[144:147], v170, s[20:23], s80 offen sc1\n"
      "s_add_u32 s80, s65, 0x5000\n"
      "buffer_load_dwordx4 v[148:151], v170, s[20:23], s80 offen sc1\n"
      "s_add_u32 s80, s65, 0x6000\n"
      "buffer_load_dwordx4 v[152:155], v170, s[20:23], s80 offen sc1\n"
      "s_add_u32 s80, s65, 0x7000\n"
      "buffer_load_dwordx4 v[156:159], v170, s[20:23], s80 offen sc1\n"
      "s_mov_b32 s82, 0x3f80\n"
      "s_mov_b32 s83, 0x3f800000\n"
      "s_lshl_b32 s85, s52, 8\n"
      "s_add_u32 s85, s85, 0xc00\n"
      "v_lshlrev_b32 v168, 2, v0\n"
      "buffer_load_dword v169, v168, s[20:23], s85 offen sc1\n"
      "s_lshr_b32 s81, s52, 1\n"
      "s_lshl_b32 s81, s81, 10\n"
      "s_and_b32 s84, s52, 1\n"
      "s_lshl3_add_u32 s81, s84, s81\n"
      "s_add_u32 s81, s81, 0x26a80\n"
      "v_and_b32 v236, 31, v0\n"
      "v_lshrrev_b32 v237, 5, v0\n"
      "v_lshl_add_u32 v236, v236, 1, v237\n"
      "v_lshl_add_u32 v236, v236, 4, s81\n"
      "v_add_u32 v237, 0x26a80, v1\n"
      "v_pk_mov_b32 v[160:161], 0, 0\n"
      "v_pk_mov_b32 v[162:163], 0, 0\n"
      "v_pk_mov_b32 v[164:165], 0, 0\n"
      "v_pk_mov_b32 v[166:167], 0, 0\n"
      "s_waitcnt vmcnt(8)\n"
      "v_dot2c_f32_bf16 v160, s82, v128\n"
      "v_dot2c_f32_bf16 v161, s83, v128\n"
      "v_dot2c_f32_bf16 v162, s82, v129\n"
      "v_dot2c_f32_bf16 v163, s83, v129\n"
      "v_dot2c_f32_bf16 v164, s82, v130\n"
      "v_dot2c_f32_bf16 v165, s83, v130\n"
      "v_dot2c_f32_bf16 v166, s82, v131\n"
      "v_dot2c_f32_bf16 v167, s83, v131\n"
      "s_waitcnt vmcnt(7)\n"
      "v_dot2c_f32_bf16 v160, s82, v132\n"
      "v_dot2c_f32_bf16 v161, s83, v132\n"
      "v_dot2c_f32_bf16 v162, s82, v133\n"
      "v_dot2c_f32_bf16 v163, s83, v133\n"
      "v_dot2c_f32_bf16 v164, s82, v134\n"
      "v_dot2c_f32_bf16 v165, s83, v134\n"
      "v_dot2c_f32_bf16 v166, s82, v135\n"
      "v_dot2c_f32_bf16 v167, s83, v135\n"
      "s_waitcnt vmcnt(6)\n"
      "v_dot2c_f32_bf16 v160, s82, v136\n"
      "v_dot2c_f32_bf16 v161, s83, v136\n"
      "v_dot2c_f32_bf16 v162, s82, v137\n"
      "v_dot2c_f32_bf16 v163, s83, v137\n"
      "v_dot2c_f32_bf16 v164, s82, v138\n"
      "v_dot2c_f32_bf16 v165, s83, v138\n"
      "v_dot2c_f32_bf16 v166, s82, v139\n"
      "v_dot2c_f32_bf16 v167, s83, v139\n"
      "s_waitcnt vmcnt(5)\n"
      "v_dot2c_f32_bf16 v160, s82, v140\n"
      "v_dot2c_f32_bf16 v161, s83, v140\n"
      "v_dot2c_f32_bf16 v162, s82, v141\n"
      "v_dot2c_f32_bf16 v163, s83, v141\n"
      "v_dot2c_f32_bf16 v164, s82, v142\n"
      "v_dot2c_f32_bf16 v165, s83, v142\n"
      "v_dot2c_f32_bf16 v166, s82, v143\n"
      "v_dot2c_f32_bf16 v167, s83, v143\n"
      "s_waitcnt vmcnt(4)\n"
      "v_dot2c_f32_bf16 v160, s82, v144\n"
      "v_dot2c_f32_bf16 v161, s83, v144\n"
      "v_dot2c_f32_bf16 v162, s82, v145\n"
      "v_dot2c_f32_bf16 v163, s83, v145\n"
      "v_dot2c_f32_bf16 v164, s82, v146\n"
      "v_dot2c_f32_bf16 v165, s83, v146\n"
      "v_dot2c_f32_bf16 v166, s82, v147\n"
      "v_dot2c_f32_bf16 v167, s83, v147\n"
      "s_waitcnt vmcnt(3)\n"
      "v_dot2c_f32_bf16 v160, s82, v148\n"
      "v_dot2c_f32_bf16 v161, s83, v148\n"
      "v_dot2c_f32_bf16 v162, s82, v149\n"
      "v_dot2c_f32_bf16 v163, s83, v149\n"
      "v_dot2c_f32_bf16 v164, s82, v150\n"
      "v_dot2c_f32_bf16 v165, s83, v150\n"
      "v_dot2c_f32_bf16 v166, s82, v151\n"
      "v_dot2c_f32_bf16 v167, s83, v151\n"
      "s_waitcnt vmcnt(2)\n"
      "v_dot2c_f32_bf16 v160, s82, v152\n"
      "v_dot2c_f32_bf16 v161, s83, v152\n"
      "v_dot2c_f32_bf16 v162, s82, v153\n"
      "v_dot2c_f32_bf16 v163, s83, v153\n"
      "v_dot2c_f32_bf16 v164, s82, v154\n"
      "v_dot2c_f32_bf16 v165, s83, v154\n"
      "v_dot2c_f32_bf16 v166, s82, v155\n"
      "v_dot2c_f32_bf16 v167, s83, v155\n"
      "s_waitcnt vmcnt(1)\n"
      "v_dot2c_f32_bf16 v160, s82, v156\n"
      "v_dot2c_f32_bf16 v161, s83, v156\n"
      "v_dot2c_f32_bf16 v162, s82, v157\n"
      "v_dot2c_f32_bf16 v163, s83, v157\n"
      "v_dot2c_f32_bf16 v164, s82, v158\n"
      "v_dot2c_f32_bf16 v165, s83, v158\n"
      "v_dot2c_f32_bf16 v166, s82, v159\n"
      "v_dot2c_f32_bf16 v167, s83, v159\n"
      "v_permlane32_swap_b32 v160, v164\n"
      "v_permlane32_swap_b32 v161, v165\n"
      "v_permlane32_swap_b32 v162, v166\n"
      "v_permlane32_swap_b32 v163, v167\n"
      "v_pk_add_f32 v[160:161], v[160:161], v[164:165]\n"
      "v_pk_add_f32 v[162:163], v[162:163], v[166:167]\n"
      "v_cmp_u_f32 vcc, v160, v162\n"
      "v_cvt_pk_bf16_f32 v164, v160, v161\n"
      "v_cvt_pk_bf16_f32 v165, v162, v163\n"
      "s_cbranch_vccnz .Lkv_bad\n"
      ".Lkv_ok:\n"
      "s_waitcnt vmcnt(0)\n"
      "v_cmp_ne_u32 vcc, s56, v169\n"
      "ds_write_b64 v236, v[164:165]\n"
      "s_cbranch_vccnz .Lfa_wait\n"
      ".Lfa_ok:\n"
      "s_waitcnt lgkmcnt(0)\n"
      "s_barrier\n"
      "ds_read_b128 v[168:171], v237\n"
      "ds_read_b128 v[172:175], v237 offset:1024\n"
      "s_waitcnt lgkmcnt(1)\n"
      "v_mfma_f32_32x32x16_bf16 v[144:159], v[168:171], v[248:251], 0\n"
      "s_waitcnt lgkmcnt(0)\n"
      "v_mfma_f32_32x32x16_bf16 v[144:159], v[172:175], v[252:255], v[144:159]\n"
      "s_nop 11\n"
      "v_mov_b32 v236, v156\n"
      "v_mov_b32 v237, v156\n"
      "s_nop 1\n"
      "v_permlane32_swap_b32 v236, v237\n"
      "v_add_f32 v237, 0x358637bd, v237\n"
      "v_rcp_f32 v236, v237\n"
      "s_nop 0\n"
      "v_pk_mul_f32 v[144:145], v[144:145], v[236:237] op_sel_hi:[1,0]\n"
      "v_pk_mul_f32 v[146:147], v[146:147], v[236:237] op_sel_hi:[1,0]\n"
      "v_pk_mul_f32 v[148:149], v[148:149], v[236:237] op_sel_hi:[1,0]\n"
      "v_pk_mul_f32 v[150:151], v[150:151], v[236:237] op_sel_hi:[1,0]\n"
      "v_cvt_pk_bf16_f32 v176, v144, v145\n"
      "v_cvt_pk_bf16_f32 v177, v146, v147\n"
      "v_cvt_pk_bf16_f32 v178, v148, v149\n"
      "v_cvt_pk_bf16_f32 v179, v150, v151\n"
      "v_min_u32 v176, -2, v176\n"
      "v_min_u32 v177, -2, v177\n"
      "v_min_u32 v178, -2, v178\n"
      "v_min_u32 v179, -2, v179\n"
      "buffer_store_dwordx4 v[176:179], v1, s[20:23], s59 offen sc1\n"
      "s_mov_b32 exec_lo, 0\n"
      "v_pk_mov_b32 v[156:157], 0, 0\n"
      "v_pk_mov_b32 v[158:159], 0, 0\n"
      "s_mov_b32 exec_lo, -1\n"
      "v_pk_mul_f32 v[152:153], v[152:153], v[236:237] op_sel_hi:[1,0]\n"
      "v_pk_mul_f32 v[154:155], v[154:155], v[236:237] op_sel_hi:[1,0]\n"
      "v_pk_mul_f32 v[156:157], v[156:157], v[236:237] op_sel_hi:[1,0]\n"
      "v_pk_mul_f32 v[158:159], v[158:159], v[236:237] op_sel_hi:[1,0]\n"
      "v_cvt_pk_bf16_f32 v180, v152, v153\n"
      "v_cvt_pk_bf16_f32 v181, v154, v155\n"
      "v_cvt_pk_bf16_f32 v182, v156, v157\n"
      "v_cvt_pk_bf16_f32 v183, v158, v159\n"
      "v_min_u32 v180, -2, v180\n"
      "v_min_u32 v181, -2, v181\n"
      "v_min_u32 v182, -2, v182\n"
      "v_min_u32 v183, -2, v183\n"
      "buffer_store_dwordx4 v[180:183], v1, s[20:23], s59 offen offset:1024 sc1\n"
      "s_cmp_eq_u32 s54, 0\n"
      "s_cbranch_scc1 .Lhelper\n"
      "v_and_b32 v248, 0x39f, v2\n"
      "v_bfe_u32 v249, v0, 3, 1\n"
      "v_mul_u32_u24 v249, 0x60, v249\n"
      "v_or_b32 v248, v248, v249\n"
      "v_and_b32 v249, 16, v0\n"
      "v_lshlrev_b32 v249, 2, v249\n"
      "v_xor_b32 v248, v248, v249\n"
      "s_and_b32 s81, s52, 1\n"
      "s_mul_i32 s81, s81, 0x4020\n"
      "s_and_b32 s82, s18, 1\n"
      "s_lshl_b32 s82, s82, 15\n"
      "s_add_u32 s81, s81, s82\n"
      "v_xor_b32 v248, s81, v248\n"
      "v_xor_b32 v252, 0x8000, v248\n"
      "v_add_u32 v248, 0x8000, v248\n"
      "v_add_u32 v252, 0x8000, v252\n"
      "v_xor_b32 v249, 32, v248\n"
      "v_xor_b32 v253, 32, v252\n"
      "s_mov_b32 s64, 0\n"
      "s_xor_b32 s81, s76, 0x1000\n"
      "s_add_u32 s81, s81, 0x20000\n"
      "v_add_u32 v250, s81, v1\n"
      "s_add_u32 s81, s76, 0x20000\n"
      "v_add_u32 v251, s81, v1\n"
      "s_cmp_lg_u32 s53, 0\n"
      "s_cbranch_scc1 .Lbg_96\n"
      "ds_read_b64_tr_b16 v[32:33], v4 offset:32768\n"
      "ds_read_b64_tr_b16 v[34:35], v4 offset:33792\n"
      "ds_read_b64_tr_b16 v[36:37], v5 offset:32768\n"
      "ds_read_b64_tr_b16 v[38:39], v5 offset:33792\n"
      "ds_read_b64_tr_b16 v[40:41], v4 offset:34816\n"
      "ds_read_b64_tr_b16 v[42:43], v4 offset:35840\n"
      "ds_read_b64_tr_b16 v[44:45], v5 offset:34816\n"
      "ds_read_b64_tr_b16 v[46:47], v5 offset:35840\n"
      "ds_read_b64_tr_b16 v[48:49], v4 offset:49152\n"
      "ds_read_b64_tr_b16 v[50:51], v4 offset:50176\n"
      "ds_read_b64_tr_b16 v[52:53], v5 offset:49152\n"
      "ds_read_b64_tr_b16 v[54:55], v5 offset:50176\n"
      "ds_read_b64_tr_b16 v[56:57], v4 offset:51200\n"
      "ds_read_b64_tr_b16 v[58:59], v4 offset:52224\n"
      "ds_read_b64_tr_b16 v[60:61], v5 offset:51200\n"
      "ds_read_b64_tr_b16 v[62:63], v5 offset:52224\n"
      "s_waitcnt lgkmcnt(0)\n"
      "s_mul_i32 s78, s13, 64\n"
      "s_add_u32 s78, s78, s75\n"
      "s_mov_b32 m0, s76\n"
      "s_nop 0\n"
      "buffer_load_dwordx4 v6, s[40:43], s78 offen lds\n"
      "s_add_u32 s78, s78, 0x7c00\n"
      "buffer_load_dwordx4 v6, s[40:43], s78 offen offset:1024 lds\n"
      "s_add_u32 s78, s78, 0x7c00\n"
      "buffer_load_dwordx4 v6, s[40:43], s78 offen offset:2048 lds\n"
      "s_add_u32 s78, s78, 0x7c00\n"
      "buffer_load_dwordx4 v6, s[40:43], s78 offen offset:3072 lds\n"
      "s_mul_i32 s78, s19, 64\n"
      "s_add_u32 s78, s78, s75\n"
      "s_add_u32 m0, s76, 0x4000\n"
      "s_add_u32 s78, s78, 0x20000\n"
      "buffer_load_dwordx4 v6, s[40:43], s78 offen lds\n"
      "s_add_u32 s78, s78, 0x7c00\n"
      "buffer_load_dwordx4 v6, s[40:43], s78 offen offset:1024 lds\n"
      "s_add_u32 s78, s78, 0x7c00\n"
      "buffer_load_dwordx4 v6, s[40:43], s78 offen offset:2048 lds\n"
      "s_add_u32 s78, s78, 0x7c00\n"
      "buffer_load_dwordx4 v6, s[40:43], s78 offen offset:3072 lds\n"
      "s_mul_i32 s78, s99, 64\n"
      "s_add_u32 s78, s78, s75\n"
      "s_add_u32 m0, s76, 0x18000\n"
      "s_add_u32 s78, s78, 0x20000\n"
      "buffer_load_dwordx4 v6, s[40:43], s78 offen lds\n"
      "s_add_u32 s78, s78, 0x7c00\n"
      "buffer_load_dwordx4 v6, s[40:43], s78 offen offset:1024 lds\n"
      "s_add_u32 s78, s78, 0x7c00\n"
      "buffer_load_dwordx4 v6, s[40:43], s78 offen offset:2048 lds\n"
      "s_add_u32 s78, s78, 0x7c00\n"
      "buffer_load_dwordx4 v6, s[40:43], s78 offen offset:3072 lds\n"
      "s_mul_i32 s78, s16, 64\n"
      "s_add_u32 s78, s78, s75\n"
      "s_add_u32 m0, s76, 0x1c000\n"
      "s_add_u32 s78, s78, 0x20000\n"
      "buffer_load_dwordx4 v6, s[40:43], s78 offen lds\n"
      "s_add_u32 s78, s78, 0x7c00\n"
      "buffer_load_dwordx4 v6, s[40:43], s78 offen offset:1024 lds\n"
      "s_add_u32 s78, s78, 0x7c00\n"
      "buffer_load_dwordx4 v6, s[40:43], s78 offen offset:2048 lds\n"
      "s_add_u32 s78, s78, 0x7c00\n"
      "buffer_load_dwordx4 v6, s[40:43], s78 offen offset:3072 lds\n"
      "s_waitcnt vmcnt(16)\n"
      "s_branch .Lbg_97\n"
      ".Lbg_96:\n"
      "s_mul_i32 s78, s19, 64\n"
      "s_add_u32 s78, s78, s75\n"
      "s_mov_b32 m0, s76\n"
      "s_nop 0\n"
      "buffer_load_dwordx4 v6, s[40:43], s78 offen lds\n"
      "s_add_u32 s78, s78, 0x7c00\n"
      "buffer_load_dwordx4 v6, s[40:43], s78 offen offset:1024 lds\n"
      "s_add_u32 s78, s78, 0x7c00\n"
      "buffer_load_dwordx4 v6, s[40:43], s78 offen offset:2048 lds\n"
      "s_add_u32 s78, s78, 0x7c00\n"
      "buffer_load_dwordx4 v6, s[40:43], s78 offen offset:3072 lds\n"
      "s_sleep 19\n"
      "s_waitcnt vmcnt(4)\n"
      ".Lbg_97:\n"
      "s_mov_b32 s80, s66\n"
      "buffer_load_dwordx4 v[128:131], v1, s[20:23], s80 offen sc1\n"
      "buffer_load_dwordx4 v[132:135], v1, s[20:23], s80 offen offset:1024 sc1\n"
      "buffer_load_dwordx4 v[136:139], v1, s[20:23], s80 offen offset:2048 sc1\n"
      "buffer_load_dwordx4 v[140:143], v1, s[20:23], s80 offen offset:3072 sc1\n"
      "s_add_u32 s80, s66, 0x1000\n"
      "buffer_load_dwordx4 v[144:147], v1, s[20:23], s80 offen sc1\n"
      "buffer_load_dwordx4 v[148:151], v1, s[20:23], s80 offen offset:1024 sc1\n"
      ".La2_go:\n"
      "ds_read_b64_tr_b16 v[224:225], v249 offset:0\n"
      "ds_read_b64_tr_b16 v[226:227], v249 offset:1024\n"
      "ds_read_b64_tr_b16 v[228:229], v248 offset:0\n"
      "ds_read_b64_tr_b16 v[230:231], v248 offset:1024\n"
      "ds_read_b64_tr_b16 v[232:233], v249 offset:2048\n"
      "ds_read_b64_tr_b16 v[234:235], v249 offset:3072\n"
      "ds_read_b64_tr_b16 v[236:237], v248 offset:2048\n"
      "ds_read_b64_tr_b16 v[238:239], v248 offset:3072\n"
      "v_lshlrev_b32 v192, 16, v18\n"
      "v_and_b32 v193, 0xffff0000, v18\n"
      "v_lshlrev_b32 v194, 16, v19\n"
      "v_and_b32 v195, 0xffff0000, v19\n"
      "v_lshlrev_b32 v196, 16, v20\n"
      "v_and_b32 v197, 0xffff0000, v20\n"
      "v_lshlrev_b32 v198, 16, v21\n"
      "v_and_b32 v199, 0xffff0000, v21\n"
      "v_lshlrev_b32 v200, 16, v22\n"
      "v_and_b32 v201, 0xffff0000, v22\n"
      "v_lshlrev_b32 v202, 16, v23\n"
      "v_and_b32 v203, 0xffff0000, v23\n"
      "v_lshlrev_b32 v204, 16, v24\n"
      "v_and_b32 v205, 0xffff0000, v24\n"
      "v_lshlrev_b32 v206, 16, v25\n"
      "v_and_b32 v207, 0xffff0000, v25\n"
      "s_waitcnt vmcnt(5)\n"
      "v_cmp_eq_u32 vcc, -1, v128\n"
      "s_nop 0\n"
      "s_cbranch_vccnz .Lpk_98\n"
      ".Lpr_99:\n"
      "s_waitcnt vmcnt(4) lgkmcnt(6)\n"
      "v_mfma_f32_32x32x16_bf16 v[208:223], v[224:227], v[128:131], 0\n"
      "buffer_load_dwordx4 v[152:155], v1, s[20:23], s80 offen offset:2048 sc1\n"
      "ds_read_b64_tr_b16 v[240:241], v249 offset:4096\n"
      "ds_read_b64_tr_b16 v[242:243], v249 offset:5120\n"
      "v_cmp_eq_u32 vcc, -1, v132\n"
      "s_waitcnt lgkmcnt(6)\n"
      "v_mfma_f32_32x32x16_bf16 v[192:207], v[228:231], v[128:131], v[192:207]\n"
      "s_cbranch_vccnz .Lpk_101\n"
      ".Lpr_102:\n"
      "buffer_load_dwordx4 v[156:159], v1, s[20:23], s80 offen offset:3072 sc1\n"
      "ds_read_b64_tr_b16 v[244:245], v248 offset:4096\n"
      "ds_read_b64_tr_b16 v[246:247], v248 offset:5120\n"
      "s_waitcnt vmcnt(5) lgkmcnt(6)\n"
      "v_mfma_f32_32x32x16_bf16 v[208:223], v[232:235], v[132:135], v[208:223]\n"
      "s_mov_b32 s80, s17\n"
      "buffer_load_dwordx4 v[160:163], v1, s[20:23], s80 offen sc1\n"
      "ds_read_b64_tr_b16 v[224:225], v249 offset:6144\n"
      "ds_read_b64_tr_b16 v[226:227], v249 offset:7168\n"
      "v_cmp_eq_u32 vcc, -1, v136\n"
      "s_waitcnt lgkmcnt(6)\n"
      "v_mfma_f32_32x32x16_bf16 v[192:207], v[236:239], v[132:135], v[192:207]\n"
      "s_cbranch_vccnz .Lpk_104\n"
      ".Lpr_105:\n"
      "buffer_load_dwordx4 v[164:167], v1, s[20:23], s80 offen offset:1024 sc1\n"
      "ds_read_b64_tr_b16 v[228:229], v248 offset:6144\n"
      "ds_read_b64_tr_b16 v[230:231], v248 offset:7168\n"
      "s_waitcnt vmcnt(6) lgkmcnt(6)\n"
      "v_mfma_f32_32x32x16_bf16 v[208:223], v[240:243], v[136:139], v[208:223]\n"
      "buffer_load_dwordx4 v[168:171], v1, s[20:23], s80 offen offset:2048 sc1\n"
      "ds_read_b64_tr_b16 v[232:233], v249 offset:8192\n"
      "ds_read_b64_tr_b16 v[234:235], v249 offset:9216\n"
      "v_cmp_eq_u32 vcc, -1, v140\n"
      "s_waitcnt lgkmcnt(6)\n"
      "v_mfma_f32_32x32x16_bf16 v[192:207], v[244:247], v[136:139], v[192:207]\n"
      "s_cbranch_vccnz .Lpk_107\n"
      ".Lpr_108:\n"
      "buffer_load_dwordx4 v[172:175], v1, s[20:23], s80 offen offset:3072 sc1\n"
      "ds_read_b64_tr_b16 v[236:237], v248 offset:8192\n"
      "ds_read_b64_tr_b16 v[238:239], v248 offset:9216\n"
      "s_waitcnt vmcnt(7) lgkmcnt(6)\n"
      "v_mfma_f32_32x32x16_bf16 v[208:223], v[224:227], v[140:143], v[208:223]\n"
      "s_add_u32 s80, s17, 0x1000\n"
      "buffer_load_dwordx4 v[176:179], v1, s[20:23], s80 offen sc1\n"
      "ds_read_b64_tr_b16 v[240:241], v249 offset:10240\n"
      "ds_read_b64_tr_b16 v[242:243], v249 offset:11264\n"
      "v_cmp_eq_u32 vcc, -1, v144\n"
      "s_waitcnt lgkmcnt(6)\n"
      "v_mfma_f32_32x32x16_bf16 v[192:207], v[228:231], v[140:143], v[192:207]\n"
      "s_cbranch_vccnz .Lpk_110\n"
      ".Lpr_111:\n"
      "buffer_load_dwordx4 v[180:183], v1, s[20:23], s80 offen offset:1024 sc1\n"
      "ds_read_b64_tr_b16 v[244:245], v248 offset:10240\n"
      "ds_read_b64_tr_b16 v[246:247], v248 offset:11264\n"
      "s_waitcnt vmcnt(8) lgkmcnt(6)\n"
      "v_mfma_f32_32x32x16_bf16 v[208:223], v[232:235], v[144:147], v[208:223]\n"
      "buffer_load_dwordx4 v[184:187], v1, s[20:23], s80 offen offset:2048 sc1\n"
      "ds_read_b64_tr_b16 v[224:225], v249 offset:12288\n"
      "ds_read_b64_tr_b16 v[226:227], v249 offset:13312\n"
      "v_cmp_eq_u32 vcc, -1, v148\n"
      "s_waitcnt lgkmcnt(6)\n"
      "v_mfma_f32_32x32x16_bf16 v[192:207], v[236:239], v[144:147], v[192:207]\n"
      "s_cbranch_vccnz .Lpk_113\n"
      ".Lpr_114:\n"
      "buffer_load_dwordx4 v[188:191], v1, s[20:23], s80 offen offset:3072 sc1\n"
      "ds_read_b64_tr_b16 v[228:229], v248 offset:12288\n"
      "ds_read_b64_tr_b16 v[230:231], v248 offset:13312\n"
      "s_waitcnt vmcnt(9) lgkmcnt(6)\n"
      "v_mfma_f32_32x32x16_bf16 v[208:223], v[240:243], v[148:151], v[208:223]\n"
      "ds_read_b64_tr_b16 v[232:233], v249 offset:14336\n"
      "ds_read_b64_tr_b16 v[234:235], v249 offset:15360\n"
      "v_cmp_eq_u32 vcc, -1, v152\n"
      "s_waitcnt lgkmcnt(6)\n"
      "v_mfma_f32_32x32x16_bf16 v[192:207], v[244:247], v[148:151], v[192:207]\n"
      "s_cbranch_vccnz .Lpk_116\n"
      ".Lpr_117:\n"
      "ds_read_b64_tr_b16 v[236:237], v248 offset:14336\n"
      "ds_read_b64_tr_b16 v[238:239], v248 offset:15360\n"
      "s_waitcnt vmcnt(8) lgkmcnt(6)\n"
      "v_mfma_f32_32x32x16_bf16 v[208:223], v[224:227], v[152:155], v[208:223]\n"
      "ds_read_b64_tr_b16 v[240:241], v253 offset:0\n"
      "ds_read_b64_tr_b16 v[242:243], v253 offset:1024\n"
      "v_cmp_eq_u32 vcc, -1, v156\n"
      "s_waitcnt lgkmcnt(6)\n"
      "v_mfma_f32_32x32x16_bf16 v[192:207], v[228:231], v[152:155], v[192:207]\n"
      "s_cbranch_vccnz .Lpk_119\n"
      ".Lpr_120:\n"
      "ds_read_b64_tr_b16 v[244:245], v253 offset:2048\n"
      "ds_read_b64_tr_b16 v[246:247], v253 offset:3072\n"
      "s_waitcnt vmcnt(7) lgkmcnt(6)\n"
      "v_mfma_f32_32x32x16_bf16 v[208:223], v[232:235], v[156:159], v[208:223]\n"
      "ds_read_b64_tr_b16 v[224:225], v253 offset:4096\n"
      "ds_read_b64_tr_b16 v[226:227], v253 offset:5120\n"
      "v_cmp_eq_u32 vcc, -1, v160\n"
      "s_waitcnt vmcnt(6) lgkmcnt(6)\n"
      "v_mfma_f32_32x32x16_bf16 v[192:207], v[236:239], v[156:159], v[192:207]\n"
      "s_cbranch_vccnz .Lpk_122\n"
      ".Lpr_123:\n"
      "ds_read_b64_tr_b16 v[228:229], v253 offset:6144\n"
      "ds_read_b64_tr_b16 v[230:231], v253 offset:7168\n"
      "v_cmp_eq_u32 vcc, -1, v164\n"
      "s_waitcnt vmcnt(5) lgkmcnt(6)\n"
      "v_mfma_f32_32x32x16_bf16 v[208:223], v[240:243], v[160:163], v[208:223]\n"
      "s_cbranch_vccnz .Lpk_125\n"
      ".Lpr_126:\n"
      "ds_read_b64_tr_b16 v[232:233], v253 offset:8192\n"
      "ds_read_b64_tr_b16 v[234:235], v253 offset:9216\n"
      "v_cmp_eq_u32 vcc, -1, v168\n"
      "s_waitcnt vmcnt(4) lgkmcnt(6)\n"
      "v_mfma_f32_32x32x16_bf16 v[208:223], v[244:247], v[164:167], v[208:223]\n"
      "s_cbranch_vccnz .Lpk_128\n"
      ".Lpr_129:\n"
      "ds_read_b64_tr_b16 v[236:237], v253 offset:10240\n"
      "ds_read_b64_tr_b16 v[238:239], v253 offset:11264\n"
      "v_cmp_eq_u32 vcc, -1, v172\n"
      "s_waitcnt vmcnt(3) lgkmcnt(6)\n"
      "v_mfma_f32_32x32x16_bf16 v[208:223], v[224:227], v[168:171], v[208:223]\n"
      "s_cbranch_vccnz .Lpk_131\n"
      ".Lpr_132:\n"
      "ds_read_b64_tr_b16 v[240:241], v253 offset:12288\n"
      "ds_read_b64_tr_b16 v[242:243], v253 offset:13312\n"
      "v_cmp_eq_u32 vcc, -1, v176\n"
      "s_waitcnt vmcnt(2) lgkmcnt(6)\n"
      "v_mfma_f32_32x32x16_bf16 v[208:223], v[228:231], v[172:175], v[208:223]\n"
      "s_cbranch_vccnz .Lpk_134\n"
      ".Lpr_135:\n"
      "ds_read_b64_tr_b16 v[244:245], v253 offset:14336\n"
      "ds_read_b64_tr_b16 v[246:247], v253 offset:15360\n"
      "v_cmp_eq_u32 vcc, -1, v180\n"
      "s_waitcnt vmcnt(1) lgkmcnt(6)\n"
      "v_mfma_f32_32x32x16_bf16 v[208:223], v[232:235], v[176:179], v[208:223]\n"
      "s_cbranch_vccnz .Lpk_137\n"
      ".Lpr_138:\n"
      "ds_read_b64_tr_b16 v[224:225], v252 offset:0\n"
      "ds_read_b64_tr_b16 v[226:227], v252 offset:1024\n"
      "v_cmp_eq_u32 vcc, -1, v184\n"
      "s_waitcnt vmcnt(0) lgkmcnt(6)\n"
      "v_mfma_f32_32x32x16_bf16 v[208:223], v[236:239], v[180:183], v[208:223]\n"
      "s_cbranch_vccnz .Lpk_140\n"
      ".Lpr_141:\n"
      "ds_read_b64_tr_b16 v[228:229], v252 offset:2048\n"
      "ds_read_b64_tr_b16 v[230:231], v252 offset:3072\n"
      "v_cmp_eq_u32 vcc, -1, v188\n"
      "s_waitcnt lgkmcnt(6)\n"
      "v_mfma_f32_32x32x16_bf16 v[208:223], v[240:243], v[184:187], v[208:223]\n"
      "s_cbranch_vccnz .Lpk_143\n"
      ".Lpr_144:\n"
      "ds_read_b64_tr_b16 v[232:233], v252 offset:4096\n"
      "ds_read_b64_tr_b16 v[234:235], v252 offset:5120\n"
      "s_waitcnt lgkmcnt(6)\n"
      "v_mfma_f32_32x32x16_bf16 v[208:223], v[244:247], v[188:191], v[208:223]\n"
      "ds_read_b64_tr_b16 v[236:237], v252 offset:6144\n"
      "ds_read_b64_tr_b16 v[238:239], v252 offset:7168\n"
      "s_waitcnt lgkmcnt(6)\n"
      "v_mfma_f32_32x32x16_bf16 v[192:207], v[224:227], v[160:163], v[192:207]\n"
      "ds_read_b64_tr_b16 v[240:241], v252 offset:8192\n"
      "ds_read_b64_tr_b16 v[242:243], v252 offset:9216\n"
      "s_waitcnt lgkmcnt(6)\n"
      "v_mfma_f32_32x32x16_bf16 v[192:207], v[228:231], v[164:167], v[192:207]\n"
      "ds_read_b64_tr_b16 v[244:245], v252 offset:10240\n"
      "ds_read_b64_tr_b16 v[246:247], v252 offset:11264\n"
      "s_nop 1\n"
      "ds_write_b128 v250, v[208:211] offset:0\n"
      "s_waitcnt lgkmcnt(7)\n"
      "v_mfma_f32_32x32x16_bf16 v[192:207], v[232:235], v[168:171], v[192:207]\n"
      "ds_read_b64_tr_b16 v[224:225], v252 offset:12288\n"
      "ds_read_b64_tr_b16 v[226:227], v252 offset:13312\n"
      "v_cmp_u_f32 vcc, v208, v208\n"
      "s_waitcnt lgkmcnt(7)\n"
      "v_mfma_f32_32x32x16_bf16 v[192:207], v[236:239], v[172:175], v[192:207]\n"
      "ds_read_b64_tr_b16 v[228:229], v252 offset:14336\n"
      "ds_read_b64_tr_b16 v[230:231], v252 offset:15360\n"
      "ds_write_b128 v250, v[212:215] offset:1024\n"
      "s_waitcnt lgkmcnt(8)\n"
      "v_mfma_f32_32x32x16_bf16 v[192:207], v[240:243], v[176:179], v[192:207]\n"
      "s_waitcnt lgkmcnt(6)\n"
      "v_mfma_f32_32x32x16_bf16 v[192:207], v[244:247], v[180:183], v[192:207]\n"
      "ds_write_b128 v250, v[216:219] offset:2048\n"
      "s_waitcnt lgkmcnt(4)\n"
      "v_mfma_f32_32x32x16_bf16 v[192:207], v[224:227], v[184:187], v[192:207]\n"
      "s_waitcnt lgkmcnt(2)\n"
      "v_mfma_f32_32x32x16_bf16 v[192:207], v[228:231], v[188:191], v[192:207]\n"
      "ds_write_b128 v250, v[220:223] offset:3072\n"
      "s_cbranch_vccnz .La2_nan\n"
      ".La2_ok:\n"
      "s_waitcnt lgkmcnt(0)\n"
      "s_barrier\n"
      "ds_read_b128 v[208:211], v251 offset:0\n"
      "ds_read_b128 v[212:215], v251 offset:1024\n"
      "ds_read_b128 v[216:219], v251 offset:2048\n"
      "ds_read_b128 v[220:223], v251 offset:3072\n"
      "v_mov_b32 v144, 0\n"
      "v_mov_b32 v145, 0\n"
      "s_waitcnt lgkmcnt(3)\n"
      "s_nop 0\n"
      "v_pk_add_f32 v[128:129], v[192:193], v[208:209]\n"
      "v_pk_add_f32 v[130:131], v[194:195], v[210:211]\n"
      "v_cvt_pk_bf16_f32 v18, v128, v129\n"
      "v_cvt_pk_bf16_f32 v19, v130, v131\n"
      "v_dot2c_f32_bf16 v144, s26, v18\n"
      "v_dot2c_f32_bf16 v145, v18, v18\n"
      "v_dot2c_f32_bf16 v144, s26, v19\n"
      "v_dot2c_f32_bf16 v145, v19, v19\n"
      "s_waitcnt lgkmcnt(2)\n"
      "v_pk_add_f32 v[132:133], v[196:197], v[212:213]\n"
      "v_pk_add_f32 v[134:135], v[198:199], v[214:215]\n"
      "v_cvt_pk_bf16_f32 v20, v132, v133\n"
      "v_cvt_pk_bf16_f32 v21, v134, v135\n"
      "v_dot2c_f32_bf16 v144, s26, v20\n"
      "v_dot2c_f32_bf16 v145, v20, v20\n"
      "v_dot2c_f32_bf16 v144, s26, v21\n"
      "v_dot2c_f32_bf16 v145, v21, v21\n"
      "s_waitcnt lgkmcnt(1)\n"
      "v_pk_add_f32 v[136:137], v[200:201], v[216:217]\n"
      "v_pk_add_f32 v[138:139], v[202:203], v[218:219]\n"
      "v_cvt_pk_bf16_f32 v22, v136, v137\n"
      "v_cvt_pk_bf16_f32 v23, v138, v139\n"
      "v_dot2c_f32_bf16 v144, s26, v22\n"
      "v_dot2c_f32_bf16 v145, v22, v22\n"
      "v_dot2c_f32_bf16 v144, s26, v23\n"
      "v_dot2c_f32_bf16 v145, v23, v23\n"
      "s_waitcnt lgkmcnt(0)\n"
      "v_pk_add_f32 v[140:141], v[204:205], v[220:221]\n"
      "v_pk_add_f32 v[142:143], v[206:207], v[222:223]\n"
      "v_cvt_pk_bf16_f32 v24, v140, v141\n"
      "v_cvt_pk_bf16_f32 v25, v142, v143\n"
      "v_dot2c_f32_bf16 v144, s26, v24\n"
      "v_dot2c_f32_bf16 v145, v24, v24\n"
      "v_dot2c_f32_bf16 v144, s26, v25\n"
      "v_dot2c_f32_bf16 v145, v25, v25\n"
      "s_nop 1\n"
      "v_mov_b32 v146, v144\n"
      "v_mov_b32 v147, v145\n"
      "s_nop 0\n"
      "v_permlane32_swap_b32 v144, v146\n"
      "v_permlane32_swap_b32 v145, v147\n"
      "v_pk_add_f32 v[144:145], v[144:145], v[146:147]\n"
      "v_min_u32 v144, -2, v144\n"
      "v_min_u32 v145, -2, v145\n"
      "s_mov_b32 exec_hi, 0\n"
      "buffer_store_dwordx2 v[144:145], v27, s[20:23], s60 offen sc1\n"
      "s_mov_b32 exec_hi, -1\n"
      "v_lshlrev_b32 v128, 16, v18\n"
      "v_and_b32 v129, 0xffff0000, v18\n"
      "v_lshlrev_b32 v130, 16, v19\n"
      "v_and_b32 v131, 0xffff0000, v19\n"
      "v_lshlrev_b32 v132, 16, v20\n"
      "v_and_b32 v133, 0xffff0000, v20\n"
      "v_lshlrev_b32 v134, 16, v21\n"
      "v_and_b32 v135, 0xffff0000, v21\n"
      "v_lshlrev_b32 v136, 16, v22\n"
      "v_and_b32 v137, 0xffff0000, v22\n"
      "v_lshlrev_b32 v138, 16, v23\n"
      "v_and_b32 v139, 0xffff0000, v23\n"
      "v_lshlrev_b32 v140, 16, v24\n"
      "v_and_b32 v141, 0xffff0000, v24\n"
      "v_lshlrev_b32 v142, 16, v25\n"
      "v_and_b32 v143, 0xffff0000, v25\n"
      "s_add_u32 s81, s76, 0x20000\n"
      "v_and_b32 v148, 31, v0\n"
      "v_mul_u32_u24 v148, 0x48, v148\n"
      "v_lshrrev_b32 v152, 5, v0\n"
      "v_lshl_add_u32 v148, v152, 3, v148\n"
      "v_add_u32 v148, s81, v148\n"
      "v_bfe_u32 v149, v0, 2, 3\n"
      "v_lshl_add_u32 v149, v152, 4, v149\n"
      "v_mul_u32_u24 v149, 0x48, v149\n"
      "v_and_b32 v152, 3, v0\n"
      "v_lshl_add_u32 v149, v152, 3, v149\n"
      "v_add_u32 v149, s81, v149\n"
      "s_add_u32 s83, s61, 0x20000\n"
      "v_mov_b32 v172, 0\n"
      "v_mov_b32 v173, 0\n"
      "v_mov_b32 v174, 0\n"
      "v_mov_b32 v175, 0\n"
      "s_cmp_lg_u32 s53, 0\n"
      "s_cbranch_scc1 .Lbg_151\n"
      "ds_read_b64_tr_b16 v[64:65], v2 offset:0\n"
      "ds_read_b64_tr_b16 v[66:67], v2 offset:1024\n"
      "ds_read_b64_tr_b16 v[68:69], v3 offset:0\n"
      "ds_read_b64_tr_b16 v[70:71], v3 offset:1024\n"
      "ds_read_b64_tr_b16 v[72:73], v2 offset:2048\n"
      "ds_read_b64_tr_b16 v[74:75], v2 offset:3072\n"
      "ds_read_b64_tr_b16 v[76:77], v3 offset:2048\n"
      "ds_read_b64_tr_b16 v[78:79], v3 offset:3072\n"
      "ds_read_b64_tr_b16 v[80:81], v2 offset:16384\n"
      "ds_read_b64_tr_b16 v[82:83], v2 offset:17408\n"
      "ds_read_b64_tr_b16 v[84:85], v3 offset:16384\n"
      "ds_read_b64_tr_b16 v[86:87], v3 offset:17408\n"
      "ds_read_b64_tr_b16 v[88:89], v2 offset:18432\n"
      "ds_read_b64_tr_b16 v[90:91], v2 offset:19456\n"
      "ds_read_b64_tr_b16 v[92:93], v3 offset:18432\n"
      "ds_read_b64_tr_b16 v[94:95], v3 offset:19456\n"
      "ds_read_b64_tr_b16 v[96:97], v4 offset:32768\n"
      "ds_read_b64_tr_b16 v[98:99], v4 offset:33792\n"
      "ds_read_b64_tr_b16 v[100:101], v5 offset:32768\n"
      "ds_read_b64_tr_b16 v[102:103], v5 offset:33792\n"
      "ds_read_b64_tr_b16 v[104:105], v4 offset:34816\n"
      "ds_read_b64_tr_b16 v[106:107], v4 offset:35840\n"
      "ds_read_b64_tr_b16 v[108:109], v5 offset:34816\n"
      "ds_read_b64_tr_b16 v[110:111], v5 offset:35840\n"
      "ds_read_b64_tr_b16 v[112:113], v4 offset:49152\n"
      "ds_read_b64_tr_b16 v[114:115], v4 offset:50176\n"
      "ds_read_b64_tr_b16 v[116:117], v5 offset:49152\n"
      "ds_read_b64_tr_b16 v[118:119], v5 offset:50176\n"
      "ds_read_b64_tr_b16 v[120:121], v4 offset:51200\n"
      "ds_read_b64_tr_b16 v[122:123], v4 offset:52224\n"
      "ds_read_b64_tr_b16 v[124:125], v5 offset:51200\n"
      "ds_read_b64_tr_b16 v[126:127], v5 offset:52224\n"
      "s_waitcnt lgkmcnt(0)\n"
      "s_mul_i32 s78, s19, 64\n"
      "s_add_u32 s78, s78, s75\n"
      "s_mov_b32 m0, s76\n"
      "s_nop 0\n"
      "buffer_load_dwordx4 v6, s[40:43], s78 offen lds\n"
      "s_add_u32 s78, s78, 0x7c00\n"
      "buffer_load_dwordx4 v6, s[40:43], s78 offen offset:1024 lds\n"
      "s_add_u32 s78, s78, 0x7c00\n"
      "buffer_load_dwordx4 v6, s[40:43], s78 offen offset:2048 lds\n"
      "s_add_u32 s78, s78, 0x7c00\n"
      "buffer_load_dwordx4 v6, s[40:43], s78 offen offset:3072 lds\n"
      "s_mul_i32 s78, s13, 64\n"
      "s_add_u32 s78, s78, s75\n"
      "s_add_u32 m0, s76, 0x4000\n"
      "s_add_u32 s78, s78, 0x20000\n"
      "buffer_load_dwordx4 v6, s[40:43], s78 offen lds\n"
      "s_add_u32 s78, s78, 0x7c00\n"
      "buffer_load_dwordx4 v6, s[40:43], s78 offen offset:1024 lds\n"
      "s_add_u32 s78, s78, 0x7c00\n"
      "buffer_load_dwordx4 v6, s[40:43], s78 offen offset:2048 lds\n"
      "s_add_u32 s78, s78, 0x7c00\n"
      "buffer_load_dwordx4 v6, s[40:43], s78 offen offset:3072 lds\n"
      "s_mul_i32 s78, s12, 64\n"
      "s_add_u32 s78, s78, s75\n"
      "s_add_u32 m0, s76, 0x8000\n"
      "s_add_u32 s78, s78, 0x20000\n"
      "buffer_load_dwordx4 v6, s[40:43], s78 offen lds\n"
      "s_add_u32 s78, s78, 0x7c00\n"
      "buffer_load_dwordx4 v6, s[40:43], s78 offen offset:1024 lds\n"
      "s_add_u32 s78, s78, 0x7c00\n"
      "buffer_load_dwordx4 v6, s[40:43], s78 offen offset:2048 lds\n"
      "s_add_u32 s78, s78, 0x7c00\n"
      "buffer_load_dwordx4 v6, s[40:43], s78 offen offset:3072 lds\n"
      "s_waitcnt vmcnt(12)\n"
      "s_branch .Lbg_152\n"
      ".Lbg_151:\n"
      "s_mul_i32 s78, s13, 64\n"
      "s_add_u32 s78, s78, s75\n"
      "s_add_u32 m0, s76, 0x4000\n"
      "s_add_u32 s78, s78, 0x20000\n"
      "buffer_load_dwordx4 v6, s[40:43], s78 offen lds\n"
      "s_add_u32 s78, s78, 0x7c00\n"
      "buffer_load_dwordx4 v6, s[40:43], s78 offen offset:1024 lds\n"
      "s_add_u32 s78, s78, 0x7c00\n"
      "buffer_load_dwordx4 v6, s[40:43], s78 offen offset:2048 lds\n"
      "s_add_u32 s78, s78, 0x7c00\n"
      "buffer_load_dwordx4 v6, s[40:43], s78 offen offset:3072 lds\n"
      "s_sleep 7\n"
      "s_waitcnt vmcnt(4)\n"
      ".Lbg_152:\n"
      "buffer_load_dwordx4 v[160:163], v1, s[20:23], s67 offen sc1\n"
      "buffer_load_dwordx4 v[164:167], v1, s[20:23], s67 offen offset:1024 sc1\n"
      "buffer_load_dwordx4 v[168:171], v1, s[20:23], s67 offen offset:2048 sc1\n"
      "s_mov_b32 exec_hi, 0\n"
      "buffer_load_dwordx4 v[172:175], v1, s[20:23], s67 offen offset:3072 sc1\n"
      "s_mov_b32 exec_hi, -1\n"
      "s_waitcnt vmcnt(2)\n"
      "v_pk_add_f32 v[160:161], v[160:161], v[164:165]\n"
      "v_pk_add_f32 v[162:163], v[162:163], v[166:167]\n"
      "s_waitcnt vmcnt(0)\n"
      "v_pk_add_f32 v[168:169], v[168:169], v[172:173]\n"
      "v_pk_add_f32 v[170:171], v[170:171], v[174:175]\n"
      "v_pk_add_f32 v[160:161], v[160:161], v[168:169]\n"
      "v_pk_add_f32 v[162:163], v[162:163], v[170:171]\n"
      "s_nop 1\n"
      "v_permlane16_swap_b32 v160, v162\n"
      "v_permlane16_swap_b32 v161, v163\n"
      "v_pk_add_f32 v[144:145], v[160:161], v[162:163]\n"
      "v_mov_b32 v146, v144\n"
      "v_mov_b32 v147, v145\n"
      "s_nop 0\n"
      "v_permlane32_swap_b32 v144, v146\n"
      "v_permlane32_swap_b32 v145, v147\n"
      "v_pk_add_f32 v[144:145], v[144:145], v[146:147]\n"
      "v_cmp_u_f32 vcc, v144, v145\n"
      "v_mul_f32 v144, 0xbb124925, v144\n"
      "v_mul_f32 v145, 0x3b124925, v145\n"
      "s_cbranch_vccnz .Lst_bad\n"
      ".Lst_ok:\n"
      "v_fma_f32 v145, v144, -v144, v145\n"
      "v_max_f32 v145, 0, v145\n"
      "v_add_f32 v146, 0x3727c5ac, v145\n"
      "v_fmamk_f32 v145, v146, 0x3727c5ac, v145\n"
      "v_rsq_f32 v145, v145\n"
      "s_nop 0\n"
      "v_mul_f32 v144, v144, v145\n"
      "v_pk_fma_f32 v[128:129], v[128:129], v[144:145], v[144:145] op_sel:[0,1,0] op_sel_hi:[1,1,0]\n"
      "v_cvt_pk_bf16_f32 v192, v128, v129\n"
      "v_pk_fma_f32 v[130:131], v[130:131], v[144:145], v[144:145] op_sel:[0,1,0] op_sel_hi:[1,1,0]\n"
      "v_cvt_pk_bf16_f32 v193, v130, v131\n"
      "ds_write_b64 v148, v[192:193] offset:0\n"
      "v_pk_fma_f32 v[132:133], v[132:133], v[144:145], v[144:145] op_sel:[0,1,0] op_sel_hi:[1,1,0]\n"
      "v_cvt_pk_bf16_f32 v194, v132, v133\n"
      "v_pk_fma_f32 v[134:135], v[134:135], v[144:145], v[144:145] op_sel:[0,1,0] op_sel_hi:[1,1,0]\n"
      "v_cvt_pk_bf16_f32 v195, v134, v135\n"
      "ds_write_b64 v148, v[194:195] offset:16\n"
      "ds_read_b64_tr_b16 v[200:201], v149 offset:0\n"
      "ds_read_b64_tr_b16 v[202:203], v149 offset:576\n"
      "v_pk_fma_f32 v[136:137], v[136:137], v[144:145], v[144:145] op_sel:[0,1,0] op_sel_hi:[1,1,0]\n"
      "v_cvt_pk_bf16_f32 v196, v136, v137\n"
      "v_pk_fma_f32 v[138:139], v[138:139], v[144:145], v[144:145] op_sel:[0,1,0] op_sel_hi:[1,1,0]\n"
      "v_cvt_pk_bf16_f32 v197, v138, v139\n"
      "ds_write_b64 v148, v[196:197] offset:32\n"
      "v_pk_fma_f32 v[140:141], v[140:141], v[144:145], v[144:145] op_sel:[0,1,0] op_sel_hi:[1,1,0]\n"
      "v_cvt_pk_bf16_f32 v198, v140, v141\n"
      "v_pk_fma_f32 v[142:143], v[142:143], v[144:145], v[144:145] op_sel:[0,1,0] op_sel_hi:[1,1,0]\n"
      "v_cvt_pk_bf16_f32 v199, v142, v143\n"
      "ds_write_b64 v148, v[198:199] offset:48\n"
      "ds_read_b64_tr_b16 v[204:205], v149 offset:32\n"
      "ds_read_b64_tr_b16 v[206:207], v149 offset:608\n"
      "s_waitcnt lgkmcnt(5)\n"
      "v_min_u32 v200, -2, v200\n"
      "v_min_u32 v201, -2, v201\n"
      "s_waitcnt lgkmcnt(4)\n"
      "v_min_u32 v202, -2, v202\n"
      "v_min_u32 v203, -2, v203\n"
      "buffer_store_dwordx4 v[200:203], v29, s[20:23], s61 offen sc1\n"
      "s_waitcnt lgkmcnt(1)\n"
      "v_min_u32 v204, -2, v204\n"
      "v_min_u32 v205, -2, v205\n"
      "s_waitcnt lgkmcnt(0)\n"
      "v_min_u32 v206, -2, v206\n"
      "v_min_u32 v207, -2, v207\n"
      "buffer_store_dwordx4 v[204:207], v29, s[20:23], s83 offen sc1\n"
      "s_lshl_b32 s81, s52, 6\n"
      "s_add_u32 s81, s81, 0x27280\n"
      "v_and_b32 v168, 32, v0\n"
      "v_add_u32 v168, s81, v168\n"
      "ds_read_b128 v[160:163], v168\n"
      "ds_read_b128 v[164:167], v168 offset:16\n"
      "v_mov_b32 v168, 0\n"
      "v_mov_b32 v169, 0\n"
      "v_mov_b32 v170, 0\n"
      "v_mov_b32 v171, 0\n"
      "v_mov_b32 v172, 0\n"
      "v_mov_b32 v173, 0\n"
      "v_mov_b32 v174, 0\n"
      "v_mov_b32 v175, 0\n"
      "s_add_u32 s86, s68, 0x20000\n"
      "s_barrier\n"
      "s_cmp_lg_u32 s53, 0\n"
      "s_cbranch_scc1 .Lbg_153\n"
      "s_mul_i32 s78, s10, 64\n"
      "s_add_u32 s78, s78, s75\n"
      "s_add_u32 m0, s76, 0xc000\n"
      "s_nop 0\n"
      "buffer_load_dwordx4 v6, s[40:43], s78 offen lds\n"
      "s_add_u32 s78, s78, 0x7c00\n"
      "buffer_load_dwordx4 v6, s[40:43], s78 offen offset:1024 lds\n"
      "s_add_u32 s78, s78, 0x7c00\n"
      "buffer_load_dwordx4 v6, s[40:43], s78 offen offset:2048 lds\n"
      "s_add_u32 s78, s78, 0x7c00\n"
      "buffer_load_dwordx4 v6, s[40:43], s78 offen offset:3072 lds\n"
      "s_mul_i32 s78, s99, 64\n"
      "s_add_u32 s78, s78, s75\n"
      "s_add_u32 m0, s76, 0x18000\n"
      "s_nop 0\n"
      "buffer_load_dwordx4 v6, s[40:43], s78 offen lds\n"
      "s_add_u32 s78, s78, 0x7c00\n"
      "buffer_load_dwordx4 v6, s[40:43], s78 offen offset:1024 lds\n"
      "s_add_u32 s78, s78, 0x7c00\n"
      "buffer_load_dwordx4 v6, s[40:43], s78 offen offset:2048 lds\n"
      "s_add_u32 s78, s78, 0x7c00\n"
      "buffer_load_dwordx4 v6, s[40:43], s78 offen offset:3072 lds\n"
      "s_mul_i32 s78, s16, 64\n"
      "s_add_u32 s78, s78, s75\n"
      "s_add_u32 m0, s76, 0x1c000\n"
      "s_nop 0\n"
      "buffer_load_dwordx4 v6, s[40:43], s78 offen lds\n"
      "s_add_u32 s78, s78, 0x7c00\n"
      "buffer_load_dwordx4 v6, s[40:43], s78 offen offset:1024 lds\n"
      "s_add_u32 s78, s78, 0x7c00\n"
      "buffer_load_dwordx4 v6, s[40:43], s78 offen offset:2048 lds\n"
      "s_add_u32 s78, s78, 0x7c00\n"
      "buffer_load_dwordx4 v6, s[40:43], s78 offen offset:3072 lds\n"
      "s_waitcnt vmcnt(12)\n"
      "s_branch .Lbg_154\n"
      ".Lbg_153:\n"
      "s_mul_i32 s78, s12, 64\n"
      "s_add_u32 s78, s78, s75\n"
      "s_add_u32 m0, s76, 0x8000\n"
      "s_add_u32 s78, s78, 0x20000\n"
      "buffer_load_dwordx4 v6, s[40:43], s78 offen lds\n"
      "s_add_u32 s78, s78, 0x7c00\n"
      "buffer_load_dwordx4 v6, s[40:43], s78 offen offset:1024 lds\n"
      "s_add_u32 s78, s78, 0x7c00\n"
      "buffer_load_dwordx4 v6, s[40:43], s78 offen offset:2048 lds\n"
      "s_add_u32 s78, s78, 0x7c00\n"
      "buffer_load_dwordx4 v6, s[40:43], s78 offen offset:3072 lds\n"
      "s_mul_i32 s78, s10, 64\n"
      "s_add_u32 s78, s78, s75\n"
      "s_add_u32 m0, s76, 0xc000\n"
      "s_nop 0\n"
      "buffer_load_dwordx4 v6, s[40:43], s78 offen lds\n"
      "s_add_u32 s78, s78, 0x7c00\n"
      "buffer_load_dwordx4 v6, s[40:43], s78 offen offset:1024 lds\n"
      "s_add_u32 s78, s78, 0x7c00\n"
      "buffer_load_dwordx4 v6, s[40:43], s78 offen offset:2048 lds\n"
      "s_add_u32 s78, s78, 0x7c00\n"
      "buffer_load_dwordx4 v6, s[40:43], s78 offen offset:3072 lds\n"
      "s_sleep 15\n"
      "s_waitcnt vmcnt(8)\n"
      ".Lbg_154:\n"
      "s_cmp_eq_u32 s55, 0\n"
      "s_cbranch_scc1 .Lb1_skip\n"
      "s_add_u32 s80, s68, s2\n"
      "buffer_load_dwordx4 v[144:147], v1, s[20:23], s80 offen sc1\n"
      "buffer_load_dwordx4 v[148:151], v1, s[20:23], s80 offen offset:1024 sc1\n"
      "buffer_load_dwordx4 v[152:155], v1, s[20:23], s80 offen offset:2048 sc1\n"
      "buffer_load_dwordx4 v[156:159], v1, s[20:23], s80 offen offset:3072 sc1\n"
      "s_waitcnt vmcnt(3)\n"
      "v_cmp_eq_u32 vcc, -1, v144\n"
      "s_nop 0\n"
      "s_cbranch_vccnz .Lpk_155\n"
      ".Lpr_156:\n"
      "s_waitcnt vmcnt(2)\n"
      "v_mfma_f32_32x32x16_bf16 v[176:191], a[4:7], v[144:147], 0\n"
      "s_add_u32 s80, s68, s3\n"
      "buffer_load_dwordx4 v[12:15], v1, s[20:23], s80 offen sc1\n"
      "v_cmp_eq_u32 vcc, -1, v148\n"
      "s_waitcnt lgkmcnt(0)\n"
      "v_mfma_f32_32x32x16_bf16 v[160:175], a[0:3], v[144:147], v[160:175]\n"
      "s_cbranch_vccnz .Lpk_158\n"
      ".Lpr_159:\n"
      "buffer_load_dwordx4 v[244:247], v1, s[20:23], s80 offen offset:1024 sc1\n"
      "s_waitcnt vmcnt(3)\n"
      "v_mfma_f32_32x32x16_bf16 v[176:191], a[12:15], v[148:151], v[176:191]\n"
      "buffer_load_dwordx4 v[248:251], v1, s[20:23], s80 offen offset:2048 sc1\n"
      "v_cmp_eq_u32 vcc, -1, v152\n"
      "v_mfma_f32_32x32x16_bf16 v[160:175], a[8:11], v[148:151], v[160:175]\n"
      "s_cbranch_vccnz .Lpk_161\n"
      ".Lpr_162:\n"
      "buffer_load_dwordx4 v[252:255], v1, s[20:23], s80 offen offset:3072 sc1\n"
      "s_waitcnt vmcnt(4)\n"
      "v_mfma_f32_32x32x16_bf16 v[176:191], a[20:23], v[152:155], v[176:191]\n"
      "s_add_u32 s80, s68, s4\n"
      "buffer_load_dwordx4 v[224:227], v1, s[20:23], s80 offen sc1\n"
      "s_lshl_b32 s81, s52, 6\n"
      "s_add_u32 s81, s81, 0x27280\n"
      "v_and_b32 v200, 32, v0\n"
      "v_cmp_eq_u32 vcc, -1, v156\n"
      "v_mfma_f32_32x32x16_bf16 v[160:175], a[16:19], v[152:155], v[160:175]\n"
      "s_cbranch_vccnz .Lpk_164\n"
      ".Lpr_165:\n"
      "buffer_load_dwordx4 v[228:231], v1, s[20:23], s80 offen offset:1024 sc1\n"
      "v_add_u32 v200, s81, v200\n"
      "ds_read_b128 v[192:195], v200\n"
      "ds_read_b128 v[196:199], v200 offset:16\n"
      "s_waitcnt vmcnt(5)\n"
      "v_mfma_f32_32x32x16_bf16 v[176:191], a[28:31], v[156:159], v[176:191]\n"
      "buffer_load_dwordx4 v[232:235], v1, s[20:23], s80 offen offset:2048 sc1\n"
      "v_mov_b32 v200, 0\n"
      "v_mov_b32 v201, 0\n"
      "v_mov_b32 v202, 0\n"
      "v_mov_b32 v203, 0\n"
      "v_cmp_eq_u32 vcc, -1, v12\n"
      "v_mfma_f32_32x32x16_bf16 v[160:175], a[24:27], v[156:159], v[160:175]\n"
      "s_cbranch_vccnz .Lpk_167\n"
      ".Lpr_168:\n"
      "buffer_load_dwordx4 v[236:239], v1, s[20:23], s80 offen offset:3072 sc1\n"
      "v_mov_b32 v204, 0\n"
      "v_mov_b32 v205, 0\n"
      "v_mov_b32 v206, 0\n"
      "s_waitcnt vmcnt(6)\n"
      "v_mfma_f32_32x32x16_bf16 v[176:191], a[36:39], v[12:15], v[176:191]\n"
      "s_add_u32 s80, s68, s5\n"
      "buffer_load_dwordx4 v[240:243], v1, s[20:23], s80 offen sc1\n"
      "v_mov_b32 v207, 0\n"
      "v_cmp_eq_u32 vcc, -1, v244\n"
      "v_mfma_f32_32x32x16_bf16 v[160:175], a[32:35], v[12:15], v[160:175]\n"
      "s_cbranch_vccnz .Lpk_170\n"
      ".Lpr_171:\n"
      "buffer_load_dwordx4 v[128:131], v1, s[20:23], s80 offen offset:1024 sc1\n"
      "s_waitcnt vmcnt(7)\n"
      "v_mfma_f32_32x32x16_bf16 v[176:191], a[44:47], v[244:247], v[176:191]\n"
      "buffer_load_dwordx4 v[132:135], v1, s[20:23], s80 offen offset:2048 sc1\n"
      "v_cmp_eq_u32 vcc, -1, v248\n"
      "v_mfma_f32_32x32x16_bf16 v[160:175], a[40:43], v[244:247], v[160:175]\n"
      "s_cbranch_vccnz .Lpk_173\n"
      ".Lpr_174:\n"
      "buffer_load_dwordx4 v[136:139], v1, s[20:23], s80 offen offset:3072 sc1\n"
      "s_waitcnt vmcnt(8)\n"
      "v_mfma_f32_32x32x16_bf16 v[176:191], a[52:55], v[248:251], v[176:191]\n"
      "s_add_u32 s80, s68, s6\n"
      "buffer_load_dwordx4 v[140:143], v1, s[20:23], s80 offen sc1\n"
      "v_cmp_eq_u32 vcc, -1, v252\n"
      "v_mfma_f32_32x32x16_bf16 v[160:175], a[48:51], v[248:251], v[160:175]\n"
      "s_cbranch_vccnz .Lpk_176\n"
      ".Lpr_177:\n"
      "buffer_load_dwordx4 v[144:147], v1, s[20:23], s80 offen offset:1024 sc1\n"
      "s_waitcnt vmcnt(9)\n"
      "v_mfma_f32_32x32x16_bf16 v[176:191], a[60:63], v[252:255], v[176:191]\n"
      "buffer_load_dwordx4 v[148:151], v1, s[20:23], s80 offen offset:2048 sc1\n"
      "v_cmp_eq_u32 vcc, -1, v224\n"
      "v_mfma_f32_32x32x16_bf16 v[160:175], a[56:59], v[252:255], v[160:175]\n"
      "s_cbranch_vccnz .Lpk_179\n"
      ".Lpr_180:\n"
      "buffer_load_dwordx4 v[152:155], v1, s[20:23], s80 offen offset:3072 sc1\n"
      "s_waitcnt vmcnt(10)\n"
      "v_mfma_f32_32x32x16_bf16 v[176:191], a[68:71], v[224:227], v[176:191]\n"
      "s_add_u32 s80, s68, s7\n"
      "buffer_load_dwordx4 v[156:159], v1, s[20:23], s80 offen sc1\n"
      "v_cmp_eq_u32 vcc, -1, v228\n"
      "v_mfma_f32_32x32x16_bf16 v[160:175], a[64:67], v[224:227], v[160:175]\n"
      "s_cbranch_vccnz .Lpk_182\n"
      ".Lpr_183:\n"
      "buffer_load_dwordx4 v[12:15], v1, s[20:23], s80 offen offset:1024 sc1\n"
      "s_waitcnt vmcnt(11)\n"
      "v_mfma_f32_32x32x16_bf16 v[176:191], a[76:79], v[228:231], v[176:191]\n"
      "buffer_load_dwordx4 v[244:247], v1, s[20:23], s80 offen offset:2048 sc1\n"
      "v_cmp_eq_u32 vcc, -1, v232\n"
      "v_mfma_f32_32x32x16_bf16 v[160:175], a[72:75], v[228:231], v[160:175]\n"
      "s_cbranch_vccnz .Lpk_185\n"
      ".Lpr_186:\n"
      "buffer_load_dwordx4 v[248:251], v1, s[20:23], s80 offen offset:3072 sc1\n"
      "s_waitcnt vmcnt(12)\n"
      "v_mfma_f32_32x32x16_bf16 v[176:191], a[84:87], v[232:235], v[176:191]\n"
      "v_cmp_eq_u32 vcc, -1, v236\n"
      "v_mfma_f32_32x32x16_bf16 v[160:175], a[80:83], v[232:235], v[160:175]\n"
      "s_cbranch_vccnz .Lpk_188\n"
      ".Lpr_189:\n"
      "s_add_u32 s80, s68, s8\n"
      "buffer_load_dwordx4 v[252:255], v1, s[20:23], s80 offen sc1\n"
      "s_waitcnt vmcnt(12)\n"
      "v_mfma_f32_32x32x16_bf16 v[176:191], a[92:95], v[236:239], v[176:191]\n"
      "v_cmp_eq_u32 vcc, -1, v240\n"
      "v_mfma_f32_32x32x16_bf16 v[160:175], a[88:91], v[236:239], v[160:175]\n"
      "s_cbranch_vccnz .Lpk_191\n"
      ".Lpr_192:\n"
      "buffer_load_dwordx4 v[224:227], v1, s[20:23], s80 offen offset:1024 sc1\n"
      "s_waitcnt vmcnt(12)\n"
      "v_mfma_f32_32x32x16_bf16 v[176:191], a[100:103], v[240:243], v[176:191]\n"
      "v_cmp_eq_u32 vcc, -1, v128\n"
      "v_mfma_f32_32x32x16_bf16 v[160:175], a[96:99], v[240:243], v[160:175]\n"
      "s_cbranch_vccnz .Lpk_194\n"
      ".Lpr_195:\n"
      "buffer_load_dwordx4 v[228:231], v1, s[20:23], s80 offen offset:2048 sc1\n"
      "s_waitcnt vmcnt(12)\n"
      "v_mfma_f32_32x32x16_bf16 v[176:191], a[108:111], v[128:131], v[176:191]\n"
      "v_cmp_eq_u32 vcc, -1, v132\n"
      "v_mfma_f32_32x32x16_bf16 v[160:175], a[104:107], v[128:131], v[160:175]\n"
      "s_cbranch_vccnz .Lpk_197\n"
      ".Lpr_198:\n"
      "buffer_load_dwordx4 v[232:235], v1, s[20:23], s80 offen offset:3072 sc1\n"
      "s_waitcnt vmcnt(12)\n"
      "v_mfma_f32_32x32x16_bf16 v[176:191], a[116:119], v[132:135], v[176:191]\n"
      "v_cmp_eq_u32 vcc, -1, v136\n"
      "v_mfma_f32_32x32x16_bf16 v[160:175], a[112:115], v[132:135], v[160:175]\n"
      "s_cbranch_vccnz .Lpk_200\n"
      ".Lpr_201:\n"
      "s_add_u32 s80, s68, s9\n"
      "buffer_load_dwordx4 v[236:239], v1, s[20:23], s80 offen sc1\n"
      "s_waitcnt vmcnt(12)\n"
      "v_mfma_f32_32x32x16_bf16 v[176:191], a[124:127], v[136:139], v[176:191]\n"
      "v_cmp_eq_u32 vcc, -1, v140\n"
      "v_mfma_f32_32x32x16_bf16 v[160:175], a[120:123], v[136:139], v[160:175]\n"
      "s_cbranch_vccnz .Lpk_203\n"
      ".Lpr_204:\n"
      "buffer_load_dwordx4 v[240:243], v1, s[20:23], s80 offen offset:1024 sc1\n"
      "s_waitcnt vmcnt(12)\n"
      "v_mfma_f32_32x32x16_bf16 v[176:191], a[132:135], v[140:143], v[176:191]\n"
      "v_cmp_eq_u32 vcc, -1, v144\n"
      "v_mfma_f32_32x32x16_bf16 v[160:175], a[128:131], v[140:143], v[160:175]\n"
      "s_cbranch_vccnz .Lpk_206\n"
      ".Lpr_207:\n"
      "buffer_load_dwordx4 v[128:131], v1, s[20:23], s80 offen offset:2048 sc1\n"
      "s_waitcnt vmcnt(12)\n"
      "v_mfma_f32_32x32x16_bf16 v[176:191], a[140:143], v[144:147], v[176:191]\n"
      "v_cmp_eq_u32 vcc, -1, v148\n"
      "v_mfma_f32_32x32x16_bf16 v[160:175], a[136:139], v[144:147], v[160:175]\n"
      "s_cbranch_vccnz .Lpk_209\n"
      ".Lpr_210:\n"
      "buffer_load_dwordx4 v[132:135], v1, s[20:23], s80 offen offset:3072 sc1\n"
      "s_waitcnt vmcnt(12)\n"
      "v_mfma_f32_32x32x16_bf16 v[176:191], a[148:151], v[148:151], v[176:191]\n"
      "v_cmp_eq_u32 vcc, -1, v152\n"
      "v_mfma_f32_32x32x16_bf16 v[160:175], a[144:147], v[148:151], v[160:175]\n"
      "s_cbranch_vccnz .Lpk_212\n"
      ".Lpr_213:\n"
      "s_add_u32 s80, s86, s2\n"
      "buffer_load_dwordx4 v[136:139], v1, s[20:23], s80 offen sc1\n"
      "s_waitcnt vmcnt(12)\n"
      "v_mfma_f32_32x32x16_bf16 v[176:191], a[156:159], v[152:155], v[176:191]\n"
      "v_cmp_eq_u32 vcc, -1, v156\n"
      "v_mfma_f32_32x32x16_bf16 v[160:175], a[152:155], v[152:155], v[160:175]\n"
      "s_cbranch_vccnz .Lpk_215\n"
      ".Lpr_216:\n"
      "buffer_load_dwordx4 v[140:143], v1, s[20:23], s80 offen offset:1024 sc1\n"
      "s_waitcnt vmcnt(12)\n"
      "v_mfma_f32_32x32x16_bf16 v[176:191], a[164:167], v[156:159], v[176:191]\n"
      "v_cmp_eq_u32 vcc, -1, v12\n"
      "v_mfma_f32_32x32x16_bf16 v[160:175], a[160:163], v[156:159], v[160:175]\n"
      "s_cbranch_vccnz .Lpk_218\n"
      ".Lpr_219:\n"
      "buffer_load_dwordx4 v[144:147], v1, s[20:23], s80 offen offset:2048 sc1\n"
      "s_waitcnt vmcnt(12)\n"
      "v_mfma_f32_32x32x16_bf16 v[176:191], a[172:175], v[12:15], v[176:191]\n"
      "v_cmp_eq_u32 vcc, -1, v244\n"
      "v_mfma_f32_32x32x16_bf16 v[160:175], a[168:171], v[12:15], v[160:175]\n"
      "s_cbranch_vccnz .Lpk_221\n"
      ".Lpr_222:\n"
      "buffer_load_dwordx4 v[148:151], v1, s[20:23], s80 offen offset:3072 sc1\n"
      "s_waitcnt vmcnt(12)\n"
      "v_mfma_f32_32x32x16_bf16 v[176:191], a[180:183], v[244:247], v[176:191]\n"
      "v_cmp_eq_u32 vcc, -1, v248\n"
      "v_mfma_f32_32x32x16_bf16 v[160:175], a[176:179], v[244:247], v[160:175]\n"
      "s_cbranch_vccnz .Lpk_224\n"
      ".Lpr_225:\n"
      "s_add_u32 s80, s86, s3\n"
      "buffer_load_dwordx4 v[152:155], v1, s[20:23], s80 offen sc1\n"
      "s_waitcnt vmcnt(12)\n"
      "v_mfma_f32_32x32x16_bf16 v[176:191], a[188:191], v[248:251], v[176:191]\n"
      "v_cmp_eq_u32 vcc, -1, v252\n"
      "v_mfma_f32_32x32x16_bf16 v[160:175], a[184:187], v[248:251], v[160:175]\n"
      "s_cbranch_vccnz .Lpk_227\n"
      ".Lpr_228:\n"
      "buffer_load_dwordx4 v[156:159], v1, s[20:23], s80 offen offset:1024 sc1\n"
      "s_waitcnt vmcnt(12)\n"
      "v_mfma_f32_32x32x16_bf16 v[176:191], a[196:199], v[252:255], v[176:191]\n"
      "v_cmp_eq_u32 vcc, -1, v224\n"
      "v_mfma_f32_32x32x16_bf16 v[160:175], a[192:195], v[252:255], v[160:175]\n"
      "s_cbranch_vccnz .Lpk_230\n"
      ".Lpr_231:\n"
      "buffer_load_dwordx4 v[12:15], v1, s[20:23], s80 offen offset:2048 sc1\n"
      "s_waitcnt vmcnt(12)\n"
      "v_mfma_f32_32x32x16_bf16 v[176:191], a[204:207], v[224:227], v[176:191]\n"
      "v_cmp_eq_u32 vcc, -1, v228\n"
      "v_mfma_f32_32x32x16_bf16 v[160:175], a[200:203], v[224:227], v[160:175]\n"
      "s_cbranch_vccnz .Lpk_233\n"
      ".Lpr_234:\n"
      "buffer_load_dwordx4 v[244:247], v1, s[20:23], s80 offen offset:3072 sc1\n"
      "s_waitcnt vmcnt(12)\n"
      "v_mfma_f32_32x32x16_bf16 v[176:191], a[212:215], v[228:231], v[176:191]\n"
      "v_cmp_eq_u32 vcc, -1, v232\n"
      "v_mfma_f32_32x32x16_bf16 v[160:175], a[208:211], v[228:231], v[160:175]\n"
      "s_cbranch_vccnz .Lpk_236\n"
      ".Lpr_237:\n"
      "s_add_u32 s80, s86, s4\n"
      "buffer_load_dwordx4 v[248:251], v1, s[20:23], s80 offen sc1\n"
      "s_waitcnt vmcnt(12)\n"
      "v_mfma_f32_32x32x16_bf16 v[176:191], a[220:223], v[232:235], v[176:191]\n"
      "v_cmp_eq_u32 vcc, -1, v236\n"
      "v_mfma_f32_32x32x16_bf16 v[160:175], a[216:219], v[232:235], v[160:175]\n"
      "s_cbranch_vccnz .Lpk_239\n"
      ".Lpr_240:\n"
      "buffer_load_dwordx4 v[252:255], v1, s[20:23], s80 offen offset:1024 sc1\n"
      "s_waitcnt vmcnt(12)\n"
      "v_mfma_f32_32x32x16_bf16 v[176:191], a[228:231], v[236:239], v[176:191]\n"
      "v_cmp_eq_u32 vcc, -1, v240\n"
      "s_waitcnt vmcnt(11)\n"
      "v_mfma_f32_32x32x16_bf16 v[160:175], a[224:227], v[236:239], v[160:175]\n"
      "s_cbranch_vccnz .Lpk_242\n"
      ".Lpr_243:\n"
      "buffer_load_dwordx4 v[224:227], v1, s[20:23], s80 offen offset:2048 sc1\n"
      "v_cmp_eq_u32 vcc, -1, v128\n"
      "s_waitcnt vmcnt(11)\n"
      "v_mfma_f32_32x32x16_bf16 v[176:191], a[236:239], v[240:243], v[176:191]\n"
      "s_cbranch_vccnz .Lpk_245\n"
      ".Lpr_246:\n"
      "v_cmp_eq_u32 vcc, -1, v132\n"
      "v_mfma_f32_32x32x16_bf16 v[176:191], a[244:247], v[128:131], v[176:191]\n"
      "s_cbranch_vccnz .Lpk_248\n"
      ".Lpr_249:\n"
      "buffer_load_dwordx4 v[228:231], v1, s[20:23], s80 offen offset:3072 sc1\n"
      "v_mfma_f32_32x32x16_bf16 v[176:191], a[252:255], v[132:135], v[176:191]\n"
      "s_add_u32 s80, s86, s5\n"
      "buffer_load_dwordx4 v[232:235], v1, s[20:23], s80 offen sc1\n"
      "v_mfma_f32_32x32x16_bf16 v[160:175], a[232:235], v[240:243], v[160:175]\n"
      "v_mfma_f32_32x32x16_bf16 v[160:175], a[240:243], v[128:131], v[160:175]\n"
      "s_nop 7\n"
      "ds_write_b128 v9, v[184:187]\n"
      "v_cmp_u_f32 vcc, v176, v176\n"
      "v_mfma_f32_32x32x16_bf16 v[160:175], a[248:251], v[132:135], v[160:175]\n"
      "buffer_load_dwordx4 v[236:239], v1, s[20:23], s80 offen offset:1024 sc1\n"
      "s_cbranch_vccnz .Lb1_fix0\n"
      ".Lb1_ok0:\n"
      "s_waitcnt vmcnt(13)\n"
      "v_mfma_f32_32x32x16_bf16 v[208:223], a[4:7], v[136:139], 0\n"
      "ds_write_b128 v9, v[188:191] offset:1024\n"
      "s_waitcnt lgkmcnt(2)\n"
      "v_mfma_f32_32x32x16_bf16 v[192:207], a[0:3], v[136:139], v[192:207]\n"
      "buffer_load_dwordx4 v[240:243], v1, s[20:23], s80 offen offset:2048 sc1\n"
      "s_waitcnt vmcnt(13)\n"
      "v_mfma_f32_32x32x16_bf16 v[208:223], a[12:15], v[140:143], v[208:223]\n"
      "ds_write_b128 v8, v[176:179]\n"
      "v_mfma_f32_32x32x16_bf16 v[192:207], a[8:11], v[140:143], v[192:207]\n"
      "buffer_load_dwordx4 v[128:131], v1, s[20:23], s80 offen offset:3072 sc1\n"
      "s_waitcnt vmcnt(13)\n"
      "v_mfma_f32_32x32x16_bf16 v[208:223], a[20:23], v[144:147], v[208:223]\n"
      "ds_write_b128 v8, v[180:183] offset:1024\n"
      "v_mfma_f32_32x32x16_bf16 v[192:207], a[16:19], v[144:147], v[192:207]\n"
      "s_add_u32 s80, s86, s6\n"
      "buffer_load_dwordx4 v[132:135], v1, s[20:23], s80 offen sc1\n"
      "s_waitcnt vmcnt(13)\n"
      "v_mfma_f32_32x32x16_bf16 v[208:223], a[28:31], v[148:151], v[208:223]\n"
      "ds_write_b128 v7, v[168:171]\n"
      "v_mfma_f32_32x32x16_bf16 v[192:207], a[24:27], v[148:151], v[192:207]\n"
      "buffer_load_dwordx4 v[136:139], v1, s[20:23], s80 offen offset:1024 sc1\n"
      "s_waitcnt vmcnt(13)\n"
      "v_mfma_f32_32x32x16_bf16 v[208:223], a[36:39], v[152:155], v[208:223]\n"
      "ds_write_b128 v7, v[172:175] offset:1024\n"
      "v_mfma_f32_32x32x16_bf16 v[192:207], a[32:35], v[152:155], v[192:207]\n"
      "buffer_load_dwordx4 v[140:143], v1, s[20:23], s80 offen offset:2048 sc1\n"
      "s_waitcnt vmcnt(13)\n"
      "v_mfma_f32_32x32x16_bf16 v[208:223], a[44:47], v[156:159], v[208:223]\n"
      "s_waitcnt lgkmcnt(0)\n"
      "s_barrier\n"
      "ds_read_b128 v[176:179], v10 offset:0\n"
      "ds_read_b128 v[180:183], v10 offset:1024\n"
      "ds_read_b128 v[184:187], v10 offset:2048\n"
      "ds_read_b128 v[188:191], v10 offset:3072\n"
      "ds_read_b128 v[168:171], v10 offset:4096\n"
      "v_mfma_f32_32x32x16_bf16 v[192:207], a[40:43], v[156:159], v[192:207]\n"
      "buffer_load_dwordx4 v[144:147], v1, s[20:23], s80 offen offset:3072 sc1\n"
      "ds_read_b128 v[172:175], v10 offset:5120\n"
      "s_waitcnt vmcnt(13)\n"
      "v_mfma_f32_32x32x16_bf16 v[208:223], a[52:55], v[12:15], v[208:223]\n"
      "v_mfma_f32_32x32x16_bf16 v[192:207], a[48:51], v[12:15], v[192:207]\n"
      "s_add_u32 s80, s86, s7\n"
      "buffer_load_dwordx4 v[148:151], v1, s[20:23], s80 offen sc1\n"
      "s_waitcnt lgkmcnt(5)\n"
      "v_add_f32 v160, v176, v160\n"
      "v_add_f32 v161, v177, v161\n"
      "v_add_f32 v162, v178, v162\n"
      "s_waitcnt vmcnt(13)\n"
      "v_mfma_f32_32x32x16_bf16 v[208:223], a[60:63], v[244:247], v[208:223]\n"
      "v_add_f32 v163, v179, v163\n"
      "s_waitcnt lgkmcnt(4)\n"
      "v_add_f32 v164, v180, v164\n"
      "v_add_f32 v165, v181, v165\n"
      "v_add_f32 v166, v182, v166\n"
      "v_add_f32 v167, v183, v167\n"
      "s_waitcnt lgkmcnt(3)\n"
      "v_add_f32 v160, v184, v160\n"
      "v_mfma_f32_32x32x16_bf16 v[192:207], a[56:59], v[244:247], v[192:207]\n"
      "buffer_load_dwordx4 v[152:155], v1, s[20:23], s80 offen offset:1024 sc1\n"
      "v_add_f32 v161, v185, v161\n"
      "v_add_f32 v162, v186, v162\n"
      "v_add_f32 v163, v187, v163\n"
      "s_waitcnt lgkmcnt(2)\n"
      "v_add_f32 v164, v188, v164\n"
      "s_waitcnt vmcnt(13)\n"
      "v_mfma_f32_32x32x16_bf16 v[208:223], a[68:71], v[248:251], v[208:223]\n"
      "v_add_f32 v165, v189, v165\n"
      "v_add_f32 v166, v190, v166\n"
      "v_add_f32 v167, v191, v167\n"
      "s_waitcnt lgkmcnt(1)\n"
      "v_add_f32 v160, v168, v160\n"
      "v_add_f32 v161, v169, v161\n"
      "v_add_f32 v162, v170, v162\n"
      "v_mfma_f32_32x32x16_bf16 v[192:207], a[64:67], v[248:251], v[192:207]\n"
      "buffer_load_dwordx4 v[156:159], v1, s[20:23], s80 offen offset:2048 sc1\n"
      "v_add_f32 v163, v171, v163\n"
      "s_waitcnt lgkmcnt(0)\n"
      "v_add_f32 v164, v172, v164\n"
      "v_add_f32 v165, v173, v165\n"
      "v_add_f32 v166, v174, v166\n"
      "s_waitcnt vmcnt(13)\n"
      "v_mfma_f32_32x32x16_bf16 v[208:223], a[76:79], v[252:255], v[208:223]\n"
      "v_add_f32 v167, v175, v167\n"
      "v_mul_f32 v176, v160, v160\n"
      "v_mul_f32 v177, v161, v161\n"
      "v_mul_f32 v178, v162, v162\n"
      "v_mul_f32 v179, v163, v163\n"
      "v_mul_f32 v180, v164, v164\n"
      "v_mfma_f32_32x32x16_bf16 v[192:207], a[72:75], v[252:255], v[192:207]\n"
      "buffer_load_dwordx4 v[12:15], v1, s[20:23], s80 offen offset:3072 sc1\n"
      "v_mul_f32 v181, v165, v165\n"
      "v_mul_f32 v182, v166, v166\n"
      "v_mul_f32 v183, v167, v167\n"
      "v_mov_b32 v184, s93\n"
      "s_waitcnt vmcnt(13)\n"
      "v_mfma_f32_32x32x16_bf16 v[208:223], a[84:87], v[224:227], v[208:223]\n"
      "v_fmaak_f32 v176, v176, v184, 0xc0135761\n"
      "v_fmaak_f32 v177, v177, v184, 0xc0135761\n"
      "v_fmaak_f32 v178, v178, v184, 0xc0135761\n"
      "v_fmaak_f32 v179, v179, v184, 0xc0135761\n"
      "v_fmaak_f32 v180, v180, v184, 0xc0135761\n"
      "v_fmaak_f32 v181, v181, v184, 0xc0135761\n"
      "v_mfma_f32_32x32x16_bf16 v[192:207], a[80:83], v[224:227], v[192:207]\n"
      "s_add_u32 s80, s86, s8\n"
      "buffer_load_dwordx4 v[244:247], v1, s[20:23], s80 offen sc1\n"
      "v_fmaak_f32 v182, v182, v184, 0xc0135761\n"
      "v_fmaak_f32 v183, v183, v184, 0xc0135761\n"
      "v_mul_f32 v176, v160, v176\n"
      "s_waitcnt vmcnt(13)\n"
      "v_mfma_f32_32x32x16_bf16 v[208:223], a[92:95], v[228:231], v[208:223]\n"
      "v_mul_f32 v177, v161, v177\n"
      "v_mul_f32 v178, v162, v178\n"
      "v_mul_f32 v179, v163, v179\n"
      "v_mul_f32 v180, v164, v180\n"
      "v_mul_f32 v181, v165, v181\n"
      "v_mul_f32 v182, v166, v182\n"
      "v_mfma_f32_32x32x16_bf16 v[192:207], a[88:91], v[228:231], v[192:207]\n"
      "buffer_load_dwordx4 v[248:251], v1, s[20:23], s80 offen offset:1024 sc1\n"
      "v_mul_f32 v183, v167, v183\n"
      "v_exp_f32 v176, v176\n"
      "s_waitcnt vmcnt(13)\n"
      "v_mfma_f32_32x32x16_bf16 v[208:223], a[100:103], v[232:235], v[208:223]\n"
      "v_exp_f32 v177, v177\n"
      "v_exp_f32 v178, v178\n"
      "v_exp_f32 v179, v179\n"
      "v_mfma_f32_32x32x16_bf16 v[192:207], a[96:99], v[232:235], v[192:207]\n"
      "buffer_load_dwordx4 v[252:255], v1, s[20:23], s80 offen offset:2048 sc1\n"
      "v_exp_f32 v180, v180\n"
      "v_exp_f32 v181, v181\n"
      "s_waitcnt vmcnt(13)\n"
      "v_mfma_f32_32x32x16_bf16 v[208:223], a[108:111], v[236:239], v[208:223]\n"
      "v_exp_f32 v182, v182\n"
      "v_exp_f32 v183, v183\n"
      "v_add_f32 v176, 1.0, v176\n"
      "v_add_f32 v177, 1.0, v177\n"
      "v_mfma_f32_32x32x16_bf16 v[192:207], a[104:107], v[236:239], v[192:207]\n"
      "buffer_load_dwordx4 v[224:227], v1, s[20:23], s80 offen offset:3072 sc1\n"
      "v_add_f32 v178, 1.0, v178\n"
      "v_add_f32 v179, 1.0, v179\n"
      "v_add_f32 v180, 1.0, v180\n"
      "v_add_f32 v181, 1.0, v181\n"
      "s_waitcnt vmcnt(13)\n"
      "v_mfma_f32_32x32x16_bf16 v[208:223], a[116:119], v[240:243], v[208:223]\n"
      "v_add_f32 v182, 1.0, v182\n"
      "v_add_f32 v183, 1.0, v183\n"
      "v_rcp_f32 v176, v176\n"
      "v_rcp_f32 v177, v177\n"
      "v_mfma_f32_32x32x16_bf16 v[192:207], a[112:115], v[240:243], v[192:207]\n"
      "s_add_u32 s80, s86, s9\n"
      "buffer_load_dwordx4 v[228:231], v1, s[20:23], s80 offen sc1\n"
      "v_rcp_f32 v178, v178\n"
      "s_waitcnt vmcnt(13)\n"
      "v_mfma_f32_32x32x16_bf16 v[208:223], a[124:127], v[128:131], v[208:223]\n"
      "v_rcp_f32 v179, v179\n"
      "v_rcp_f32 v180, v180\n"
      "v_rcp_f32 v181, v181\n"
      "v_mfma_f32_32x32x16_bf16 v[192:207], a[120:123], v[128:131], v[192:207]\n"
      "buffer_load_dwordx4 v[232:235], v1, s[20:23], s80 offen offset:1024 sc1\n"
      "v_rcp_f32 v182, v182\n"
      "v_rcp_f32 v183, v183\n"
      "s_waitcnt vmcnt(13)\n"
      "v_mfma_f32_32x32x16_bf16 v[208:223], a[132:135], v[132:135], v[208:223]\n"
      "v_mul_f32 v176, v160, v176\n"
      "v_mul_f32 v177, v161, v177\n"
      "v_mul_f32 v178, v162, v178\n"
      "v_mul_f32 v179, v163, v179\n"
      "v_mul_f32 v180, v164, v180\n"
      "v_mul_f32 v181, v165, v181\n"
      "v_mfma_f32_32x32x16_bf16 v[192:207], a[128:131], v[132:135], v[192:207]\n"
      "buffer_load_dwordx4 v[236:239], v1, s[20:23], s80 offen offset:2048 sc1\n"
      "v_mul_f32 v182, v166, v182\n"
      "v_mul_f32 v183, v167, v183\n"
      "v_cvt_pk_bf16_f32 v184, v176, v177\n"
      "v_cvt_pk_bf16_f32 v185, v178, v179\n"
      "s_waitcnt vmcnt(13)\n"
      "v_mfma_f32_32x32x16_bf16 v[208:223], a[140:143], v[136:139], v[208:223]\n"
      "v_cvt_pk_bf16_f32 v186, v180, v181\n"
      "v_cvt_pk_bf16_f32 v187, v182, v183\n"
      "v_min_u32 v184, -2, v184\n"
      "v_min_u32 v185, -2, v185\n"
      "v_min_u32 v186, -2, v186\n"
      "v_min_u32 v187, -2, v187\n"
      "v_mfma_f32_32x32x16_bf16 v[192:207], a[136:139], v[136:139], v[192:207]\n"
      "buffer_load_dwordx4 v[240:243], v1, s[20:23], s80 offen offset:3072 sc1\n"
      "s_mov_b32 s82, s62\n"
      "buffer_store_dwordx4 v[184:187], v1, s[20:23], s82 offen sc1\n"
      "s_waitcnt vmcnt(14)\n"
      "v_mfma_f32_32x32x16_bf16 v[208:223], a[148:151], v[140:143], v[208:223]\n"
      "v_mfma_f32_32x32x16_bf16 v[192:207], a[144:147], v[140:143], v[192:207]\n"
      "s_waitcnt vmcnt(13)\n"
      "v_mfma_f32_32x32x16_bf16 v[208:223], a[156:159], v[144:147], v[208:223]\n"
      "v_mfma_f32_32x32x16_bf16 v[192:207], a[152:155], v[144:147], v[192:207]\n"
      "s_waitcnt vmcnt(12)\n"
      "v_mfma_f32_32x32x16_bf16 v[208:223], a[164:167], v[148:151], v[208:223]\n"
      "v_mfma_f32_32x32x16_bf16 v[192:207], a[160:163], v[148:151], v[192:207]\n"
      "s_waitcnt vmcnt(11)\n"
      "v_mfma_f32_32x32x16_bf16 v[208:223], a[172:175], v[152:155], v[208:223]\n"
      "v_mfma_f32_32x32x16_bf16 v[192:207], a[168:171], v[152:155], v[192:207]\n"
      "s_waitcnt vmcnt(10)\n"
      "v_mfma_f32_32x32x16_bf16 v[208:223], a[180:183], v[156:159], v[208:223]\n"
      "v_mfma_f32_32x32x16_bf16 v[192:207], a[176:179], v[156:159], v[192:207]\n"
      "s_waitcnt vmcnt(9)\n"
      "v_mfma_f32_32x32x16_bf16 v[208:223], a[188:191], v[12:15], v[208:223]\n"
      "s_mul_i32 s78, s11, 64\n"
      "s_add_u32 s78, s78, s75\n"
      "s_add_u32 m0, s76, 0x10000\n"
      "s_nop 0\n"
      "buffer_load_dwordx4 v6, s[40:43], s78 offen lds\n"
      "v_mfma_f32_32x32x16_bf16 v[192:207], a[184:187], v[12:15], v[192:207]\n"
      "s_waitcnt vmcnt(9)\n"
      "v_mfma_f32_32x32x16_bf16 v[208:223], a[196:199], v[244:247], v[208:223]\n"
      "s_add_u32 s78, s78, 0x7c00\n"
      "buffer_load_dwordx4 v6, s[40:43], s78 offen offset:1024 lds\n"
      "s_barrier\n"
      "s_waitcnt vmcnt(9)\n"
      "v_mfma_f32_32x32x16_bf16 v[208:223], a[204:207], v[248:251], v[208:223]\n"
      "v_lshlrev_b32 v160, 16, v18\n"
      "v_and_b32 v161, 0xffff0000, v18\n"
      "v_lshlrev_b32 v162, 16, v19\n"
      "v_and_b32 v163, 0xffff0000, v19\n"
      "v_lshlrev_b32 v164, 16, v20\n"
      "s_waitcnt vmcnt(8)\n"
      "v_mfma_f32_32x32x16_bf16 v[208:223], a[212:215], v[252:255], v[208:223]\n"
      "s_add_u32 s78, s78, 0x7c00\n"
      "buffer_load_dwordx4 v6, s[40:43], s78 offen offset:2048 lds\n"
      "v_and_b32 v165, 0xffff0000, v20\n"
      "v_lshlrev_b32 v166, 16, v21\n"
      "v_and_b32 v167, 0xffff0000, v21\n"
      "v_add_f32 v160, v26, v160\n"
      "s_waitcnt vmcnt(8)\n"
      "v_mfma_f32_32x32x16_bf16 v[208:223], a[220:223], v[224:227], v[208:223]\n"
      "v_add_f32 v161, v26, v161\n"
      "v_add_f32 v162, v26, v162\n"
      "v_add_f32 v163, v26, v163\n"
      "v_add_f32 v164, v26, v164\n"
      "v_add_f32 v165, v26, v165\n"
      "s_waitcnt vmcnt(7)\n"
      "v_mfma_f32_32x32x16_bf16 v[208:223], a[228:231], v[228:231], v[208:223]\n"
      "s_add_u32 s78, s78, 0x7c00\n"
      "buffer_load_dwordx4 v6, s[40:43], s78 offen offset:3072 lds\n"
      "v_add_f32 v166, v26, v166\n"
      "v_add_f32 v167, v26, v167\n"
      "v_mov_b32 v168, 0\n"
      "v_mov_b32 v169, 0\n"
      "s_waitcnt vmcnt(7)\n"
      "v_mfma_f32_32x32x16_bf16 v[208:223], a[236:239], v[232:235], v[208:223]\n"
      "v_mov_b32 v170, 0\n"
      "v_mov_b32 v171, 0\n"
      "v_mov_b32 v172, 0\n"
      "v_mov_b32 v173, 0\n"
      "v_mov_b32 v174, 0\n"
      "s_waitcnt vmcnt(6)\n"
      "v_mfma_f32_32x32x16_bf16 v[208:223], a[244:247], v[236:239], v[208:223]\n"
      "s_mul_i32 s78, s12, 64\n"
      "s_add_u32 s78, s78, s75\n"
      "s_add_u32 m0, s76, 0x14000\n"
      "s_nop 0\n"
      "buffer_load_dwordx4 v6, s[40:43], s78 offen lds\n"
      "v_mov_b32 v175, 0\n"
      "s_waitcnt vmcnt(6)\n"
      "v_mfma_f32_32x32x16_bf16 v[208:223], a[252:255], v[240:243], v[208:223]\n"
      "v_mfma_f32_32x32x16_bf16 v[192:207], a[192:195], v[244:247], v[192:207]\n"
      "s_add_u32 s78, s78, 0x7c00\n"
      "buffer_load_dwordx4 v6, s[40:43], s78 offen offset:1024 lds\n"
      "v_mfma_f32_32x32x16_bf16 v[192:207], a[200:203], v[248:251], v[192:207]\n"
      "s_nop 7\n"
      "ds_write_b128 v9, v[216:219]\n"
      "v_cmp_u_f32 vcc, v208, v208\n"
      "v_mfma_f32_32x32x16_bf16 v[192:207], a[208:211], v[252:255], v[192:207]\n"
      "s_add_u32 s78, s78, 0x7c00\n"
      "buffer_load_dwordx4 v6, s[40:43], s78 offen offset:2048 lds\n"
      "v_mfma_f32_32x32x16_bf16 v[192:207], a[216:219], v[224:227], v[192:207]\n"
      "ds_write_b128 v9, v[220:223] offset:1024\n"
      "v_mfma_f32_32x32x16_bf16 v[192:207], a[224:227], v[228:231], v[192:207]\n"
      "s_add_u32 s78, s78, 0x7c00\n"
      "buffer_load_dwordx4 v6, s[40:43], s78 offen offset:3072 lds\n"
      "v_mfma_f32_32x32x16_bf16 v[192:207], a[232:235], v[232:235], v[192:207]\n"
      "ds_write_b128 v8, v[208:211]\n"
      "v_mfma_f32_32x32x16_bf16 v[192:207], a[240:243], v[236:239], v[192:207]\n"
      "v_mfma_f32_32x32x16_bf16 v[192:207], a[248:251], v[240:243], v[192:207]\n"
      "ds_write_b128 v8, v[212:215] offset:1024\n"
      "s_cbranch_vccnz .Lb1_fix1\n"
      ".Lb1_ok1:\n"
      "s_add_u32 s80, s69, s10\n"
      "buffer_load_dwordx4 v[128:131], v28, s[20:23], s80 offen sc1\n"
      "buffer_load_dwordx4 v[132:135], v28, s[20:23], s80 offen offset:1024 sc1\n"
      "s_add_u32 s86, s69, 0x1c000\n"
      "s_mov_b32 s48, s20\n"
      "s_mov_b32 s49, s21\n"
      "s_add_u32 s50, s72, 0x15000\n"
      "s_add_u32 s85, s72, s76\n"
      "s_nop 1\n"
      "ds_write_b128 v7, v[200:203]\n"
      "ds_write_b128 v7, v[204:207] offset:1024\n"
      "s_waitcnt lgkmcnt(0)\n"
      "s_barrier\n"
      "ds_read_b128 v[208:211], v10 offset:0\n"
      "ds_read_b128 v[212:215], v10 offset:1024\n"
      "ds_read_b128 v[216:219], v10 offset:2048\n"
      "ds_read_b128 v[220:223], v10 offset:3072\n"
      "ds_read_b128 v[200:203], v10 offset:4096\n"
      "ds_read_b128 v[204:207], v10 offset:5120\n"
      "buffer_load_dwordx4 v[136:139], v28, s[20:23], s80 offen offset:2048 sc1\n"
      "buffer_load_dwordx4 v[140:143], v28, s[20:23], s80 offen offset:3072 sc1\n"
      "s_add_u32 s80, s69, s11\n"
      "buffer_load_dwordx4 v[144:147], v28, s[20:23], s80 offen sc1\n"
      "s_waitcnt lgkmcnt(5)\n"
      "v_pk_add_f32 v[192:193], v[192:193], v[208:209]\n"
      "v_pk_add_f32 v[194:195], v[194:195], v[210:211]\n"
      "s_waitcnt lgkmcnt(4)\n"
      "v_pk_add_f32 v[196:197], v[196:197], v[212:213]\n"
      "v_pk_add_f32 v[198:199], v[198:199], v[214:215]\n"
      "s_waitcnt lgkmcnt(3)\n"
      "v_pk_add_f32 v[192:193], v[192:193], v[216:217]\n"
      "v_pk_add_f32 v[194:195], v[194:195], v[218:219]\n"
      "s_waitcnt lgkmcnt(2)\n"
      "v_pk_add_f32 v[196:197], v[196:197], v[220:221]\n"
      "v_pk_add_f32 v[198:199], v[198:199], v[222:223]\n"
      "s_waitcnt lgkmcnt(1)\n"
      "v_pk_add_f32 v[192:193], v[192:193], v[200:201]\n"
      "v_pk_add_f32 v[194:195], v[194:195], v[202:203]\n"
      "s_waitcnt lgkmcnt(0)\n"
      "v_pk_add_f32 v[196:197], v[196:197], v[204:205]\n"
      "v_pk_add_f32 v[198:199], v[198:199], v[206:207]\n"
      "v_pk_mul_f32 v[208:209], v[192:193], v[192:193]\n"
      "v_pk_mul_f32 v[210:211], v[194:195], v[194:195]\n"
      "v_pk_mul_f32 v[212:213], v[196:197], v[196:197]\n"
      "v_pk_mul_f32 v[214:215], v[198:199], v[198:199]\n"
      "v_pk_fma_f32 v[208:209], v[208:209], s[92:93], s[92:93] op_sel:[0,1,0] op_sel_hi:[1,1,0]\n"
      "v_pk_fma_f32 v[210:211], v[210:211], s[92:93], s[92:93] op_sel:[0,1,0] op_sel_hi:[1,1,0]\n"
      "v_pk_fma_f32 v[212:213], v[212:213], s[92:93], s[92:93] op_sel:[0,1,0] op_sel_hi:[1,1,0]\n"
      "v_pk_fma_f32 v[214:215], v[214:215], s[92:93], s[92:93] op_sel:[0,1,0] op_sel_hi:[1,1,0]\n"
      "v_pk_mul_f32 v[208:209], v[208:209], v[192:193]\n"
      "v_pk_mul_f32 v[210:211], v[210:211], v[194:195]\n"
      "v_pk_mul_f32 v[212:213], v[212:213], v[196:197]\n"
      "v_pk_mul_f32 v[214:215], v[214:215], v[198:199]\n"
      "v_exp_f32 v208, v208\n"
      "v_exp_f32 v209, v209\n"
      "v_exp_f32 v210, v210\n"
      "v_exp_f32 v211, v211\n"
      "v_exp_f32 v212, v212\n"
      "v_exp_f32 v213, v213\n"
      "v_exp_f32 v214, v214\n"
      "v_exp_f32 v215, v215\n"
      "v_pk_add_f32 v[208:209], v[208:209], 1.0 op_sel_hi:[1,0]\n"
      "v_pk_add_f32 v[210:211], v[210:211], 1.0 op_sel_hi:[1,0]\n"
      "v_pk_add_f32 v[212:213], v[212:213], 1.0 op_sel_hi:[1,0]\n"
      "v_pk_add_f32 v[214:215], v[214:215], 1.0 op_sel_hi:[1,0]\n"
      "v_rcp_f32 v208, v208\n"
      "v_rcp_f32 v209, v209\n"
      "v_rcp_f32 v210, v210\n"
      "v_rcp_f32 v211, v211\n"
      "v_rcp_f32 v212, v212\n"
      "v_rcp_f32 v213, v213\n"
      "v_rcp_f32 v214, v214\n"
      "v_rcp_f32 v215, v215\n"
      "v_pk_mul_f32 v[208:209], v[208:209], v[192:193]\n"
      "v_pk_mul_f32 v[210:211], v[210:211], v[194:195]\n"
      "v_pk_mul_f32 v[212:213], v[212:213], v[196:197]\n"
      "v_pk_mul_f32 v[214:215], v[214:215], v[198:199]\n"
      "v_cvt_pk_bf16_f32 v216, v208, v209\n"
      "v_cvt_pk_bf16_f32 v217, v210, v211\n"
      "v_cvt_pk_bf16_f32 v218, v212, v213\n"
      "v_cvt_pk_bf16_f32 v219, v214, v215\n"
      "v_min_u32 v216, -2, v216\n"
      "v_min_u32 v217, -2, v217\n"
      "v_min_u32 v218, -2, v218\n"
      "v_min_u32 v219, -2, v219\n"
      "s_add_u32 s82, s62, 0x1c000\n"
      "buffer_store_dwordx4 v[216:219], v1, s[20:23], s82 offen sc1\n"
      ".Lb2_start:\n"
      "s_waitcnt vmcnt(6)\n"
      "ds_read_b64_tr_b16 v[224:225], v3 offset:49152\n"
      "ds_read_b64_tr_b16 v[226:227], v3 offset:50176\n"
      "ds_read_b64_tr_b16 v[228:229], v2 offset:49152\n"
      "ds_read_b64_tr_b16 v[230:231], v2 offset:50176\n"
      "ds_read_b64_tr_b16 v[232:233], v3 offset:51200\n"
      "ds_read_b64_tr_b16 v[234:235], v3 offset:52224\n"
      "ds_read_b64_tr_b16 v[236:237], v2 offset:51200\n"
      "ds_read_b64_tr_b16 v[238:239], v2 offset:52224\n"
      "s_waitcnt vmcnt(5)\n"
      "v_cmp_eq_u32 vcc, -1, v128\n"
      "s_nop 0\n"
      "s_cbranch_vccnz .Lpk_259\n"
      ".Lpr_260:\n"
      "s_waitcnt vmcnt(4) lgkmcnt(4)\n"
      "v_mfma_f32_32x32x16_bf16 v[176:191], v[128:131], v[224:227], 0\n"
      "buffer_load_dwordx4 v[148:151], v28, s[20:23], s80 offen offset:1024 sc1\n"
      "v_cmp_eq_u32 vcc, -1, v132\n"
      "v_mfma_f32_32x32x16_bf16 v[160:175], v[128:131], v[228:231], v[160:175]\n"
      "s_cbranch_vccnz .Lpk_262\n"
      ".Lpr_263:\n"
      "buffer_load_dwordx4 v[152:155], v28, s[20:23], s80 offen offset:2048 sc1\n"
      "s_waitcnt vmcnt(5) lgkmcnt(0)\n"
      "v_mfma_f32_32x32x16_bf16 v[176:191], v[132:135], v[232:235], v[176:191]\n"
      "buffer_load_dwordx4 v[156:159], v28, s[20:23], s80 offen offset:3072 sc1\n"
      "v_cmp_eq_u32 vcc, -1, v136\n"
      "v_mfma_f32_32x32x16_bf16 v[160:175], v[132:135], v[236:239], v[160:175]\n"
      "s_cbranch_vccnz .Lpk_265\n"
      ".Lpr_266:\n"
      "s_add_u32 s80, s69, s12\n"
      "buffer_load_dwordx4 v[12:15], v28, s[20:23], s80 offen sc1\n"
      "s_waitcnt vmcnt(6)\n"
      "v_mfma_f32_32x32x16_bf16 v[176:191], v[136:139], v[36:39], v[176:191]\n"
      "buffer_load_dwordx4 v[244:247], v28, s[20:23], s80 offen offset:1024 sc1\n"
      "ds_read_b64_tr_b16 v[236:237], v5 offset:0\n"
      "ds_read_b64_tr_b16 v[238:239], v5 offset:1024\n"
      "v_lshlrev_b32 v192, 16, v22\n"
      "v_and_b32 v193, 0xffff0000, v22\n"
      "v_cmp_eq_u32 vcc, -1, v140\n"
      "v_mfma_f32_32x32x16_bf16 v[160:175], v[136:139], v[32:35], v[160:175]\n"
      "s_cbranch_vccnz .Lpk_268\n"
      ".Lpr_269:\n"
      "buffer_load_dwordx4 v[248:251], v28, s[20:23], s80 offen offset:2048 sc1\n"
      "ds_read_b64_tr_b16 v[240:241], v4 offset:0\n"
      "ds_read_b64_tr_b16 v[242:243], v4 offset:1024\n"
      "v_lshlrev_b32 v194, 16, v23\n"
      "s_waitcnt vmcnt(7)\n"
      "v_mfma_f32_32x32x16_bf16 v[176:191], v[140:143], v[44:47], v[176:191]\n"
      "buffer_load_dwordx4 v[252:255], v28, s[20:23], s80 offen offset:3072 sc1\n"
      "ds_read_b64_tr_b16 v[224:225], v5 offset:2048\n"
      "ds_read_b64_tr_b16 v[226:227], v5 offset:3072\n"
      "v_and_b32 v195, 0xffff0000, v23\n"
      "v_lshlrev_b32 v196, 16, v24\n"
      "v_cmp_eq_u32 vcc, -1, v144\n"
      "v_mfma_f32_32x32x16_bf16 v[160:175], v[140:143], v[40:43], v[160:175]\n"
      "s_cbranch_vccnz .Lpk_271\n"
      ".Lpr_272:\n"
      "s_add_u32 s80, s69, s13\n"
      "buffer_load_dwordx4 v[128:131], v28, s[20:23], s80 offen sc1\n"
      "ds_read_b64_tr_b16 v[228:229], v4 offset:2048\n"
      "ds_read_b64_tr_b16 v[230:231], v4 offset:3072\n"
      "s_waitcnt vmcnt(7) lgkmcnt(4)\n"
      "v_mfma_f32_32x32x16_bf16 v[176:191], v[144:147], v[236:239], v[176:191]\n"
      "buffer_load_dwordx4 v[132:135], v28, s[20:23], s80 offen offset:1024 sc1\n"
      "v_and_b32 v197, 0xffff0000, v24\n"
      "v_lshlrev_b32 v198, 16, v25\n"
      "v_and_b32 v199, 0xffff0000, v25\n"
      "v_add_f32 v192, v26, v192\n"
      "v_cmp_eq_u32 vcc, -1, v148\n"
      "v_mfma_f32_32x32x16_bf16 v[160:175], v[144:147], v[240:243], v[160:175]\n"
      "s_cbranch_vccnz .Lpk_274\n"
      ".Lpr_275:\n"
      "buffer_load_dwordx4 v[136:139], v28, s[20:23], s80 offen offset:2048 sc1\n"
      "v_add_f32 v193, v26, v193\n"
      "v_add_f32 v194, v26, v194\n"
      "v_add_f32 v195, v26, v195\n"
      "s_waitcnt vmcnt(8) lgkmcnt(0)\n"
      "v_mfma_f32_32x32x16_bf16 v[176:191], v[148:151], v[224:227], v[176:191]\n"
      "buffer_load_dwordx4 v[140:143], v28, s[20:23], s80 offen offset:3072 sc1\n"
      "v_add_f32 v196, v26, v196\n"
      "v_add_f32 v197, v26, v197\n"
      "v_add_f32 v198, v26, v198\n"
      "v_add_f32 v199, v26, v199\n"
      "v_cmp_eq_u32 vcc, -1, v152\n"
      "v_mfma_f32_32x32x16_bf16 v[160:175], v[148:151], v[228:231], v[160:175]\n"
      "s_cbranch_vccnz .Lpk_277\n"
      ".Lpr_278:\n"
      "s_add_u32 s80, s69, s19\n"
      "buffer_load_dwordx4 v[144:147], v28, s[20:23], s80 offen sc1\n"
      "v_mov_b32 v200, 0\n"
      "v_mov_b32 v201, 0\n"
      "s_waitcnt vmcnt(9)\n"
      "v_mfma_f32_32x32x16_bf16 v[176:191], v[152:155], v[52:55], v[176:191]\n"
      "ds_read_b64_tr_b16 v[228:229], v5 offset:16384\n"
      "ds_read_b64_tr_b16 v[230:231], v5 offset:17408\n"
      "v_mov_b32 v202, 0\n"
      "v_mov_b32 v203, 0\n"
      "v_mov_b32 v204, 0\n"
      "v_cmp_eq_u32 vcc, -1, v156\n"
      "v_mfma_f32_32x32x16_bf16 v[160:175], v[152:155], v[48:51], v[160:175]\n"
      "s_cbranch_vccnz .Lpk_280\n"
      ".Lpr_281:\n"
      "buffer_load_dwordx4 v[148:151], v28, s[20:23], s80 offen offset:1024 sc1\n"
      "ds_read_b64_tr_b16 v[232:233], v4 offset:16384\n"
      "ds_read_b64_tr_b16 v[234:235], v4 offset:17408\n"
      "v_mov_b32 v205, 0\n"
      "s_waitcnt vmcnt(9)\n"
      "v_mfma_f32_32x32x16_bf16 v[176:191], v[156:159], v[60:63], v[176:191]\n"
      "ds_read_b64_tr_b16 v[236:237], v5 offset:18432\n"
      "ds_read_b64_tr_b16 v[238:239], v5 offset:19456\n"
      "v_mov_b32 v206, 0\n"
      "v_mov_b32 v207, 0\n"
      "v_cmp_eq_u32 vcc, -1, v12\n"
      "v_mfma_f32_32x32x16_bf16 v[160:175], v[156:159], v[56:59], v[160:175]\n"
      "s_cbranch_vccnz .Lpk_283\n"
      ".Lpr_284:\n"
      "buffer_load_dwordx4 v[152:155], v28, s[20:23], s80 offen offset:2048 sc1\n"
      "ds_read_b64_tr_b16 v[240:241], v4 offset:18432\n"
      "ds_read_b64_tr_b16 v[242:243], v4 offset:19456\n"
      "s_waitcnt vmcnt(9) lgkmcnt(4)\n"
      "v_mfma_f32_32x32x16_bf16 v[176:191], v[12:15], v[228:231], v[176:191]\n"
      "ds_read_b64_tr_b16 v[224:225], v3 offset:32768\n"
      "ds_read_b64_tr_b16 v[226:227], v3 offset:33792\n"
      "v_cmp_eq_u32 vcc, -1, v244\n"
      "v_mfma_f32_32x32x16_bf16 v[160:175], v[12:15], v[232:235], v[160:175]\n"
      "s_cbranch_vccnz .Lpk_286\n"
      ".Lpr_287:\n"
      "buffer_load_dwordx4 v[156:159], v28, s[20:23], s80 offen offset:3072 sc1\n"
      "ds_read_b64_tr_b16 v[228:229], v2 offset:32768\n"
      "ds_read_b64_tr_b16 v[230:231], v2 offset:33792\n"
      "s_waitcnt vmcnt(9) lgkmcnt(4)\n"
      "v_mfma_f32_32x32x16_bf16 v[176:191], v[244:247], v[236:239], v[176:191]\n"
      "ds_read_b64_tr_b16 v[232:233], v3 offset:34816\n"
      "ds_read_b64_tr_b16 v[234:235], v3 offset:35840\n"
      "v_cmp_eq_u32 vcc, -1, v248\n"
      "v_mfma_f32_32x32x16_bf16 v[160:175], v[244:247], v[240:243], v[160:175]\n"
      "s_cbranch_vccnz .Lpk_289\n"
      ".Lpr_290:\n"
      "s_add_u32 s80, s69, s99\n"
      "buffer_load_dwordx4 v[12:15], v28, s[20:23], s80 offen sc1\n"
      "ds_read_b64_tr_b16 v[236:237], v2 offset:34816\n"
      "ds_read_b64_tr_b16 v[238:239], v2 offset:35840\n"
      "s_waitcnt vmcnt(9) lgkmcnt(4)\n"
      "v_mfma_f32_32x32x16_bf16 v[176:191], v[248:251], v[224:227], v[176:191]\n"
      "v_cmp_eq_u32 vcc, -1, v252\n"
      "v_mfma_f32_32x32x16_bf16 v[160:175], v[248:251], v[228:231], v[160:175]\n"
      "s_cbranch_vccnz .Lpk_292\n"
      ".Lpr_293:\n"
      "buffer_load_dwordx4 v[244:247], v28, s[20:23], s80 offen offset:1024 sc1\n"
      "s_waitcnt vmcnt(9) lgkmcnt(0)\n"
      "v_mfma_f32_32x32x16_bf16 v[176:191], v[252:255], v[232:235], v[176:191]\n"
      "v_cmp_eq_u32 vcc, -1, v128\n"
      "v_mfma_f32_32x32x16_bf16 v[160:175], v[252:255], v[236:239], v[160:175]\n"
      "s_cbranch_vccnz .Lpk_295\n"
      ".Lpr_296:\n"
      "buffer_load_dwordx4 v[248:251], v28, s[20:23], s80 offen offset:2048 sc1\n"
      "s_waitcnt vmcnt(9)\n"
      "v_mfma_f32_32x32x16_bf16 v[176:191], v[128:131], v[68:71], v[176:191]\n"
      "ds_read_b64_tr_b16 v[236:237], v3 offset:16384\n"
      "ds_read_b64_tr_b16 v[238:239], v3 offset:17408\n"
      "v_cmp_eq_u32 vcc, -1, v132\n"
      "v_mfma_f32_32x32x16_bf16 v[160:175], v[128:131], v[64:67], v[160:175]\n"
      "s_cbranch_vccnz .Lpk_298\n"
      ".Lpr_299:\n"
      "buffer_load_dwordx4 v[252:255], v28, s[20:23], s80 offen offset:3072 sc1\n"
      "ds_read_b64_tr_b16 v[240:241], v2 offset:16384\n"
      "ds_read_b64_tr_b16 v[242:243], v2 offset:17408\n"
      "s_waitcnt vmcnt(9)\n"
      "v_mfma_f32_32x32x16_bf16 v[176:191], v[132:135], v[76:79], v[176:191]\n"
      "ds_read_b64_tr_b16 v[224:225], v3 offset:18432\n"
      "ds_read_b64_tr_b16 v[226:227], v3 offset:19456\n"
      "v_cmp_eq_u32 vcc, -1, v136\n"
      "v_mfma_f32_32x32x16_bf16 v[160:175], v[132:135], v[72:75], v[160:175]\n"
      "s_cbranch_vccnz .Lpk_301\n"
      ".Lpr_302:\n"
      "s_add_u32 s80, s69, s16\n"
      "buffer_load_dwordx4 v[128:131], v28, s[20:23], s80 offen sc1\n"
      "ds_read_b64_tr_b16 v[228:229], v2 offset:18432\n"
      "ds_read_b64_tr_b16 v[230:231], v2 offset:19456\n"
      "s_waitcnt vmcnt(9) lgkmcnt(4)\n"
      "v_mfma_f32_32x32x16_bf16 v[176:191], v[136:139], v[236:239], v[176:191]\n"
      "ds_read_b64_tr_b16 v[232:233], v3 offset:0\n"
      "ds_read_b64_tr_b16 v[234:235], v3 offset:1024\n"
      "v_cmp_eq_u32 vcc, -1, v140\n"
      "v_mfma_f32_32x32x16_bf16 v[160:175], v[136:139], v[240:243], v[160:175]\n"
      "s_cbranch_vccnz .Lpk_304\n"
      ".Lpr_305:\n"
      "buffer_load_dwordx4 v[132:135], v28, s[20:23], s80 offen offset:1024 sc1\n"
      "ds_read_b64_tr_b16 v[236:237], v2 offset:0\n"
      "ds_read_b64_tr_b16 v[238:239], v2 offset:1024\n"
      "s_waitcnt vmcnt(9) lgkmcnt(4)\n"
      "v_mfma_f32_32x32x16_bf16 v[176:191], v[140:143], v[224:227], v[176:191]\n"
      "ds_read_b64_tr_b16 v[240:241], v3 offset:2048\n"
      "ds_read_b64_tr_b16 v[242:243], v3 offset:3072\n"
      "v_cmp_eq_u32 vcc, -1, v144\n"
      "v_mfma_f32_32x32x16_bf16 v[160:175], v[140:143], v[228:231], v[160:175]\n"
      "s_cbranch_vccnz .Lpk_307\n"
      ".Lpr_308:\n"
      "buffer_load_dwordx4 v[136:139], v28, s[20:23], s80 offen offset:2048 sc1\n"
      "ds_read_b64_tr_b16 v[224:225], v2 offset:2048\n"
      "ds_read_b64_tr_b16 v[226:227], v2 offset:3072\n"
      "s_waitcnt vmcnt(9) lgkmcnt(4)\n"
      "v_mfma_f32_32x32x16_bf16 v[176:191], v[144:147], v[232:235], v[176:191]\n"
      "v_cmp_eq_u32 vcc, -1, v148\n"
      "v_mfma_f32_32x32x16_bf16 v[160:175], v[144:147], v[236:239], v[160:175]\n"
      "s_cbranch_vccnz .Lpk_310\n"
      ".Lpr_311:\n"
      "buffer_load_dwordx4 v[140:143], v28, s[20:23], s80 offen offset:3072 sc1\n"
      "s_waitcnt vmcnt(9) lgkmcnt(0)\n"
      "v_mfma_f32_32x32x16_bf16 v[176:191], v[148:151], v[240:243], v[176:191]\n"
      "v_cmp_eq_u32 vcc, -1, v152\n"
      "v_mfma_f32_32x32x16_bf16 v[160:175], v[148:151], v[224:227], v[160:175]\n"
      "s_cbranch_vccnz .Lpk_313\n"
      ".Lpr_314:\n"
      "s_add_u32 s80, s86, s10\n"
      "buffer_load_dwordx4 v[144:147], v28, s[20:23], s80 offen sc1\n"
      "s_waitcnt vmcnt(9)\n"
      "v_mfma_f32_32x32x16_bf16 v[176:191], v[152:155], v[84:87], v[176:191]\n"
      "ds_read_b64_tr_b16 v[224:225], v5 offset:32768\n"
      "ds_read_b64_tr_b16 v[226:227], v5 offset:33792\n"
      "v_cmp_eq_u32 vcc, -1, v156\n"
      "v_mfma_f32_32x32x16_bf16 v[160:175], v[152:155], v[80:83], v[160:175]\n"
      "s_cbranch_vccnz .Lpk_316\n"
      ".Lpr_317:\n"
      "buffer_load_dwordx4 v[148:151], v28, s[20:23], s80 offen offset:1024 sc1\n"
      "ds_read_b64_tr_b16 v[228:229], v4 offset:32768\n"
      "ds_read_b64_tr_b16 v[230:231], v4 offset:33792\n"
      "s_waitcnt vmcnt(9)\n"
      "v_mfma_f32_32x32x16_bf16 v[176:191], v[156:159], v[92:95], v[176:191]\n"
      "ds_read_b64_tr_b16 v[232:233], v5 offset:34816\n"
      "ds_read_b64_tr_b16 v[234:235], v5 offset:35840\n"
      "v_cmp_eq_u32 vcc, -1, v12\n"
      "v_mfma_f32_32x32x16_bf16 v[160:175], v[156:159], v[88:91], v[160:175]\n"
      "s_cbranch_vccnz .Lpk_319\n"
      ".Lpr_320:\n"
      "buffer_load_dwordx4 v[152:155], v28, s[20:23], s80 offen offset:2048 sc1\n"
      "ds_read_b64_tr_b16 v[236:237], v4 offset:34816\n"
      "ds_read_b64_tr_b16 v[238:239], v4 offset:35840\n"
      "s_waitcnt vmcnt(9) lgkmcnt(4)\n"
      "v_mfma_f32_32x32x16_bf16 v[176:191], v[12:15], v[224:227], v[176:191]\n"
      "v_cmp_eq_u32 vcc, -1, v244\n"
      "v_mfma_f32_32x32x16_bf16 v[160:175], v[12:15], v[228:231], v[160:175]\n"
      "s_cbranch_vccnz .Lpk_322\n"
      ".Lpr_323:\n"
      "buffer_load_dwordx4 v[156:159], v28, s[20:23], s80 offen offset:3072 sc1\n"
      "s_waitcnt vmcnt(9) lgkmcnt(0)\n"
      "v_mfma_f32_32x32x16_bf16 v[176:191], v[244:247], v[232:235], v[176:191]\n"
      "v_cmp_eq_u32 vcc, -1, v248\n"
      "v_mfma_f32_32x32x16_bf16 v[160:175], v[244:247], v[236:239], v[160:175]\n"
      "s_cbranch_vccnz .Lpk_325\n"
      ".Lpr_326:\n"
      "s_add_u32 s80, s86, s11\n"
      "buffer_load_dwordx4 v[12:15], v28, s[20:23], s80 offen sc1\n"
      "s_waitcnt vmcnt(9)\n"
      "v_mfma_f32_32x32x16_bf16 v[176:191], v[248:251], v[100:103], v[176:191]\n"
      "ds_read_b64_tr_b16 v[236:237], v5 offset:49152\n"
      "ds_read_b64_tr_b16 v[238:239], v5 offset:50176\n"
      "v_cmp_eq_u32 vcc, -1, v252\n"
      "v_mfma_f32_32x32x16_bf16 v[160:175], v[248:251], v[96:99], v[160:175]\n"
      "s_cbranch_vccnz .Lpk_328\n"
      ".Lpr_329:\n"
      "buffer_load_dwordx4 v[244:247], v28, s[20:23], s80 offen offset:1024 sc1\n"
      "ds_read_b64_tr_b16 v[240:241], v4 offset:49152\n"
      "ds_read_b64_tr_b16 v[242:243], v4 offset:50176\n"
      "s_waitcnt vmcnt(9)\n"
      "v_mfma_f32_32x32x16_bf16 v[176:191], v[252:255], v[108:111], v[176:191]\n"
      "ds_read_b64_tr_b16 v[224:225], v5 offset:51200\n"
      "ds_read_b64_tr_b16 v[226:227], v5 offset:52224\n"
      "v_cmp_eq_u32 vcc, -1, v128\n"
      "v_mfma_f32_32x32x16_bf16 v[160:175], v[252:255], v[104:107], v[160:175]\n"
      "s_cbranch_vccnz .Lpk_331\n"
      ".Lpr_332:\n"
      "buffer_load_dwordx4 v[248:251], v28, s[20:23], s80 offen offset:2048 sc1\n"
      "s_waitcnt vmcnt(9) lgkmcnt(2)\n"
      "v_mfma_f32_32x32x16_bf16 v[176:191], v[128:131], v[236:239], v[176:191]\n"
      "v_cmp_eq_u32 vcc, -1, v132\n"
      "s_waitcnt vmcnt(8) lgkmcnt(0)\n"
      "v_mfma_f32_32x32x16_bf16 v[160:175], v[128:131], v[240:243], v[160:175]\n"
      "s_cbranch_vccnz .Lpk_334\n"
      ".Lpr_335:\n"
      "buffer_load_dwordx4 v[252:255], v28, s[20:23], s80 offen offset:3072 sc1\n"
      "ds_read_b64_tr_b16 v[236:237], v4 offset:51200\n"
      "ds_read_b64_tr_b16 v[238:239], v4 offset:52224\n"
      "v_cmp_eq_u32 vcc, -1, v136\n"
      "s_waitcnt vmcnt(8)\n"
      "v_mfma_f32_32x32x16_bf16 v[176:191], v[132:135], v[224:227], v[176:191]\n"
      "s_cbranch_vccnz .Lpk_337\n"
      ".Lpr_338:\n"
      "s_barrier\n"
      "v_cmp_eq_u32 vcc, -1, v140\n"
      "v_mfma_f32_32x32x16_bf16 v[176:191], v[136:139], v[116:119], v[176:191]\n"
      "s_cbranch_vccnz .Lpk_340\n"
      ".Lpr_341:\n"
      "s_add_u32 s80, s86, s12\n"
      "buffer_load_dwordx4 v[128:131], v28, s[20:23], s80 offen sc1\n"
      "v_mfma_f32_32x32x16_bf16 v[176:191], v[140:143], v[124:127], v[176:191]\n"
      "ds_read_b64_tr_b16 v[228:229], v3 offset:49152\n"
      "ds_read_b64_tr_b16 v[230:231], v3 offset:50176\n"
      "s_waitcnt lgkmcnt(2)\n"
      "v_mfma_f32_32x32x16_bf16 v[160:175], v[132:135], v[236:239], v[160:175]\n"
      "ds_read_b64_tr_b16 v[232:233], v2 offset:49152\n"
      "ds_read_b64_tr_b16 v[234:235], v2 offset:50176\n"
      "v_mfma_f32_32x32x16_bf16 v[160:175], v[136:139], v[112:115], v[160:175]\n"
      "ds_read_b64_tr_b16 v[236:237], v3 offset:51200\n"
      "ds_read_b64_tr_b16 v[238:239], v3 offset:52224\n"
      "s_nop 2\n"
      "ds_write_b128 v9, v[184:187]\n"
      "v_fma_f32 v208, v176, v177, v178\n"
      "v_fma_f32 v209, v179, v180, v181\n"
      "v_fma_f32 v210, v182, v183, v184\n"
      "v_mfma_f32_32x32x16_bf16 v[160:175], v[140:143], v[120:123], v[160:175]\n"
      "buffer_load_dwordx4 v[132:135], v28, s[20:23], s80 offen offset:1024 sc1\n"
      "ds_read_b64_tr_b16 v[240:241], v2 offset:51200\n"
      "ds_read_b64_tr_b16 v[242:243], v2 offset:52224\n"
      "v_fma_f32 v211, v185, v186, v187\n"
      "v_fma_f32 v212, v188, v189, v190\n"
      "v_fma_f32 v208, v208, v209, v210\n"
      "v_fma_f32 v211, v211, v212, v191\n"
      "v_cmp_u_f32 vcc, v208, v211\n"
      "s_nop 0\n"
      "s_cbranch_vccnz .Lb2_fix0\n"
      ".Lb2_ok0:\n"
      "s_waitcnt vmcnt(9)\n"
      "v_cmp_eq_u32 vcc, -1, v144\n"
      "s_nop 0\n"
      "s_cbranch_vccnz .Lpk_345\n"
      ".Lpr_346:\n"
      "s_waitcnt vmcnt(8) lgkmcnt(5)\n"
      "v_mfma_f32_32x32x16_bf16 v[208:223], v[144:147], v[228:231], 0\n"
      "buffer_load_dwordx4 v[136:139], v28, s[20:23], s80 offen offset:2048 sc1\n"
      "ds_write_b128 v9, v[188:191] offset:1024\n"
      "v_cmp_eq_u32 vcc, -1, v148\n"
      "v_mfma_f32_32x32x16_bf16 v[192:207], v[144:147], v[232:235], v[192:207]\n"
      "s_cbranch_vccnz .Lpk_348\n"
      ".Lpr_349:\n"
      "buffer_load_dwordx4 v[140:143], v28, s[20:23], s80 offen offset:3072 sc1\n"
      "s_waitcnt vmcnt(9) lgkmcnt(1)\n"
      "v_mfma_f32_32x32x16_bf16 v[208:223], v[148:151], v[236:239], v[208:223]\n"
      "v_and_b32 v11, 0x1f0, v1\n"
      "buffer_load_dword v11, v11, s[20:23], 0 offen offset:2048 sc1\n"
      "ds_write_b128 v8, v[176:179]\n"
      "v_cmp_eq_u32 vcc, -1, v152\n"
      "v_mfma_f32_32x32x16_bf16 v[192:207], v[148:151], v[240:243], v[192:207]\n"
      "s_cbranch_vccnz .Lpk_351\n"
      ".Lpr_352:\n"
      "s_add_u32 s80, s86, s13\n"
      "buffer_load_dwordx4 v[144:147], v28, s[20:23], s80 offen sc1\n"
      "s_waitcnt vmcnt(10)\n"
      "v_mfma_f32_32x32x16_bf16 v[208:223], v[152:155], v[36:39], v[208:223]\n"
      "ds_read_b64_tr_b16 v[240:241], v5 offset:0\n"
      "ds_read_b64_tr_b16 v[242:243], v5 offset:1024\n"
      "ds_write_b128 v8, v[180:183] offset:1024\n"
      "v_cmp_eq_u32 vcc, -1, v156\n"
      "v_mfma_f32_32x32x16_bf16 v[192:207], v[152:155], v[32:35], v[192:207]\n"
      "s_cbranch_vccnz .Lpk_354\n"
      ".Lpr_355:\n"
      "buffer_load_dwordx4 v[148:151], v28, s[20:23], s80 offen offset:1024 sc1\n"
      "ds_read_b64_tr_b16 v[224:225], v4 offset:0\n"
      "ds_read_b64_tr_b16 v[226:227], v4 offset:1024\n"
      "s_waitcnt vmcnt(10)\n"
      "v_mfma_f32_32x32x16_bf16 v[208:223], v[156:159], v[44:47], v[208:223]\n"
      "ds_read_b64_tr_b16 v[228:229], v5 offset:2048\n"
      "ds_read_b64_tr_b16 v[230:231], v5 offset:3072\n"
      "ds_write_b128 v7, v[168:171]\n"
      "v_cmp_eq_u32 vcc, -1, v12\n"
      "v_mfma_f32_32x32x16_bf16 v[192:207], v[156:159], v[40:43], v[192:207]\n"
      "s_cbranch_vccnz .Lpk_357\n"
      ".Lpr_358:\n"
      "buffer_load_dwordx4 v[152:155], v28, s[20:23], s80 offen offset:2048 sc1\n"
      "ds_read_b64_tr_b16 v[232:233], v4 offset:2048\n"
      "ds_read_b64_tr_b16 v[234:235], v4 offset:3072\n"
      "s_waitcnt vmcnt(10) lgkmcnt(5)\n"
      "v_mfma_f32_32x32x16_bf16 v[208:223], v[12:15], v[240:243], v[208:223]\n"
      "ds_write_b128 v7, v[172:175] offset:1024\n"
      "v_cmp_eq_u32 vcc, -1, v244\n"
      "v_mfma_f32_32x32x16_bf16 v[192:207], v[12:15], v[224:227], v[192:207]\n"
      "s_cbranch_vccnz .Lpk_360\n"
      ".Lpr_361:\n"
      "buffer_load_dwordx4 v[156:159], v28, s[20:23], s80 offen offset:3072 sc1\n"
      "s_waitcnt vmcnt(10) lgkmcnt(1)\n"
      "v_mfma_f32_32x32x16_bf16 v[208:223], v[244:247], v[228:231], v[208:223]\n"
      "s_waitcnt lgkmcnt(0)\n"
      "s_barrier\n"
      "ds_read_b128 v[176:179], v10 offset:0\n"
      "ds_read_b128 v[180:183], v10 offset:1024\n"
      "ds_read_b128 v[184:187], v10 offset:2048\n"
      "ds_read_b128 v[188:191], v10 offset:3072\n"
      "v_cmp_eq_u32 vcc, -1, v248\n"
      "v_mfma_f32_32x32x16_bf16 v[192:207], v[244:247], v[232:235], v[192:207]\n"
      "s_cbranch_vccnz .Lpk_363\n"
      ".Lpr_364:\n"
      "s_add_u32 s80, s86, s19\n"
      "buffer_load_dwordx4 v[12:15], v28, s[20:23], s80 offen sc1\n"
      "ds_read_b128 v[168:171], v10 offset:4096\n"
      "ds_read_b128 v[172:175], v10 offset:5120\n"
      "s_waitcnt vmcnt(10)\n"
      "v_mfma_f32_32x32x16_bf16 v[208:223], v[248:251], v[52:55], v[208:223]\n"
      "ds_read_b64_tr_b16 v[232:233], v5 offset:16384\n"
      "ds_read_b64_tr_b16 v[234:235], v5 offset:17408\n"
      "v_cmp_eq_u32 vcc, -1, v252\n"
      "v_mfma_f32_32x32x16_bf16 v[192:207], v[248:251], v[48:51], v[192:207]\n"
      "s_cbranch_vccnz .Lpk_366\n"
      ".Lpr_367:\n"
      "buffer_load_dwordx4 v[244:247], v28, s[20:23], s80 offen offset:1024 sc1\n"
      "ds_read_b64_tr_b16 v[236:237], v4 offset:16384\n"
      "ds_read_b64_tr_b16 v[238:239], v4 offset:17408\n"
      "s_waitcnt lgkmcnt(9)\n"
      "v_add_f32 v160, v176, v160\n"
      "s_waitcnt vmcnt(10)\n"
      "v_mfma_f32_32x32x16_bf16 v[208:223], v[252:255], v[60:63], v[208:223]\n"
      "ds_read_b64_tr_b16 v[240:241], v5 offset:18432\n"
      "ds_read_b64_tr_b16 v[242:243], v5 offset:19456\n"
      "v_add_f32 v161, v177, v161\n"
      "v_add_f32 v162, v178, v162\n"
      "v_add_f32 v163, v179, v163\n"
      "v_cmp_eq_u32 vcc, -1, v128\n"
      "v_mfma_f32_32x32x16_bf16 v[192:207], v[252:255], v[56:59], v[192:207]\n"
      "s_cbranch_vccnz .Lpk_369\n"
      ".Lpr_370:\n"
      "buffer_load_dwordx4 v[248:251], v28, s[20:23], s80 offen offset:2048 sc1\n"
      "ds_read_b64_tr_b16 v[224:225], v4 offset:18432\n"
      "ds_read_b64_tr_b16 v[226:227], v4 offset:19456\n"
      "s_waitcnt lgkmcnt(12)\n"
      "v_add_f32 v164, v180, v164\n"
      "s_waitcnt vmcnt(10) lgkmcnt(4)\n"
      "v_mfma_f32_32x32x16_bf16 v[208:223], v[128:131], v[232:235], v[208:223]\n"
      "ds_read_b64_tr_b16 v[228:229], v3 offset:32768\n"
      "ds_read_b64_tr_b16 v[230:231], v3 offset:33792\n"
      "v_add_f32 v165, v181, v165\n"
      "v_add_f32 v166, v182, v166\n"
      "v_add_f32 v167, v183, v167\n"
      "v_cmp_eq_u32 vcc, -1, v132\n"
      "v_mfma_f32_32x32x16_bf16 v[192:207], v[128:131], v[236:239], v[192:207]\n"
      "s_cbranch_vccnz .Lpk_372\n"
      ".Lpr_373:\n"
      "buffer_load_dwordx4 v[252:255], v28, s[20:23], s80 offen offset:3072 sc1\n"
      "ds_read_b64_tr_b16 v[232:233], v2 offset:32768\n"
      "ds_read_b64_tr_b16 v[234:235], v2 offset:33792\n"
      "v_add_f32 v160, v184, v160\n"
      "s_waitcnt vmcnt(10) lgkmcnt(4)\n"
      "v_mfma_f32_32x32x16_bf16 v[208:223], v[132:135], v[240:243], v[208:223]\n"
      "ds_read_b64_tr_b16 v[236:237], v3 offset:34816\n"
      "ds_read_b64_tr_b16 v[238:239], v3 offset:35840\n"
      "v_add_f32 v161, v185, v161\n"
      "v_add_f32 v162, v186, v162\n"
      "v_add_f32 v163, v187, v163\n"
      "v_cmp_eq_u32 vcc, -1, v136\n"
      "v_mfma_f32_32x32x16_bf16 v[192:207], v[132:135], v[224:227], v[192:207]\n"
      "s_cbranch_vccnz .Lpk_375\n"
      ".Lpr_376:\n"
      "s_add_u32 s80, s86, s99\n"
      "buffer_load_dwordx4 v[128:131], v28, s[20:23], s80 offen sc1\n"
      "ds_read_b64_tr_b16 v[240:241], v2 offset:34816\n"
      "ds_read_b64_tr_b16 v[242:243], v2 offset:35840\n"
      "s_waitcnt vmcnt(10) lgkmcnt(4)\n"
      "v_mfma_f32_32x32x16_bf16 v[208:223], v[136:139], v[228:231], v[208:223]\n"
      "v_add_f32 v164, v188, v164\n"
      "v_add_f32 v165, v189, v165\n"
      "v_add_f32 v166, v190, v166\n"
      "v_add_f32 v167, v191, v167\n"
      "v_add_f32 v160, v168, v160\n"
      "v_cmp_eq_u32 vcc, -1, v140\n"
      "v_mfma_f32_32x32x16_bf16 v[192:207], v[136:139], v[232:235], v[192:207]\n"
      "s_cbranch_vccnz .Lpk_378\n"
      ".Lpr_379:\n"
      "buffer_load_dwordx4 v[132:135], v28, s[20:23], s80 offen offset:1024 sc1\n"
      "v_add_f32 v161, v169, v161\n"
      "v_add_f32 v162, v170, v162\n"
      "v_add_f32 v163, v171, v163\n"
      "s_waitcnt vmcnt(9) lgkmcnt(0)\n"
      "v_mfma_f32_32x32x16_bf16 v[208:223], v[140:143], v[236:239], v[208:223]\n"
      "v_add_f32 v164, v172, v164\n"
      "v_add_f32 v165, v173, v165\n"
      "v_add_f32 v166, v174, v166\n"
      "v_add_f32 v167, v175, v167\n"
      "v_cvt_pk_bf16_f32 v18, v160, v161\n"
      "v_cmp_eq_u32 vcc, -1, v144\n"
      "v_mfma_f32_32x32x16_bf16 v[192:207], v[140:143], v[240:243], v[192:207]\n"
      "s_cbranch_vccnz .Lpk_381\n"
      ".Lpr_382:\n"
      "buffer_load_dwordx4 v[136:139], v28, s[20:23], s80 offen offset:2048 sc1\n"
      "v_cvt_pk_bf16_f32 v19, v162, v163\n"
      "v_cvt_pk_bf16_f32 v20, v164, v165\n"
      "v_cvt_pk_bf16_f32 v21, v166, v167\n"
      "s_waitcnt vmcnt(9)\n"
      "v_mfma_f32_32x32x16_bf16 v[208:223], v[144:147], v[68:71], v[208:223]\n"
      "ds_read_b64_tr_b16 v[240:241], v3 offset:16384\n"
      "ds_read_b64_tr_b16 v[242:243], v3 offset:17408\n"
      "v_cmp_eq_u32 vcc, -1, v148\n"
      "v_mfma_f32_32x32x16_bf16 v[192:207], v[144:147], v[64:67], v[192:207]\n"
      "s_cbranch_vccnz .Lpk_384\n"
      ".Lpr_385:\n"
      "buffer_load_dwordx4 v[140:143], v28, s[20:23], s80 offen offset:3072 sc1\n"
      "ds_read_b64_tr_b16 v[224:225], v2 offset:16384\n"
      "ds_read_b64_tr_b16 v[226:227], v2 offset:17408\n"
      "s_cmp_lg_u32 s53, 0\n"
      "s_cbranch_scc1 .Lb2_tail\n"
      "s_waitcnt vmcnt(9)\n"
      "v_mfma_f32_32x32x16_bf16 v[208:223], v[148:151], v[76:79], v[208:223]\n"
      "ds_read_b64_tr_b16 v[228:229], v3 offset:18432\n"
      "ds_read_b64_tr_b16 v[230:231], v3 offset:19456\n"
      "v_cmp_ne_u32 s[94:95], s56, v11\n"
      "v_min_u32 v160, -2, v18\n"
      "v_min_u32 v161, -2, v19\n"
      "v_min_u32 v162, -2, v20\n"
      "v_min_u32 v163, -2, v21\n"
      "s_cmp_lg_u64 s[94:95], 0\n"
      "s_cbranch_scc1 .Lhf_429\n"
      ".Lhf_429r:\n"
      "buffer_store_dwordx4 v[160:163], v1, s[20:23], s63 offen sc1\n"
      "v_cmp_eq_u32 vcc, -1, v152\n"
      "v_mfma_f32_32x32x16_bf16 v[192:207], v[148:151], v[72:75], v[192:207]\n"
      "s_cbranch_vccnz .Lpk_432\n"
      ".Lpr_433:\n"
      "s_add_u32 s80, s86, s16\n"
      "buffer_load_dwordx4 v[144:147], v28, s[20:23], s80 offen sc1\n"
      "ds_read_b64_tr_b16 v[232:233], v2 offset:18432\n"
      "ds_read_b64_tr_b16 v[234:235], v2 offset:19456\n"
      "s_waitcnt vmcnt(10) lgkmcnt(4)\n"
      "v_mfma_f32_32x32x16_bf16 v[208:223], v[152:155], v[240:243], v[208:223]\n"
      "ds_read_b64_tr_b16 v[236:237], v3 offset:0\n"
      "ds_read_b64_tr_b16 v[238:239], v3 offset:1024\n"
      "s_add_u32 m0, s76, 0xc000\n"
      "s_add_u32 s78, s85, 0xc000\n"
      "buffer_load_dwordx4 v1, s[48:51], s78 offen sc1 lds\n"
      "v_cmp_eq_u32 vcc, -1, v156\n"
      "v_mfma_f32_32x32x16_bf16 v[192:207], v[152:155], v[224:227], v[192:207]\n"
      "s_cbranch_vccnz .Lpk_435\n"
      ".Lpr_436:\n"
      "buffer_load_dwordx4 v[148:151], v28, s[20:23], s80 offen offset:1024 sc1\n"
      "ds_read_b64_tr_b16 v[240:241], v2 offset:0\n"
      "ds_read_b64_tr_b16 v[242:243], v2 offset:1024\n"
      "s_waitcnt vmcnt(11) lgkmcnt(4)\n"
      "v_mfma_f32_32x32x16_bf16 v[208:223], v[156:159], v[228:231], v[208:223]\n"
      "ds_read_b64_tr_b16 v[224:225], v3 offset:2048\n"
      "ds_read_b64_tr_b16 v[226:227], v3 offset:3072\n"
      "buffer_load_dwordx4 v1, s[48:51], s78 offen offset:1024 sc1 lds\n"
      "v_cmp_eq_u32 vcc, -1, v12\n"
      "v_mfma_f32_32x32x16_bf16 v[192:207], v[156:159], v[232:235], v[192:207]\n"
      "s_cbranch_vccnz .Lpk_438\n"
      ".Lpr_439:\n"
      "buffer_load_dwordx4 v[152:155], v28, s[20:23], s80 offen offset:2048 sc1\n"
      "ds_read_b64_tr_b16 v[228:229], v2 offset:2048\n"
      "ds_read_b64_tr_b16 v[230:231], v2 offset:3072\n"
      "s_waitcnt vmcnt(12) lgkmcnt(4)\n"
      "v_mfma_f32_32x32x16_bf16 v[208:223], v[12:15], v[236:239], v[208:223]\n"
      "buffer_load_dwordx4 v1, s[48:51], s78 offen offset:2048 sc1 lds\n"
      "v_cmp_eq_u32 vcc, -1, v244\n"
      "v_mfma_f32_32x32x16_bf16 v[192:207], v[12:15], v[240:243], v[192:207]\n"
      "s_cbranch_vccnz .Lpk_441\n"
      ".Lpr_442:\n"
      "buffer_load_dwordx4 v[156:159], v28, s[20:23], s80 offen offset:3072 sc1\n"
      "s_waitcnt vmcnt(13) lgkmcnt(0)\n"
      "v_mfma_f32_32x32x16_bf16 v[208:223], v[244:247], v[224:227], v[208:223]\n"
      "buffer_load_dwordx4 v1, s[48:51], s78 offen offset:3072 sc1 lds\n"
      "v_cmp_eq_u32 vcc, -1, v248\n"
      "v_mfma_f32_32x32x16_bf16 v[192:207], v[244:247], v[228:231], v[192:207]\n"
      "s_cbranch_vccnz .Lpk_444\n"
      ".Lpr_445:\n"
      "s_waitcnt vmcnt(13)\n"
      "v_mfma_f32_32x32x16_bf16 v[208:223], v[248:251], v[84:87], v[208:223]\n"
      "ds_read_b64_tr_b16 v[228:229], v5 offset:32768\n"
      "ds_read_b64_tr_b16 v[230:231], v5 offset:33792\n"
      "s_add_u32 m0, s76, 0x10000\n"
      "s_add_u32 s78, s85, 0x10000\n"
      "buffer_load_dwordx4 v1, s[48:51], s78 offen sc1 lds\n"
      "v_cmp_eq_u32 vcc, -1, v252\n"
      "v_mfma_f32_32x32x16_bf16 v[192:207], v[248:251], v[80:83], v[192:207]\n"
      "s_cbranch_vccnz .Lpk_447\n"
      ".Lpr_448:\n"
      "ds_read_b64_tr_b16 v[232:233], v5 offset:34816\n"
      "ds_read_b64_tr_b16 v[234:235], v5 offset:35840\n"
      "s_waitcnt vmcnt(13)\n"
      "v_mfma_f32_32x32x16_bf16 v[208:223], v[252:255], v[92:95], v[208:223]\n"
      "buffer_load_dwordx4 v1, s[48:51], s78 offen offset:1024 sc1 lds\n"
      "v_cmp_eq_u32 vcc, -1, v128\n"
      "s_waitcnt vmcnt(13) lgkmcnt(2)\n"
      "v_mfma_f32_32x32x16_bf16 v[192:207], v[252:255], v[88:91], v[192:207]\n"
      "s_cbranch_vccnz .Lpk_450\n"
      ".Lpr_451:\n"
      "v_cmp_eq_u32 vcc, -1, v132\n"
      "s_waitcnt vmcnt(12) lgkmcnt(0)\n"
      "v_mfma_f32_32x32x16_bf16 v[208:223], v[128:131], v[228:231], v[208:223]\n"
      "s_cbranch_vccnz .Lpk_453\n"
      ".Lpr_454:\n"
      "ds_read_b64_tr_b16 v[224:225], v5 offset:49152\n"
      "ds_read_b64_tr_b16 v[226:227], v5 offset:50176\n"
      "buffer_load_dwordx4 v1, s[48:51], s78 offen offset:2048 sc1 lds\n"
      "s_barrier\n"
      "v_cmp_eq_u32 vcc, -1, v136\n"
      "s_waitcnt vmcnt(12)\n"
      "v_mfma_f32_32x32x16_bf16 v[208:223], v[132:135], v[232:235], v[208:223]\n"
      "s_cbranch_vccnz .Lpk_456\n"
      ".Lpr_457:\n"
      "ds_read_b64_tr_b16 v[228:229], v5 offset:51200\n"
      "ds_read_b64_tr_b16 v[230:231], v5 offset:52224\n"
      "v_cmp_eq_u32 vcc, -1, v140\n"
      "s_waitcnt vmcnt(10)\n"
      "v_mfma_f32_32x32x16_bf16 v[208:223], v[136:139], v[100:103], v[208:223]\n"
      "s_cbranch_vccnz .Lpk_459\n"
      ".Lpr_460:\n"
      "buffer_load_dwordx4 v1, s[48:51], s78 offen offset:3072 sc1 lds\n"
      "v_cmp_eq_u32 vcc, -1, v144\n"
      "s_waitcnt vmcnt(9) lgkmcnt(2)\n"
      "v_mfma_f32_32x32x16_bf16 v[208:223], v[140:143], v[108:111], v[208:223]\n"
      "s_cbranch_vccnz .Lpk_462\n"
      ".Lpr_463:\n"
      "v_cmp_eq_u32 vcc, -1, v148\n"
      "s_waitcnt vmcnt(7) lgkmcnt(0)\n"
      "v_mfma_f32_32x32x16_bf16 v[208:223], v[144:147], v[224:227], v[208:223]\n"
      "s_cbranch_vccnz .Lpk_465\n"
      ".Lpr_466:\n"
      "ds_read_b64_tr_b16 v[240:241], v4 offset:32768\n"
      "ds_read_b64_tr_b16 v[242:243], v4 offset:33792\n"
      "s_add_u32 m0, s76, 0x14000\n"
      "s_add_u32 s78, s85, 0x14000\n"
      "buffer_load_dwordx4 v1, s[48:51], s78 offen sc1 lds\n"
      "v_cmp_eq_u32 vcc, -1, v152\n"
      "s_waitcnt vmcnt(6)\n"
      "v_mfma_f32_32x32x16_bf16 v[208:223], v[148:151], v[228:231], v[208:223]\n"
      "s_cbranch_vccnz .Lpk_468\n"
      ".Lpr_469:\n"
      "ds_read_b64_tr_b16 v[224:225], v4 offset:34816\n"
      "ds_read_b64_tr_b16 v[226:227], v4 offset:35840\n"
      "v_cmp_eq_u32 vcc, -1, v156\n"
      "v_mfma_f32_32x32x16_bf16 v[208:223], v[152:155], v[116:119], v[208:223]\n"
      "s_cbranch_vccnz .Lpk_471\n"
      ".Lpr_472:\n"
      "buffer_load_dwordx4 v1, s[48:51], s78 offen offset:1024 sc1 lds\n"
      "v_mfma_f32_32x32x16_bf16 v[208:223], v[156:159], v[124:127], v[208:223]\n"
      "s_waitcnt lgkmcnt(2)\n"
      "v_mfma_f32_32x32x16_bf16 v[192:207], v[128:131], v[240:243], v[192:207]\n"
      "ds_read_b64_tr_b16 v[236:237], v4 offset:49152\n"
      "ds_read_b64_tr_b16 v[238:239], v4 offset:50176\n"
      "buffer_load_dwordx4 v1, s[48:51], s78 offen offset:2048 sc1 lds\n"
      "s_waitcnt lgkmcnt(2)\n"
      "v_mfma_f32_32x32x16_bf16 v[192:207], v[132:135], v[224:227], v[192:207]\n"
      "ds_read_b64_tr_b16 v[240:241], v4 offset:51200\n"
      "ds_read_b64_tr_b16 v[242:243], v4 offset:52224\n"
      "s_nop 2\n"
      "ds_write_b128 v9, v[216:219]\n"
      "v_fma_f32 v176, v208, v209, v210\n"
      "v_fma_f32 v177, v211, v212, v213\n"
      "v_mfma_f32_32x32x16_bf16 v[192:207], v[136:139], v[96:99], v[192:207]\n"
      "buffer_load_dwordx4 v1, s[48:51], s78 offen offset:3072 sc1 lds\n"
      "v_fma_f32 v178, v214, v215, v216\n"
      "v_fma_f32 v179, v217, v218, v219\n"
      "v_fma_f32 v180, v220, v221, v222\n"
      "v_fma_f32 v176, v176, v177, v178\n"
      "v_fma_f32 v179, v179, v180, v223\n"
      "v_mfma_f32_32x32x16_bf16 v[192:207], v[140:143], v[104:107], v[192:207]\n"
      "ds_write_b128 v9, v[220:223] offset:1024\n"
      "v_cmp_u_f32 vcc, v176, v179\n"
      "s_waitcnt lgkmcnt(4)\n"
      "v_mfma_f32_32x32x16_bf16 v[192:207], v[144:147], v[236:239], v[192:207]\n"
      "s_add_u32 m0, s76, 0x8000\n"
      "s_add_u32 s78, s85, 0x8000\n"
      "buffer_load_dwordx4 v1, s[48:51], s78 offen sc1 lds\n"
      "s_waitcnt lgkmcnt(2)\n"
      "v_mfma_f32_32x32x16_bf16 v[192:207], v[148:151], v[240:243], v[192:207]\n"
      "ds_write_b128 v8, v[208:211]\n"
      "v_mfma_f32_32x32x16_bf16 v[192:207], v[152:155], v[112:115], v[192:207]\n"
      "buffer_load_dwordx4 v1, s[48:51], s78 offen offset:1024 sc1 lds\n"
      "v_mfma_f32_32x32x16_bf16 v[192:207], v[156:159], v[120:123], v[192:207]\n"
      "ds_write_b128 v8, v[212:215] offset:1024\n"
      ".Lb2_tail_done:\n"
      "s_cbranch_vccnz .Lb2_fix1\n"
      ".Lb2_ok1:\n"
      "s_cmp_lg_u32 s53, 0\n"
      "s_cbranch_scc1 .Lfinal\n"
      "buffer_load_dwordx4 v1, s[48:51], s78 offen offset:2048 sc1 lds\n"
      "buffer_load_dwordx4 v1, s[48:51], s78 offen offset:3072 sc1 lds\n"
      "s_add_u32 s58, s58, 0x808000\n"
      "s_add_u32 s59, s59, 0x808000\n"
      "s_add_u32 s61, s61, 0x808000\n"
      "s_add_u32 s62, s62, 0x808000\n"
      "s_add_u32 s65, s65, 0x808000\n"
      "s_add_u32 s66, s66, 0x808000\n"
      "s_add_u32 s17, s17, 0x808000\n"
      "s_add_u32 s67, s67, 0x808000\n"
      "s_add_u32 s68, s68, 0x808000\n"
      "s_add_u32 s69, s69, 0x808000\n"
      "s_add_u32 s71, s71, 0x808000\n"
      "ds_write_b128 v7, v[200:203]\n"
      "ds_write_b128 v7, v[204:207] offset:1024\n"
      "v_mov_b32 v230, 0\n"
      "v_mov_b32 v231, 0\n"
      "v_dot2c_f32_bf16 v230, s26, v18\n"
      "v_dot2c_f32_bf16 v231, v18, v18\n"
      "v_dot2c_f32_bf16 v230, s26, v19\n"
      "v_dot2c_f32_bf16 v231, v19, v19\n"
      "v_dot2c_f32_bf16 v230, s26, v20\n"
      "v_dot2c_f32_bf16 v231, v20, v20\n"
      "v_dot2c_f32_bf16 v230, s26, v21\n"
      "v_dot2c_f32_bf16 v231, v21, v21\n"
      "s_waitcnt lgkmcnt(0)\n"
      "s_barrier\n"
      "ds_read_b128 v[208:211], v10 offset:0\n"
      "ds_read_b128 v[212:215], v10 offset:1024\n"
      "ds_read_b128 v[216:219], v10 offset:2048\n"
      "ds_read_b128 v[220:223], v10 offset:3072\n"
      "ds_read_b128 v[200:203], v10 offset:4096\n"
      "ds_read_b128 v[204:207], v10 offset:5120\n"
      "s_add_u32 s84, s70, 0x808000\n"
      "buffer_load_dwordx4 v[160:163], v1, s[20:23], s84 offen sc1\n"
      "buffer_load_dwordx4 v[164:167], v1, s[20:23], s84 offen offset:1024 sc1\n"
      "s_waitcnt lgkmcnt(5)\n"
      "v_pk_add_f32 v[192:193], v[192:193], v[208:209]\n"
      "v_pk_add_f32 v[194:195], v[194:195], v[210:211]\n"
      "s_waitcnt lgkmcnt(4)\n"
      "v_pk_add_f32 v[196:197], v[196:197], v[212:213]\n"
      "s_add_u32 s84, s70, 0x809000\n"
      "buffer_load_dwordx4 v[168:171], v1, s[20:23], s84 offen sc1\n"
      "v_pk_add_f32 v[198:199], v[198:199], v[214:215]\n"
      "s_waitcnt lgkmcnt(3)\n"
      "v_pk_add_f32 v[192:193], v[192:193], v[216:217]\n"
      "v_pk_add_f32 v[194:195], v[194:195], v[218:219]\n"
      "s_waitcnt lgkmcnt(2)\n"
      "v_pk_add_f32 v[196:197], v[196:197], v[220:221]\n"
      "buffer_load_dwordx4 v[172:175], v1, s[20:23], s84 offen offset:1024 sc1\n"
      "v_pk_add_f32 v[198:199], v[198:199], v[222:223]\n"
      "s_waitcnt lgkmcnt(1)\n"
      "v_pk_add_f32 v[192:193], v[192:193], v[200:201]\n"
      "v_pk_add_f32 v[194:195], v[194:195], v[202:203]\n"
      "s_waitcnt lgkmcnt(0)\n"
      "v_pk_add_f32 v[196:197], v[196:197], v[204:205]\n"
      "s_add_u32 s84, s70, 0x80a000\n"
      "buffer_load_dwordx4 v[176:179], v1, s[20:23], s84 offen sc1\n"
      "v_pk_add_f32 v[198:199], v[198:199], v[206:207]\n"
      "v_cvt_pk_bf16_f32 v22, v192, v193\n"
      "v_cvt_pk_bf16_f32 v23, v194, v195\n"
      "v_cvt_pk_bf16_f32 v24, v196, v197\n"
      "v_cvt_pk_bf16_f32 v25, v198, v199\n"
      "v_min_u32 v224, -2, v22\n"
      "v_min_u32 v225, -2, v23\n"
      "v_min_u32 v226, -2, v24\n"
      "v_min_u32 v227, -2, v25\n"
      "buffer_store_dwordx4 v[224:227], v1, s[20:23], s63 offen offset:2048 sc1\n"
      "v_dot2c_f32_bf16 v230, s26, v22\n"
      "v_dot2c_f32_bf16 v231, v22, v22\n"
      "v_dot2c_f32_bf16 v230, s26, v23\n"
      "v_dot2c_f32_bf16 v231, v23, v23\n"
      "v_dot2c_f32_bf16 v230, s26, v24\n"
      "v_dot2c_f32_bf16 v231, v24, v24\n"
      "v_dot2c_f32_bf16 v230, s26, v25\n"
      "v_dot2c_f32_bf16 v231, v25, v25\n"
      "s_add_u32 s81, s60, 0xa08000\n"
      "s_nop 0\n"
      "v_mov_b32 v228, v230\n"
      "v_mov_b32 v229, v231\n"
      "s_nop 0\n"
      "v_permlane32_swap_b32 v230, v228\n"
      "v_permlane32_swap_b32 v231, v229\n"
      "v_pk_add_f32 v[230:231], v[230:231], v[228:229]\n"
      "v_min_u32 v230, -2, v230\n"
      "v_min_u32 v231, -2, v231\n"
      "s_mov_b32 exec_hi, 0\n"
      "buffer_store_dwordx2 v[230:231], v27, s[20:23], s81 offen sc1\n"
      "s_mov_b32 exec_hi, -1\n"
      "buffer_load_dwordx4 v[180:183], v1, s[20:23], s84 offen offset:1024 sc1\n"
      "s_add_u32 s84, s70, 0x80b000\n"
      "buffer_load_dwordx4 v[184:187], v1, s[20:23], s84 offen sc1\n"
      "s_waitcnt vmcnt(9)\n"
      ".Lnext_layer:\n"
      "s_add_u32 s60, s60, 0x808000\n"
      "s_add_u32 s70, s70, 0x808000\n"
      "s_mov_b32 s53, 1\n"
      "s_mov_b32 s80, s84\n"
      "s_barrier\n"
      "s_branch .Llayer\n"
      ".Lfinal:\n"
      "v_and_b32 v224, 31, v0\n"
      "v_mul_u32_u24 v224, 0x380, v224\n"
      "v_lshrrev_b32 v225, 5, v0\n"
      "v_lshl_add_u32 v224, v225, 4, v224\n"
      "s_nop 3\n"
      "ds_write_b128 v7, v[200:203]\n"
      "ds_write_b128 v7, v[204:207] offset:1024\n"
      "s_waitcnt lgkmcnt(0)\n"
      "s_barrier\n"
      "ds_read_b128 v[208:211], v10 offset:0\n"
      "ds_read_b128 v[212:215], v10 offset:1024\n"
      "ds_read_b128 v[216:219], v10 offset:2048\n"
      "ds_read_b128 v[220:223], v10 offset:3072\n"
      "ds_read_b128 v[200:203], v10 offset:4096\n"
      "ds_read_b128 v[204:207], v10 offset:5120\n"
      "s_waitcnt lgkmcnt(5)\n"
      "v_pk_add_f32 v[192:193], v[192:193], v[208:209]\n"
      "v_pk_add_f32 v[194:195], v[194:195], v[210:211]\n"
      "s_waitcnt lgkmcnt(4)\n"
      "v_pk_add_f32 v[196:197], v[196:197], v[212:213]\n"
      "v_pk_add_f32 v[198:199], v[198:199], v[214:215]\n"
      "s_waitcnt lgkmcnt(3)\n"
      "v_pk_add_f32 v[192:193], v[192:193], v[216:217]\n"
      "v_pk_add_f32 v[194:195], v[194:195], v[218:219]\n"
      "s_waitcnt lgkmcnt(2)\n"
      "v_pk_add_f32 v[196:197], v[196:197], v[220:221]\n"
      "v_pk_add_f32 v[198:199], v[198:199], v[222:223]\n"
      "s_waitcnt lgkmcnt(1)\n"
      "v_pk_add_f32 v[192:193], v[192:193], v[200:201]\n"
      "v_pk_add_f32 v[194:195], v[194:195], v[202:203]\n"
      "s_waitcnt lgkmcnt(0)\n"
      "v_pk_add_f32 v[196:197], v[196:197], v[204:205]\n"
      "v_pk_add_f32 v[198:199], v[198:199], v[206:207]\n"
      "v_cvt_pk_bf16_f32 v22, v192, v193\n"
      "v_cvt_pk_bf16_f32 v23, v194, v195\n"
      "v_cvt_pk_bf16_f32 v24, v196, v197\n"
      "v_cvt_pk_bf16_f32 v25, v198, v199\n"
      "s_nop 0\n"
      "v_permlane32_swap_b32 v22, v24\n"
      "v_permlane32_swap_b32 v23, v25\n"
      "buffer_store_dwordx4 v[22:25], v224, s[44:47], s96 offen offset:64 \n"
      "s_or_b32 s81, s18, s52\n"
      "s_cbranch_scc1 .Lend\n"
      "v_mov_b32 v204, s56\n"
      "s_mov_b64 exec, 1\n"
      "buffer_store_dword v204, off, s[20:23], 0 sc1\n"
      "s_mov_b64 exec, -1\n"
      ".Lend:\n"
      "s_endpgm\n"
      ".Lno_w1:\n"
      "s_mov_b32 s38, 0\n"
      "s_branch .Lw1_early\n"
      ".Lno_w1b:\n"
      "s_lshr_b32 s81, s18, 6\n"
      "s_bfe_u32 s82, s18, 0x20003\n"
      "s_and_b32 s83, s18, 1\n"
      "s_add_u32 s82, s82, s83\n"
      "s_add_u32 s82, s82, 3\n"
      "s_cmp_eq_u32 s54, 0\n"
      "s_cselect_b32 s81, s82, s81\n"
      "s_mul_i32 s83, s83, 7\n"
      "s_add_u32 s81, s81, s83\n"
      "s_bfe_u32 s82, s18, 0x20001\n"
      "s_mul_i32 s82, s82, 15\n"
      "s_add_u32 s81, s81, s82\n"
      "s_lshl2_add_u32 s81, s81, s52\n"
      "s_mul_i32 s81, s81, 0x3800\n"
      "s_add_u32 s81, s81, 0x739000\n"
      "buffer_store_dwordx4 v[212:215], v1, s[20:23], s81 offen sc1\n"
      "buffer_store_dwordx4 v[212:215], v1, s[20:23], s81 offen offset:1024 sc1\n"
      "buffer_store_dwordx4 v[212:215], v1, s[20:23], s81 offen offset:2048 sc1\n"
      "buffer_store_dwordx4 v[212:215], v1, s[20:23], s81 offen offset:3072 sc1\n"
      "s_add_u32 s81, s81, 0x1000\n"
      "buffer_store_dwordx4 v[212:215], v1, s[20:23], s81 offen sc1\n"
      "buffer_store_dwordx4 v[212:215], v1, s[20:23], s81 offen offset:1024 sc1\n"
      "buffer_store_dwordx4 v[212:215], v1, s[20:23], s81 offen offset:2048 sc1\n"
      "buffer_store_dwordx4 v[212:215], v1, s[20:23], s81 offen offset:3072 sc1\n"
      "s_add_u32 s81, s81, 0x1000\n"
      "buffer_store_dwordx4 v[212:215], v1, s[20:23], s81 offen sc1\n"
      "buffer_store_dwordx4 v[212:215], v1, s[20:23], s81 offen offset:1024 sc1\n"
      "buffer_store_dwordx4 v[212:215], v1, s[20:23], s81 offen offset:2048 sc1\n"
      "buffer_store_dwordx4 v[212:215], v1, s[20:23], s81 offen offset:3072 sc1\n"
      "s_add_u32 s81, s81, 0x1000\n"
      "buffer_store_dwordx4 v[212:215], v1, s[20:23], s81 offen sc1\n"
      "buffer_store_dwordx4 v[212:215], v1, s[20:23], s81 offen offset:1024 sc1\n"
      "s_waitcnt vmcnt(0)\n"
      "s_barrier\n"
      "s_cmp_lg_u32 s52, 0\n"
      "s_cbranch_scc1 .Lnof_4\n"
      "s_mov_b64 exec, 7\n"
      "buffer_store_dword v209, v210, s[20:23], 0 offen sc1\n"
      "s_mov_b64 exec, -1\n"
      ".Lnof_4:\n"
      "s_lshr_b32 s81, s18, 6\n"
      "s_bfe_u32 s82, s18, 0x20003\n"
      "s_add_u32 s82, s82, 3\n"
      "s_cmp_eq_u32 s54, 0\n"
      "s_cselect_b32 s81, s82, s81\n"
      "s_lshl2_add_u32 s81, s81, s52\n"
      "s_mul_i32 s83, s81, 0x3800\n"
      "s_and_b32 s82, s18, 7\n"
      "s_mul_i32 s82, s82, 0x700\n"
      "s_add_u32 s83, s83, s82\n"
      "v_min_u32 v218, 13, v0\n"
      "v_lshlrev_b32 v218, 7, v218\n"
      "buffer_load_dword v224, v218, s[32:35], s83 offen\n"
      "s_lshl_b32 s81, s81, 18\n"
      "s_and_b32 s82, s18, 7\n"
      "s_lshl_b32 s82, s82, 9\n"
      "s_add_u32 s81, s81, s82\n"
      "v_lshrrev_b32 v216, 2, v0\n"
      "v_and_b32 v217, 3, v0\n"
      "v_lshlrev_b32 v217, 7, v217\n"
      "v_lshl_add_u32 v216, v216, 12, v217\n"
      "buffer_load_dword v220, v216, s[40:43], s81 offen\n"
      "s_add_u32 s81, s81, 0x10000\n"
      "buffer_load_dword v221, v216, s[40:43], s81 offen\n"
      "s_add_u32 s81, s81, 0x10000\n"
      "buffer_load_dword v222, v216, s[40:43], s81 offen\n"
      "s_add_u32 s81, s81, 0x10000\n"
      "buffer_load_dword v223, v216, s[40:43], s81 offen\n"
      "s_add_u32 s81, s81, 0x10000\n"
      ".Lfl_wait:\n"
      "buffer_load_dword v216, v211, s[20:23], 0 offen sc1\n"
      "s_waitcnt vmcnt(0)\n"
      "v_cmp_ne_u32 vcc, s56, v216\n"
      "s_sub_u32 s79, s79, 1\n"
      "s_cmp_lt_i32 s79, 0\n"
      "s_cbranch_scc1 .Lfl_ok\n"
      "s_cbranch_vccnz .Lfl_wait\n"
      "s_branch .Lfl_ok\n"
      ".Lb1_skip:\n"
      "v_and_b32 v200, 31, v0\n"
      "v_lshlrev_b32 v200, 7, v200\n"
      "s_add_u32 s83, s68, 0x20000\n"
      "v_add_u32 v201, s6, v200\n"
      "s_mov_b32 exec_lo, 0\n"
      "v_add_u32 v201, s7, v200\n"
      "s_mov_b32 exec_lo, -1\n"
      "buffer_load_dword v204, v201, s[20:23], s83 offen sc1\n"
      "v_add_u32 v201, s8, v200\n"
      "s_mov_b32 exec_lo, 0\n"
      "v_add_u32 v201, s9, v200\n"
      "s_mov_b32 exec_lo, -1\n"
      "buffer_load_dword v205, v201, s[20:23], s83 offen sc1\n"
      "s_mul_i32 s78, s11, 64\n"
      "s_add_u32 s78, s78, s75\n"
      "s_add_u32 m0, s76, 0x10000\n"
      "s_nop 0\n"
      "buffer_load_dwordx4 v6, s[40:43], s78 offen lds\n"
      "s_add_u32 s78, s78, 0x7c00\n"
      "buffer_load_dwordx4 v6, s[40:43], s78 offen offset:1024 lds\n"
      "s_add_u32 s78, s78, 0x7c00\n"
      "buffer_load_dwordx4 v6, s[40:43], s78 offen offset:2048 lds\n"
      "s_add_u32 s78, s78, 0x7c00\n"
      "buffer_load_dwordx4 v6, s[40:43], s78 offen offset:3072 lds\n"
      "s_mul_i32 s78, s12, 64\n"
      "s_add_u32 s78, s78, s75\n"
      "s_add_u32 m0, s76, 0x14000\n"
      "s_nop 0\n"
      "buffer_load_dwordx4 v6, s[40:43], s78 offen lds\n"
      "s_add_u32 s78, s78, 0x7c00\n"
      "buffer_load_dwordx4 v6, s[40:43], s78 offen offset:1024 lds\n"
      "s_add_u32 s78, s78, 0x7c00\n"
      "buffer_load_dwordx4 v6, s[40:43], s78 offen offset:2048 lds\n"
      "s_add_u32 s78, s78, 0x7c00\n"
      "buffer_load_dwordx4 v6, s[40:43], s78 offen offset:3072 lds\n"
      "s_cmp_eq_u32 s53, 0\n"
      "s_cbranch_scc1 .Lw2_load\n"
      ".Lb1_skip2:\n"
      "s_sleep 40\n"
      ".Lb1_skip3:\n"
      "s_waitcnt lgkmcnt(1)\n"
      "v_min_u32 v160, 0, v0\n"
      "v_mul_u32_u24 v160, 0x400, v160\n"
      ".Lcan_255:\n"
      "s_sleep 6\n"
      "buffer_load_dword v161, v160, s[20:23], s69 offen sc1\n"
      "s_waitcnt vmcnt(0)\n"
      "v_cmp_eq_u32 vcc, -1, v161\n"
      "s_sub_u32 s79, s79, 1\n"
      "s_cmp_lt_i32 s79, 0\n"
      "s_cbranch_scc1 .Lcand_256\n"
      "s_cbranch_vccnz .Lcan_255\n"
      ".Lcand_256:\n"
      "v_min_u32 v160, 27, v0\n"
      "v_mul_u32_u24 v160, 0x400, v160\n"
      ".Lcan_257:\n"
      "buffer_load_dword v161, v160, s[20:23], s69 offen sc1\n"
      "s_waitcnt vmcnt(0)\n"
      "v_cmp_eq_u32 vcc, -1, v161\n"
      "s_sub_u32 s79, s79, 1\n"
      "s_cmp_lt_i32 s79, 0\n"
      "s_cbranch_scc1 .Lcand_258\n"
      "s_cbranch_vccnz .Lcan_257\n"
      ".Lcand_258:\n"
      "s_add_u32 s80, s69, s10\n"
      "buffer_load_dwordx4 v[128:131], v28, s[20:23], s80 offen sc1\n"
      "buffer_load_dwordx4 v[132:135], v28, s[20:23], s80 offen offset:1024 sc1\n"
      "buffer_load_dwordx4 v[136:139], v28, s[20:23], s80 offen offset:2048 sc1\n"
      "buffer_load_dwordx4 v[140:143], v28, s[20:23], s80 offen offset:3072 sc1\n"
      "s_add_u32 s80, s69, s11\n"
      "buffer_load_dwordx4 v[144:147], v28, s[20:23], s80 offen sc1\n"
      "v_lshlrev_b32 v160, 16, v18\n"
      "v_and_b32 v161, 0xffff0000, v18\n"
      "v_lshlrev_b32 v162, 16, v19\n"
      "v_and_b32 v163, 0xffff0000, v19\n"
      "s_waitcnt lgkmcnt(0)\n"
      "v_lshlrev_b32 v164, 16, v20\n"
      "v_and_b32 v165, 0xffff0000, v20\n"
      "v_lshlrev_b32 v166, 16, v21\n"
      "v_and_b32 v167, 0xffff0000, v21\n"
      "v_pk_add_f32 v[160:161], v[160:161], v[26:27] op_sel_hi:[1,0]\n"
      "v_pk_add_f32 v[162:163], v[162:163], v[26:27] op_sel_hi:[1,0]\n"
      "v_pk_add_f32 v[164:165], v[164:165], v[26:27] op_sel_hi:[1,0]\n"
      "v_pk_add_f32 v[166:167], v[166:167], v[26:27] op_sel_hi:[1,0]\n"
      "v_mov_b32 v168, 0\n"
      "v_mov_b32 v169, 0\n"
      "v_mov_b32 v170, 0\n"
      "v_mov_b32 v171, 0\n"
      "v_mov_b32 v172, 0\n"
      "v_mov_b32 v173, 0\n"
      "v_mov_b32 v174, 0\n"
      "v_mov_b32 v175, 0\n"
      "s_add_u32 s86, s69, 0x1c000\n"
      "s_mov_b32 s48, s20\n"
      "s_mov_b32 s49, s21\n"
      "s_add_u32 s50, s72, 0x15000\n"
      "s_add_u32 s85, s72, s76\n"
      "s_waitcnt vmcnt(0)\n"
      "s_branch .Lb2_start\n"
      ".Lw2_load:\n"
      "s_branch .Lb1_skip2\n"
      ".Lb2_tail:\n"
      "v_and_b32 v11, 31, v0\n"
      "v_mul_u32_u24 v11, 0x380, v11\n"
      "v_lshrrev_b32 v160, 5, v0\n"
      "v_lshl_add_u32 v11, v160, 4, v11\n"
      "v_permlane32_swap_b32 v18, v20\n"
      "v_permlane32_swap_b32 v19, v21\n"
      "buffer_store_dwordx4 v[18:21], v11, s[44:47], s96 offen \n"
      "s_waitcnt vmcnt(10)\n"
      "v_mfma_f32_32x32x16_bf16 v[208:223], v[148:151], v[76:79], v[208:223]\n"
      "ds_read_b64_tr_b16 v[228:229], v3 offset:18432\n"
      "ds_read_b64_tr_b16 v[230:231], v3 offset:19456\n"
      "v_cmp_eq_u32 vcc, -1, v152\n"
      "v_mfma_f32_32x32x16_bf16 v[192:207], v[148:151], v[72:75], v[192:207]\n"
      "s_cbranch_vccnz .Lpk_387\n"
      ".Lpr_388:\n"
      "s_add_u32 s80, s86, s16\n"
      "buffer_load_dwordx4 v[144:147], v28, s[20:23], s80 offen sc1\n"
      "ds_read_b64_tr_b16 v[232:233], v2 offset:18432\n"
      "ds_read_b64_tr_b16 v[234:235], v2 offset:19456\n"
      "s_waitcnt vmcnt(10) lgkmcnt(4)\n"
      "v_mfma_f32_32x32x16_bf16 v[208:223], v[152:155], v[240:243], v[208:223]\n"
      "ds_read_b64_tr_b16 v[236:237], v3 offset:0\n"
      "ds_read_b64_tr_b16 v[238:239], v3 offset:1024\n"
      "v_cmp_eq_u32 vcc, -1, v156\n"
      "v_mfma_f32_32x32x16_bf16 v[192:207], v[152:155], v[224:227], v[192:207]\n"
      "s_cbranch_vccnz .Lpk_390\n"
      ".Lpr_391:\n"
      "buffer_load_dwordx4 v[148:151], v28, s[20:23], s80 offen offset:1024 sc1\n"
      "ds_read_b64_tr_b16 v[240:241], v2 offset:0\n"
      "ds_read_b64_tr_b16 v[242:243], v2 offset:1024\n"
      "s_waitcnt vmcnt(10) lgkmcnt(4)\n"
      "v_mfma_f32_32x32x16_bf16 v[208:223], v[156:159], v[228:231], v[208:223]\n"
      "ds_read_b64_tr_b16 v[224:225], v3 offset:2048\n"
      "ds_read_b64_tr_b16 v[226:227], v3 offset:3072\n"
      "v_cmp_eq_u32 vcc, -1, v12\n"
      "v_mfma_f32_32x32x16_bf16 v[192:207], v[156:159], v[232:235], v[192:207]\n"
      "s_cbranch_vccnz .Lpk_393\n"
      ".Lpr_394:\n"
      "buffer_load_dwordx4 v[152:155], v28, s[20:23], s80 offen offset:2048 sc1\n"
      "ds_read_b64_tr_b16 v[228:229], v2 offset:2048\n"
      "ds_read_b64_tr_b16 v[230:231], v2 offset:3072\n"
      "s_waitcnt vmcnt(10) lgkmcnt(4)\n"
      "v_mfma_f32_32x32x16_bf16 v[208:223], v[12:15], v[236:239], v[208:223]\n"
      "v_cmp_eq_u32 vcc, -1, v244\n"
      "v_mfma_f32_32x32x16_bf16 v[192:207], v[12:15], v[240:243], v[192:207]\n"
      "s_cbranch_vccnz .Lpk_396\n"
      ".Lpr_397:\n"
      "buffer_load_dwordx4 v[156:159], v28, s[20:23], s80 offen offset:3072 sc1\n"
      "s_waitcnt vmcnt(10) lgkmcnt(0)\n"
      "v_mfma_f32_32x32x16_bf16 v[208:223], v[244:247], v[224:227], v[208:223]\n"
      "v_cmp_eq_u32 vcc, -1, v248\n"
      "v_mfma_f32_32x32x16_bf16 v[192:207], v[244:247], v[228:231], v[192:207]\n"
      "s_cbranch_vccnz .Lpk_399\n"
      ".Lpr_400:\n"
      "s_waitcnt vmcnt(9)\n"
      "v_mfma_f32_32x32x16_bf16 v[208:223], v[248:251], v[84:87], v[208:223]\n"
      "ds_read_b64_tr_b16 v[228:229], v5 offset:32768\n"
      "ds_read_b64_tr_b16 v[230:231], v5 offset:33792\n"
      "v_cmp_eq_u32 vcc, -1, v252\n"
      "v_mfma_f32_32x32x16_bf16 v[192:207], v[248:251], v[80:83], v[192:207]\n"
      "s_cbranch_vccnz .Lpk_402\n"
      ".Lpr_403:\n"
      "ds_read_b64_tr_b16 v[232:233], v5 offset:34816\n"
      "ds_read_b64_tr_b16 v[234:235], v5 offset:35840\n"
      "s_waitcnt vmcnt(8)\n"
      "v_mfma_f32_32x32x16_bf16 v[208:223], v[252:255], v[92:95], v[208:223]\n"
      "v_cmp_eq_u32 vcc, -1, v128\n"
      "s_waitcnt vmcnt(7) lgkmcnt(2)\n"
      "v_mfma_f32_32x32x16_bf16 v[192:207], v[252:255], v[88:91], v[192:207]\n"
      "s_cbranch_vccnz .Lpk_405\n"
      ".Lpr_406:\n"
      "v_cmp_eq_u32 vcc, -1, v132\n"
      "s_waitcnt vmcnt(6) lgkmcnt(0)\n"
      "v_mfma_f32_32x32x16_bf16 v[208:223], v[128:131], v[228:231], v[208:223]\n"
      "s_cbranch_vccnz .Lpk_408\n"
      ".Lpr_409:\n"
      "ds_read_b64_tr_b16 v[224:225], v5 offset:49152\n"
      "ds_read_b64_tr_b16 v[226:227], v5 offset:50176\n"
      "s_barrier\n"
      "v_cmp_eq_u32 vcc, -1, v136\n"
      "s_waitcnt vmcnt(5)\n"
      "v_mfma_f32_32x32x16_bf16 v[208:223], v[132:135], v[232:235], v[208:223]\n"
      "s_cbranch_vccnz .Lpk_411\n"
      ".Lpr_412:\n"
      "ds_read_b64_tr_b16 v[228:229], v5 offset:51200\n"
      "ds_read_b64_tr_b16 v[230:231], v5 offset:52224\n"
      "v_cmp_eq_u32 vcc, -1, v140\n"
      "s_waitcnt vmcnt(3)\n"
      "v_mfma_f32_32x32x16_bf16 v[208:223], v[136:139], v[100:103], v[208:223]\n"
      "s_cbranch_vccnz .Lpk_414\n"
      ".Lpr_415:\n"
      "v_cmp_eq_u32 vcc, -1, v144\n"
      "s_waitcnt vmcnt(2) lgkmcnt(2)\n"
      "v_mfma_f32_32x32x16_bf16 v[208:223], v[140:143], v[108:111], v[208:223]\n"
      "s_cbranch_vccnz .Lpk_417\n"
      ".Lpr_418:\n"
      "v_cmp_eq_u32 vcc, -1, v148\n"
      "s_waitcnt vmcnt(1) lgkmcnt(0)\n"
      "v_mfma_f32_32x32x16_bf16 v[208:223], v[144:147], v[224:227], v[208:223]\n"
      "s_cbranch_vccnz .Lpk_420\n"
      ".Lpr_421:\n"
      "ds_read_b64_tr_b16 v[240:241], v4 offset:32768\n"
      "ds_read_b64_tr_b16 v[242:243], v4 offset:33792\n"
      "v_cmp_eq_u32 vcc, -1, v152\n"
      "s_waitcnt vmcnt(0)\n"
      "v_mfma_f32_32x32x16_bf16 v[208:223], v[148:151], v[228:231], v[208:223]\n"
      "s_cbranch_vccnz .Lpk_423\n"
      ".Lpr_424:\n"
      "ds_read_b64_tr_b16 v[224:225], v4 offset:34816\n"
      "ds_read_b64_tr_b16 v[226:227], v4 offset:35840\n"
      "v_cmp_eq_u32 vcc, -1, v156\n"
      "v_mfma_f32_32x32x16_bf16 v[208:223], v[152:155], v[116:119], v[208:223]\n"
      "s_cbranch_vccnz .Lpk_426\n"
      ".Lpr_427:\n"
      "v_mfma_f32_32x32x16_bf16 v[208:223], v[156:159], v[124:127], v[208:223]\n"
      "s_waitcnt lgkmcnt(2)\n"
      "v_mfma_f32_32x32x16_bf16 v[192:207], v[128:131], v[240:243], v[192:207]\n"
      "ds_read_b64_tr_b16 v[236:237], v4 offset:49152\n"
      "ds_read_b64_tr_b16 v[238:239], v4 offset:50176\n"
      "s_waitcnt lgkmcnt(2)\n"
      "v_mfma_f32_32x32x16_bf16 v[192:207], v[132:135], v[224:227], v[192:207]\n"
      "ds_read_b64_tr_b16 v[240:241], v4 offset:51200\n"
      "ds_read_b64_tr_b16 v[242:243], v4 offset:52224\n"
      "s_nop 3\n"
      "ds_write_b128 v9, v[216:219]\n"
      "v_fma_f32 v176, v208, v209, v210\n"
      "v_fma_f32 v177, v211, v212, v213\n"
      "v_mfma_f32_32x32x16_bf16 v[192:207], v[136:139], v[96:99], v[192:207]\n"
      "v_fma_f32 v178, v214, v215, v216\n"
      "v_fma_f32 v179, v217, v218, v219\n"
      "v_fma_f32 v180, v220, v221, v222\n"
      "v_fma_f32 v176, v176, v177, v178\n"
      "v_fma_f32 v179, v179, v180, v223\n"
      "v_cmp_u_f32 vcc, v176, v179\n"
      "v_mfma_f32_32x32x16_bf16 v[192:207], v[140:143], v[104:107], v[192:207]\n"
      "ds_write_b128 v9, v[220:223] offset:1024\n"
      "s_waitcnt lgkmcnt(4)\n"
      "v_mfma_f32_32x32x16_bf16 v[192:207], v[144:147], v[236:239], v[192:207]\n"
      "s_waitcnt lgkmcnt(2)\n"
      "v_mfma_f32_32x32x16_bf16 v[192:207], v[148:151], v[240:243], v[192:207]\n"
      "ds_write_b128 v8, v[208:211]\n"
      "v_mfma_f32_32x32x16_bf16 v[192:207], v[152:155], v[112:115], v[192:207]\n"
      "v_mfma_f32_32x32x16_bf16 v[192:207], v[156:159], v[120:123], v[192:207]\n"
      "ds_write_b128 v8, v[212:215] offset:1024\n"
      "s_branch .Lb2_tail_done\n"
      ".Lhelper:\n"
      "s_sub_u32 s86, s18, 0xe0\n"
      "s_lshl2_add_u32 s86, s86, s52\n"
      "v_mov_b32 v128, -1\n"
      "v_mov_b32 v129, -1\n"
      "v_mov_b32 v130, -1\n"
      "v_mov_b32 v131, -1\n"
      "v_mov_b32 v132, s56\n"
      "s_cmp_lg_u32 s53, 0\n"
      "s_cbranch_scc1 .Lh5_476\n"
      "s_movk_i32 s84, 0x800\n"
      "s_mov_b32 s48, s20\n"
      "s_mov_b32 s49, s21\n"
      "s_mov_b32 s50, 0xf41000\n"
      "s_mul_i32 s81, s86, 0x9c00\n"
      "s_add_u32 s81, s81, 0xa81000\n"
      "s_movk_i32 s82, 12\n"
      ".Lms_477:\n"
      "buffer_store_dwordx4 v[128:131], v1, s[48:51], s81 offen offset:0 sc1\n"
      "buffer_store_dwordx4 v[128:131], v1, s[48:51], s81 offen offset:1024 sc1\n"
      "buffer_store_dwordx4 v[128:131], v1, s[48:51], s81 offen offset:2048 sc1\n"
      "s_add_u32 s81, s81, 0xc00\n"
      "s_sub_u32 s82, s82, 1\n"
      "s_cbranch_scc0 .Lms_477\n"
      "s_waitcnt vmcnt(0)\n"
      "s_barrier\n"
      "s_lshr_b32 s81, s86, 2\n"
      "s_lshl4_add_u32 s81, s81, s84\n"
      "s_mov_b64 exec, 1\n"
      "buffer_store_dword v132, off, s[20:23], s81 sc1\n"
      "s_mov_b64 exec, -1\n"
      "s_branch .Lh5_476b\n"
      ".Lh5_476:\n"
      "s_sleep 0x6e\n"
      ".Lh5_476b:\n"
      "s_and_b32 s85, s18, 25\n"
      "s_add_u32 s81, s68, 0x20000\n"
      "v_min_u32 v236, 0, v0\n"
      "v_mul_u32_u24 v236, 0x400, v236\n"
      ".Lcan_478:\n"
      "s_sleep 6\n"
      "buffer_load_dword v237, v236, s[20:23], s81 offen sc1\n"
      "s_waitcnt vmcnt(0)\n"
      "v_cmp_eq_u32 vcc, -1, v237\n"
      "s_sub_u32 s79, s79, 1\n"
      "s_cmp_lt_i32 s79, 0\n"
      "s_cbranch_scc1 .Lcand_479\n"
      "s_cbranch_vccnz .Lcan_478\n"
      ".Lcand_479:\n"
      "v_and_b32 v200, 31, v0\n"
      "v_lshlrev_b32 v200, 7, v200\n"
      "s_add_u32 s83, s68, 0x20000\n"
      "v_add_u32 v201, s6, v200\n"
      "s_mov_b32 exec_lo, 0\n"
      "v_add_u32 v201, s7, v200\n"
      "s_mov_b32 exec_lo, -1\n"
      "buffer_load_dword v204, v201, s[20:23], s83 offen sc1\n"
      "v_add_u32 v201, s8, v200\n"
      "s_mov_b32 exec_lo, 0\n"
      "v_add_u32 v201, s9, v200\n"
      "s_mov_b32 exec_lo, -1\n"
      "buffer_load_dword v205, v201, s[20:23], s83 offen sc1\n"
      ".Lpf_480:\n"
      "v_min_u32 v236, 0, v0\n"
      "v_mul_u32_u24 v236, 0x400, v236\n"
      ".Lcan_481:\n"
      "s_sleep 6\n"
      "buffer_load_dword v237, v236, s[20:23], s69 offen sc1\n"
      "s_waitcnt vmcnt(0)\n"
      "v_cmp_eq_u32 vcc, -1, v237\n"
      "s_sub_u32 s79, s79, 1\n"
      "s_cmp_lt_i32 s79, 0\n"
      "s_cbranch_scc1 .Lcand_482\n"
      "s_cbranch_vccnz .Lcan_481\n"
      ".Lcand_482:\n"
      "v_and_b32 v200, 31, v0\n"
      "v_lshlrev_b32 v200, 7, v200\n"
      "s_mov_b32 s83, s69\n"
      "v_add_u32 v201, s10, v200\n"
      "s_mov_b32 exec_lo, 0\n"
      "v_add_u32 v201, s10, v200\n"
      "s_mov_b32 exec_lo, -1\n"
      "buffer_load_dword v204, v201, s[20:23], s83 offen sc1\n"
      "v_and_b32 v200, 31, v0\n"
      "v_lshlrev_b32 v200, 7, v200\n"
      "s_add_u32 s83, s69, 0x38000\n"
      "v_add_u32 v201, s11, v200\n"
      "s_mov_b32 exec_lo, 0\n"
      "v_add_u32 v201, s11, v200\n"
      "s_mov_b32 exec_lo, -1\n"
      "buffer_load_dword v208, v201, s[20:23], s83 offen sc1\n"
      "s_add_u32 s81, s69, 0x1c000\n"
      "v_min_u32 v236, 0, v0\n"
      "v_mul_u32_u24 v236, 0x400, v236\n"
      ".Lcan_483:\n"
      "s_sleep 6\n"
      "buffer_load_dword v237, v236, s[20:23], s81 offen sc1\n"
      "s_waitcnt vmcnt(0)\n"
      "v_cmp_eq_u32 vcc, -1, v237\n"
      "s_sub_u32 s79, s79, 1\n"
      "s_cmp_lt_i32 s79, 0\n"
      "s_cbranch_scc1 .Lcand_484\n"
      "s_cbranch_vccnz .Lcan_483\n"
      ".Lcand_484:\n"
      "v_and_b32 v200, 31, v0\n"
      "v_lshlrev_b32 v200, 7, v200\n"
      "s_add_u32 s83, s69, 0x1c000\n"
      "v_add_u32 v201, s10, v200\n"
      "s_mov_b32 exec_lo, 0\n"
      "v_add_u32 v201, s10, v200\n"
      "s_mov_b32 exec_lo, -1\n"
      "buffer_load_dword v204, v201, s[20:23], s83 offen sc1\n"
      "v_and_b32 v200, 31, v0\n"
      "v_lshlrev_b32 v200, 7, v200\n"
      "s_add_u32 s83, s69, 0x54000\n"
      "v_add_u32 v201, s11, v200\n"
      "s_mov_b32 exec_lo, 0\n"
      "v_add_u32 v201, s11, v200\n"
      "s_mov_b32 exec_lo, -1\n"
      "buffer_load_dword v208, v201, s[20:23], s83 offen sc1\n"
      "s_cmp_lg_u32 s53, 0\n"
      "s_cbranch_scc1 .Lend\n"
      "s_mov_b32 s48, s20\n"
      "s_mov_b32 s49, s21\n"
      "s_mov_b32 s50, 0x1289000\n"
      "s_mul_i32 s81, s86, 0x6c00\n"
      "s_add_u32 s81, s81, 0xf41000\n"
      "s_movk_i32 s82, 8\n"
      ".Lms_485:\n"
      "buffer_store_dwordx4 v[128:131], v1, s[48:51], s81 offen offset:0 sc1\n"
      "buffer_store_dwordx4 v[128:131], v1, s[48:51], s81 offen offset:1024 sc1\n"
      "buffer_store_dwordx4 v[128:131], v1, s[48:51], s81 offen offset:2048 sc1\n"
      "s_add_u32 s81, s81, 0xc00\n"
      "s_sub_u32 s82, s82, 1\n"
      "s_cbranch_scc0 .Lms_485\n"
      "s_movk_i32 s84, 0x800\n"
      "v_and_b32 v236, 0x1f0, v1\n"
      ".Lhf_486:\n"
      "buffer_load_dword v237, v236, s[20:23], s84 offen sc1\n"
      "s_waitcnt vmcnt(0)\n"
      "v_cmp_ne_u32 vcc, s56, v237\n"
      "s_sub_u32 s79, s79, 1\n"
      "s_cmp_lt_i32 s79, 0\n"
      "s_cbranch_scc1 .Lhf_487\n"
      "s_cbranch_vccnz .Lhf_486\n"
      ".Lhf_487:\n"
      "s_mov_b32 s48, s20\n"
      "s_mov_b32 s49, s21\n"
      "s_add_u32 s50, s72, 0x15000\n"
      "s_add_u32 s85, s72, s76\n"
      "s_mov_b32 m0, s76\n"
      "s_mov_b32 s78, s85\n"
      "buffer_load_dwordx4 v1, s[48:51], s78 offen sc1 lds\n"
      "buffer_load_dwordx4 v1, s[48:51], s78 offen offset:1024 sc1 lds\n"
      "buffer_load_dwordx4 v1, s[48:51], s78 offen offset:2048 sc1 lds\n"
      "buffer_load_dwordx4 v1, s[48:51], s78 offen offset:3072 sc1 lds\n"
      "s_add_u32 m0, s76, 0x4000\n"
      "s_add_u32 s78, s85, 0x4000\n"
      "buffer_load_dwordx4 v1, s[48:51], s78 offen sc1 lds\n"
      "buffer_load_dwordx4 v1, s[48:51], s78 offen offset:1024 sc1 lds\n"
      "buffer_load_dwordx4 v1, s[48:51], s78 offen offset:2048 sc1 lds\n"
      "buffer_load_dwordx4 v1, s[48:51], s78 offen offset:3072 sc1 lds\n"
      "s_add_u32 m0, s76, 0x8000\n"
      "s_add_u32 s78, s85, 0x8000\n"
      "buffer_load_dwordx4 v1, s[48:51], s78 offen sc1 lds\n"
      "buffer_load_dwordx4 v1, s[48:51], s78 offen offset:1024 sc1 lds\n"
      "buffer_load_dwordx4 v1, s[48:51], s78 offen offset:2048 sc1 lds\n"
      "buffer_load_dwordx4 v1, s[48:51], s78 offen offset:3072 sc1 lds\n"
      "s_add_u32 m0, s76, 0xc000\n"
      "s_add_u32 s78, s85, 0xc000\n"
      "buffer_load_dwordx4 v1, s[48:51], s78 offen sc1 lds\n"
      "buffer_load_dwordx4 v1, s[48:51], s78 offen offset:1024 sc1 lds\n"
      "buffer_load_dwordx4 v1, s[48:51], s78 offen offset:2048 sc1 lds\n"
      "buffer_load_dwordx4 v1, s[48:51], s78 offen offset:3072 sc1 lds\n"
      "s_add_u32 m0, s76, 0x10000\n"
      "s_add_u32 s78, s85, 0x10000\n"
      "buffer_load_dwordx4 v1, s[48:51], s78 offen sc1 lds\n"
      "buffer_load_dwordx4 v1, s[48:51], s78 offen offset:1024 sc1 lds\n"
      "buffer_load_dwordx4 v1, s[48:51], s78 offen offset:2048 sc1 lds\n"
      "buffer_load_dwordx4 v1, s[48:51], s78 offen offset:3072 sc1 lds\n"
      "s_add_u32 m0, s76, 0x14000\n"
      "s_add_u32 s78, s85, 0x14000\n"
      "buffer_load_dwordx4 v1, s[48:51], s78 offen sc1 lds\n"
      "buffer_load_dwordx4 v1, s[48:51], s78 offen offset:1024 sc1 lds\n"
      "buffer_load_dwordx4 v1, s[48:51], s78 offen offset:2048 sc1 lds\n"
      "buffer_load_dwordx4 v1, s[48:51], s78 offen offset:3072 sc1 lds\n"
      "s_add_u32 s81, s70, 0x808000\n"
      "v_min_u32 v236, 6, v0\n"
      "v_mul_u32_u24 v236, 0x1000, v236\n"
      ".Lcan_488:\n"
      "s_sleep 6\n"
      "buffer_load_dword v237, v236, s[20:23], s81 offen sc1\n"
      "s_waitcnt vmcnt(0)\n"
      "v_cmp_eq_u32 vcc, -1, v237\n"
      "s_sub_u32 s79, s79, 1\n"
      "s_cmp_lt_i32 s79, 0\n"
      "s_cbranch_scc1 .Lcand_489\n"
      "s_cbranch_vccnz .Lcan_488\n"
      ".Lcand_489:\n"
      "s_add_u32 s84, s70, 0x808000\n"
      "buffer_load_dwordx4 v[160:163], v1, s[20:23], s84 offen sc1\n"
      "buffer_load_dwordx4 v[164:167], v1, s[20:23], s84 offen offset:1024 sc1\n"
      "s_add_u32 s84, s70, 0x809000\n"
      "buffer_load_dwordx4 v[168:171], v1, s[20:23], s84 offen sc1\n"
      "buffer_load_dwordx4 v[172:175], v1, s[20:23], s84 offen offset:1024 sc1\n"
      "s_add_u32 s84, s70, 0x80a000\n"
      "buffer_load_dwordx4 v[176:179], v1, s[20:23], s84 offen sc1\n"
      "buffer_load_dwordx4 v[180:183], v1, s[20:23], s84 offen offset:1024 sc1\n"
      "s_add_u32 s84, s70, 0x80b000\n"
      "buffer_load_dwordx4 v[184:187], v1, s[20:23], s84 offen sc1\n"
      "s_add_u32 s58, s58, 0x808000\n"
      "s_add_u32 s59, s59, 0x808000\n"
      "s_add_u32 s61, s61, 0x808000\n"
      "s_add_u32 s62, s62, 0x808000\n"
      "s_add_u32 s65, s65, 0x808000\n"
      "s_add_u32 s66, s66, 0x808000\n"
      "s_add_u32 s17, s17, 0x808000\n"
      "s_add_u32 s67, s67, 0x808000\n"
      "s_add_u32 s68, s68, 0x808000\n"
      "s_add_u32 s69, s69, 0x808000\n"
      "s_add_u32 s71, s71, 0x808000\n"
      "s_waitcnt vmcnt(0)\n"
      "s_branch .Lnext_layer\n"
      ".Lpk_5:\n"
      "s_mov_b32 s98, s80\n"
      ".Lpk_5l:\n"
      "s_mov_b32 s80, s70\n"
      "buffer_load_dwordx4 v[160:163], v1, s[20:23], s80 offen sc1\n"
      "s_waitcnt vmcnt(8)\n"
      "buffer_load_dwordx4 v[164:167], v1, s[20:23], s80 offen offset:1024 sc1\n"
      "s_waitcnt vmcnt(0)\n"
      "v_cmp_eq_u32 vcc, -1, v160\n"
      "s_sub_u32 s79, s79, 1\n"
      "s_cmp_lt_i32 s79, 0\n"
      "s_cbranch_scc1 .Lpk_7\n"
      "s_cbranch_vccnz .Lpk_5l\n"
      ".Lpk_7:\n"
      "s_mov_b32 s80, s98\n"
      "s_branch .Lpr_6\n"
      ".Lpk_8:\n"
      "s_mov_b32 s98, s80\n"
      ".Lpk_8l:\n"
      "s_mov_b32 s80, s70\n"
      "buffer_load_dwordx4 v[164:167], v1, s[20:23], s80 offen offset:1024 sc1\n"
      "s_waitcnt vmcnt(0)\n"
      "v_cmp_eq_u32 vcc, -1, v164\n"
      "s_sub_u32 s79, s79, 1\n"
      "s_cmp_lt_i32 s79, 0\n"
      "s_cbranch_scc1 .Lpk_10\n"
      "s_cbranch_vccnz .Lpk_8l\n"
      ".Lpk_10:\n"
      "s_mov_b32 s80, s98\n"
      "s_branch .Lpr_9\n"
      ".Lpk_11:\n"
      "s_mov_b32 s98, s80\n"
      ".Lpk_11l:\n"
      "s_add_u32 s80, s70, 0x1000\n"
      "buffer_load_dwordx4 v[168:171], v1, s[20:23], s80 offen sc1\n"
      "s_waitcnt vmcnt(11)\n"
      "buffer_load_dwordx4 v[172:175], v1, s[20:23], s80 offen offset:1024 sc1\n"
      "s_waitcnt vmcnt(0)\n"
      "v_cmp_eq_u32 vcc, -1, v168\n"
      "s_sub_u32 s79, s79, 1\n"
      "s_cmp_lt_i32 s79, 0\n"
      "s_cbranch_scc1 .Lpk_13\n"
      "s_cbranch_vccnz .Lpk_11l\n"
      ".Lpk_13:\n"
      "s_mov_b32 s80, s98\n"
      "s_branch .Lpr_12\n"
      ".Lpk_14:\n"
      "s_mov_b32 s98, s80\n"
      ".Lpk_14l:\n"
      "s_add_u32 s80, s70, 0x1000\n"
      "buffer_load_dwordx4 v[172:175], v1, s[20:23], s80 offen offset:1024 sc1\n"
      "s_waitcnt vmcnt(0)\n"
      "v_cmp_eq_u32 vcc, -1, v172\n"
      "s_sub_u32 s79, s79, 1\n"
      "s_cmp_lt_i32 s79, 0\n"
      "s_cbranch_scc1 .Lpk_16\n"
      "s_cbranch_vccnz .Lpk_14l\n"
      ".Lpk_16:\n"
      "s_mov_b32 s80, s98\n"
      "s_branch .Lpr_15\n"
      ".Lpk_17:\n"
      "s_mov_b32 s98, s80\n"
      ".Lpk_17l:\n"
      "s_add_u32 s80, s70, 0x2000\n"
      "buffer_load_dwordx4 v[176:179], v1, s[20:23], s80 offen sc1\n"
      "s_waitcnt vmcnt(11)\n"
      "buffer_load_dwordx4 v[180:183], v1, s[20:23], s80 offen offset:1024 sc1\n"
      "s_waitcnt vmcnt(0)\n"
      "v_cmp_eq_u32 vcc, -1, v176\n"
      "s_sub_u32 s79, s79, 1\n"
      "s_cmp_lt_i32 s79, 0\n"
      "s_cbranch_scc1 .Lpk_19\n"
      "s_cbranch_vccnz .Lpk_17l\n"
      ".Lpk_19:\n"
      "s_mov_b32 s80, s98\n"
      "s_branch .Lpr_18\n"
      ".Lpk_20:\n"
      "s_mov_b32 s98, s80\n"
      ".Lpk_20l:\n"
      "s_add_u32 s80, s70, 0x2000\n"
      "buffer_load_dwordx4 v[180:183], v1, s[20:23], s80 offen offset:1024 sc1\n"
      "s_waitcnt vmcnt(0)\n"
      "v_cmp_eq_u32 vcc, -1, v180\n"
      "s_sub_u32 s79, s79, 1\n"
      "s_cmp_lt_i32 s79, 0\n"
      "s_cbranch_scc1 .Lpk_22\n"
      "s_cbranch_vccnz .Lpk_20l\n"
      ".Lpk_22:\n"
      "s_mov_b32 s80, s98\n"
      "s_branch .Lpr_21\n"
      ".Lpk_23:\n"
      "s_mov_b32 s98, s80\n"
      ".Lpk_23l:\n"
      "s_add_u32 s80, s70, 0x3000\n"
      "buffer_load_dwordx4 v[184:187], v1, s[20:23], s80 offen sc1\n"
      "s_waitcnt vmcnt(13)\n"
      "buffer_load_dwordx4 v[188:191], v1, s[20:23], s80 offen offset:1024 sc1\n"
      "s_waitcnt vmcnt(0)\n"
      "v_cmp_eq_u32 vcc, -1, v184\n"
      "s_sub_u32 s79, s79, 1\n"
      "s_cmp_lt_i32 s79, 0\n"
      "s_cbranch_scc1 .Lpk_25\n"
      "s_cbranch_vccnz .Lpk_23l\n"
      ".Lpk_25:\n"
      "s_mov_b32 s80, s98\n"
      "s_branch .Lpr_24\n"
      ".Lpk_26:\n"
      "s_mov_b32 s98, s80\n"
      ".Lpk_26l:\n"
      "s_add_u32 s80, s70, 0x3000\n"
      "buffer_load_dwordx4 v[188:191], v1, s[20:23], s80 offen offset:1024 sc1\n"
      "s_waitcnt vmcnt(0)\n"
      "v_cmp_eq_u32 vcc, -1, v188\n"
      "s_sub_u32 s79, s79, 1\n"
      "s_cmp_lt_i32 s79, 0\n"
      "s_cbranch_scc1 .Lpk_28\n"
      "s_cbranch_vccnz .Lpk_26l\n"
      ".Lpk_28:\n"
      "s_mov_b32 s80, s98\n"
      "s_branch .Lpr_27\n"
      ".Lpk_29:\n"
      "s_mov_b32 s98, s80\n"
      ".Lpk_29l:\n"
      "s_add_u32 s80, s70, 0x4000\n"
      "buffer_load_dwordx4 v[160:163], v1, s[20:23], s80 offen sc1\n"
      "s_waitcnt vmcnt(10)\n"
      "buffer_load_dwordx4 v[164:167], v1, s[20:23], s80 offen offset:1024 sc1\n"
      "s_waitcnt vmcnt(0)\n"
      "v_cmp_eq_u32 vcc, -1, v160\n"
      "s_sub_u32 s79, s79, 1\n"
      "s_cmp_lt_i32 s79, 0\n"
      "s_cbranch_scc1 .Lpk_31\n"
      "s_cbranch_vccnz .Lpk_29l\n"
      ".Lpk_31:\n"
      "s_mov_b32 s80, s98\n"
      "s_branch .Lpr_30\n"
      ".Lpk_32:\n"
      "s_mov_b32 s98, s80\n"
      ".Lpk_32l:\n"
      "s_add_u32 s80, s70, 0x4000\n"
      "buffer_load_dwordx4 v[164:167], v1, s[20:23], s80 offen offset:1024 sc1\n"
      "s_waitcnt vmcnt(0)\n"
      "v_cmp_eq_u32 vcc, -1, v164\n"
      "s_sub_u32 s79, s79, 1\n"
      "s_cmp_lt_i32 s79, 0\n"
      "s_cbranch_scc1 .Lpk_34\n"
      "s_cbranch_vccnz .Lpk_32l\n"
      ".Lpk_34:\n"
      "s_mov_b32 s80, s98\n"
      "s_branch .Lpr_33\n"
      ".Lpk_35:\n"
      "s_mov_b32 s98, s80\n"
      ".Lpk_35l:\n"
      "s_add_u32 s80, s70, 0x5000\n"
      "buffer_load_dwordx4 v[168:171], v1, s[20:23], s80 offen sc1\n"
      "s_waitcnt vmcnt(9)\n"
      "buffer_load_dwordx4 v[172:175], v1, s[20:23], s80 offen offset:1024 sc1\n"
      "s_waitcnt vmcnt(0)\n"
      "v_cmp_eq_u32 vcc, -1, v168\n"
      "s_sub_u32 s79, s79, 1\n"
      "s_cmp_lt_i32 s79, 0\n"
      "s_cbranch_scc1 .Lpk_37\n"
      "s_cbranch_vccnz .Lpk_35l\n"
      ".Lpk_37:\n"
      "s_mov_b32 s80, s98\n"
      "s_branch .Lpr_36\n"
      ".Lpk_38:\n"
      "s_mov_b32 s98, s80\n"
      ".Lpk_38l:\n"
      "s_add_u32 s80, s70, 0x5000\n"
      "buffer_load_dwordx4 v[172:175], v1, s[20:23], s80 offen offset:1024 sc1\n"
      "s_waitcnt vmcnt(0)\n"
      "v_cmp_eq_u32 vcc, -1, v172\n"
      "s_sub_u32 s79, s79, 1\n"
      "s_cmp_lt_i32 s79, 0\n"
      "s_cbranch_scc1 .Lpk_40\n"
      "s_cbranch_vccnz .Lpk_38l\n"
      ".Lpk_40:\n"
      "s_mov_b32 s80, s98\n"
      "s_branch .Lpr_39\n"
      ".Lpk_41:\n"
      "s_mov_b32 s98, s80\n"
      ".Lpk_41l:\n"
      "s_add_u32 s80, s70, 0x6000\n"
      "buffer_load_dwordx4 v[176:179], v1, s[20:23], s80 offen sc1\n"
      "s_waitcnt vmcnt(9)\n"
      "buffer_load_dwordx4 v[180:183], v1, s[20:23], s80 offen offset:1024 sc1\n"
      "s_waitcnt vmcnt(0)\n"
      "v_cmp_eq_u32 vcc, -1, v176\n"
      "s_sub_u32 s79, s79, 1\n"
      "s_cmp_lt_i32 s79, 0\n"
      "s_cbranch_scc1 .Lpk_43\n"
      "s_cbranch_vccnz .Lpk_41l\n"
      ".Lpk_43:\n"
      "s_mov_b32 s80, s98\n"
      "s_branch .Lpr_42\n"
      ".Lpk_44:\n"
      "s_mov_b32 s98, s80\n"
      ".Lpk_44l:\n"
      "s_add_u32 s80, s70, 0x6000\n"
      "buffer_load_dwordx4 v[180:183], v1, s[20:23], s80 offen offset:1024 sc1\n"
      "s_waitcnt vmcnt(0)\n"
      "v_cmp_eq_u32 vcc, -1, v180\n"
      "s_sub_u32 s79, s79, 1\n"
      "s_cmp_lt_i32 s79, 0\n"
      "s_cbranch_scc1 .Lpk_46\n"
      "s_cbranch_vccnz .Lpk_44l\n"
      ".Lpk_46:\n"
      "s_mov_b32 s80, s98\n"
      "s_branch .Lpr_45\n"
      ".Lpk_47:\n"
      "s_mov_b32 s98, s80\n"
      ".Lpk_47l:\n"
      "s_mov_b32 s80, s70\n"
      "buffer_load_dwordx4 v[184:187], v1, s[20:23], s80 offen offset:2048 sc1\n"
      "s_waitcnt vmcnt(10)\n"
      "buffer_load_dwordx4 v[188:191], v1, s[20:23], s80 offen offset:3072 sc1\n"
      "s_waitcnt vmcnt(0)\n"
      "v_cmp_eq_u32 vcc, -1, v184\n"
      "s_sub_u32 s79, s79, 1\n"
      "s_cmp_lt_i32 s79, 0\n"
      "s_cbranch_scc1 .Lpk_49\n"
      "s_cbranch_vccnz .Lpk_47l\n"
      ".Lpk_49:\n"
      "s_mov_b32 s80, s98\n"
      "s_branch .Lpr_48\n"
      ".Lpk_50:\n"
      "s_mov_b32 s98, s80\n"
      ".Lpk_50l:\n"
      "s_mov_b32 s80, s70\n"
      "buffer_load_dwordx4 v[188:191], v1, s[20:23], s80 offen offset:3072 sc1\n"
      "s_waitcnt vmcnt(0)\n"
      "v_cmp_eq_u32 vcc, -1, v188\n"
      "s_sub_u32 s79, s79, 1\n"
      "s_cmp_lt_i32 s79, 0\n"
      "s_cbranch_scc1 .Lpk_52\n"
      "s_cbranch_vccnz .Lpk_50l\n"
      ".Lpk_52:\n"
      "s_mov_b32 s80, s98\n"
      "s_branch .Lpr_51\n"
      ".Lpk_53:\n"
      "s_mov_b32 s98, s80\n"
      ".Lpk_53l:\n"
      "s_add_u32 s80, s70, 0x1000\n"
      "buffer_load_dwordx4 v[160:163], v1, s[20:23], s80 offen offset:2048 sc1\n"
      "buffer_load_dwordx4 v[164:167], v1, s[20:23], s80 offen offset:3072 sc1\n"
      "s_waitcnt vmcnt(0)\n"
      "v_cmp_eq_u32 vcc, -1, v160\n"
      "s_sub_u32 s79, s79, 1\n"
      "s_cmp_lt_i32 s79, 0\n"
      "s_cbranch_scc1 .Lpk_55\n"
      "s_cbranch_vccnz .Lpk_53l\n"
      ".Lpk_55:\n"
      "s_mov_b32 s80, s98\n"
      "s_branch .Lpr_54\n"
      ".Lpk_56:\n"
      "s_mov_b32 s98, s80\n"
      ".Lpk_56l:\n"
      "s_add_u32 s80, s70, 0x1000\n"
      "buffer_load_dwordx4 v[164:167], v1, s[20:23], s80 offen offset:3072 sc1\n"
      "s_waitcnt vmcnt(0)\n"
      "v_cmp_eq_u32 vcc, -1, v164\n"
      "s_sub_u32 s79, s79, 1\n"
      "s_cmp_lt_i32 s79, 0\n"
      "s_cbranch_scc1 .Lpk_58\n"
      "s_cbranch_vccnz .Lpk_56l\n"
      ".Lpk_58:\n"
      "s_mov_b32 s80, s98\n"
      "s_branch .Lpr_57\n"
      ".Lpk_59:\n"
      "s_mov_b32 s98, s80\n"
      ".Lpk_59l:\n"
      "s_add_u32 s80, s70, 0x2000\n"
      "buffer_load_dwordx4 v[168:171], v1, s[20:23], s80 offen offset:2048 sc1\n"
      "s_waitcnt vmcnt(6)\n"
      "buffer_load_dwordx4 v[172:175], v1, s[20:23], s80 offen offset:3072 sc1\n"
      "s_waitcnt vmcnt(0)\n"
      "v_cmp_eq_u32 vcc, -1, v168\n"
      "s_sub_u32 s79, s79, 1\n"
      "s_cmp_lt_i32 s79, 0\n"
      "s_cbranch_scc1 .Lpk_61\n"
      "s_cbranch_vccnz .Lpk_59l\n"
      ".Lpk_61:\n"
      "s_mov_b32 s80, s98\n"
      "s_branch .Lpr_60\n"
      ".Lpk_62:\n"
      "s_mov_b32 s98, s80\n"
      ".Lpk_62l:\n"
      "s_add_u32 s80, s70, 0x2000\n"
      "buffer_load_dwordx4 v[172:175], v1, s[20:23], s80 offen offset:3072 sc1\n"
      "s_waitcnt vmcnt(0)\n"
      "v_cmp_eq_u32 vcc, -1, v172\n"
      "s_sub_u32 s79, s79, 1\n"
      "s_cmp_lt_i32 s79, 0\n"
      "s_cbranch_scc1 .Lpk_64\n"
      "s_cbranch_vccnz .Lpk_62l\n"
      ".Lpk_64:\n"
      "s_mov_b32 s80, s98\n"
      "s_branch .Lpr_63\n"
      ".Lpk_65:\n"
      "s_mov_b32 s98, s80\n"
      ".Lpk_65l:\n"
      "s_add_u32 s80, s70, 0x3000\n"
      "buffer_load_dwordx4 v[176:179], v1, s[20:23], s80 offen offset:2048 sc1\n"
      "s_waitcnt vmcnt(6)\n"
      "buffer_load_dwordx4 v[180:183], v1, s[20:23], s80 offen offset:3072 sc1\n"
      "s_waitcnt vmcnt(0)\n"
      "v_cmp_eq_u32 vcc, -1, v176\n"
      "s_sub_u32 s79, s79, 1\n"
      "s_cmp_lt_i32 s79, 0\n"
      "s_cbranch_scc1 .Lpk_67\n"
      "s_cbranch_vccnz .Lpk_65l\n"
      ".Lpk_67:\n"
      "s_mov_b32 s80, s98\n"
      "s_branch .Lpr_66\n"
      ".Lpk_68:\n"
      "s_mov_b32 s98, s80\n"
      ".Lpk_68l:\n"
      "s_add_u32 s80, s70, 0x3000\n"
      "buffer_load_dwordx4 v[180:183], v1, s[20:23], s80 offen offset:3072 sc1\n"
      "s_waitcnt vmcnt(0)\n"
      "v_cmp_eq_u32 vcc, -1, v180\n"
      "s_sub_u32 s79, s79, 1\n"
      "s_cmp_lt_i32 s79, 0\n"
      "s_cbranch_scc1 .Lpk_70\n"
      "s_cbranch_vccnz .Lpk_68l\n"
      ".Lpk_70:\n"
      "s_mov_b32 s80, s98\n"
      "s_branch .Lpr_69\n"
      ".Lpk_71:\n"
      "s_mov_b32 s98, s80\n"
      ".Lpk_71l:\n"
      "s_add_u32 s80, s70, 0x4000\n"
      "buffer_load_dwordx4 v[184:187], v1, s[20:23], s80 offen offset:2048 sc1\n"
      "s_waitcnt vmcnt(5)\n"
      "buffer_load_dwordx4 v[188:191], v1, s[20:23], s80 offen offset:3072 sc1\n"
      "s_waitcnt vmcnt(0)\n"
      "v_cmp_eq_u32 vcc, -1, v184\n"
      "s_sub_u32 s79, s79, 1\n"
      "s_cmp_lt_i32 s79, 0\n"
      "s_cbranch_scc1 .Lpk_73\n"
      "s_cbranch_vccnz .Lpk_71l\n"
      ".Lpk_73:\n"
      "s_mov_b32 s80, s98\n"
      "s_branch .Lpr_72\n"
      ".Lpk_74:\n"
      "s_mov_b32 s98, s80\n"
      ".Lpk_74l:\n"
      "s_add_u32 s80, s70, 0x4000\n"
      "buffer_load_dwordx4 v[188:191], v1, s[20:23], s80 offen offset:3072 sc1\n"
      "s_waitcnt vmcnt(0)\n"
      "v_cmp_eq_u32 vcc, -1, v188\n"
      "s_sub_u32 s79, s79, 1\n"
      "s_cmp_lt_i32 s79, 0\n"
      "s_cbranch_scc1 .Lpk_76\n"
      "s_cbranch_vccnz .Lpk_74l\n"
      ".Lpk_76:\n"
      "s_mov_b32 s80, s98\n"
      "s_branch .Lpr_75\n"
      ".Lpk_77:\n"
      "s_mov_b32 s98, s80\n"
      ".Lpk_77l:\n"
      "s_add_u32 s80, s70, 0x5000\n"
      "buffer_load_dwordx4 v[160:163], v1, s[20:23], s80 offen offset:2048 sc1\n"
      "s_waitcnt vmcnt(3)\n"
      "buffer_load_dwordx4 v[164:167], v1, s[20:23], s80 offen offset:3072 sc1\n"
      "s_waitcnt vmcnt(0)\n"
      "v_cmp_eq_u32 vcc, -1, v160\n"
      "s_sub_u32 s79, s79, 1\n"
      "s_cmp_lt_i32 s79, 0\n"
      "s_cbranch_scc1 .Lpk_79\n"
      "s_cbranch_vccnz .Lpk_77l\n"
      ".Lpk_79:\n"
      "s_mov_b32 s80, s98\n"
      "s_branch .Lpr_78\n"
      ".Lpk_80:\n"
      "s_mov_b32 s98, s80\n"
      ".Lpk_80l:\n"
      "s_add_u32 s80, s70, 0x5000\n"
      "buffer_load_dwordx4 v[164:167], v1, s[20:23], s80 offen offset:3072 sc1\n"
      "s_waitcnt vmcnt(0)\n"
      "v_cmp_eq_u32 vcc, -1, v164\n"
      "s_sub_u32 s79, s79, 1\n"
      "s_cmp_lt_i32 s79, 0\n"
      "s_cbranch_scc1 .Lpk_82\n"
      "s_cbranch_vccnz .Lpk_80l\n"
      ".Lpk_82:\n"
      "s_mov_b32 s80, s98\n"
      "s_branch .Lpr_81\n"
      ".Lpk_83:\n"
      "s_mov_b32 s98, s80\n"
      ".Lpk_83l:\n"
      "s_add_u32 s80, s70, 0x6000\n"
      "buffer_load_dwordx4 v[168:171], v1, s[20:23], s80 offen offset:2048 sc1\n"
      "s_waitcnt vmcnt(1)\n"
      "buffer_load_dwordx4 v[172:175], v1, s[20:23], s80 offen offset:3072 sc1\n"
      "s_waitcnt vmcnt(0)\n"
      "v_cmp_eq_u32 vcc, -1, v168\n"
      "s_sub_u32 s79, s79, 1\n"
      "s_cmp_lt_i32 s79, 0\n"
      "s_cbranch_scc1 .Lpk_85\n"
      "s_cbranch_vccnz .Lpk_83l\n"
      ".Lpk_85:\n"
      "s_mov_b32 s80, s98\n"
      "s_branch .Lpr_84\n"
      ".Lpk_86:\n"
      "s_mov_b32 s98, s80\n"
      ".Lpk_86l:\n"
      "s_add_u32 s80, s70, 0x6000\n"
      "buffer_load_dwordx4 v[172:175], v1, s[20:23], s80 offen offset:3072 sc1\n"
      "s_waitcnt vmcnt(0)\n"
      "v_cmp_eq_u32 vcc, -1, v172\n"
      "s_sub_u32 s79, s79, 1\n"
      "s_cmp_lt_i32 s79, 0\n"
      "s_cbranch_scc1 .Lpk_88\n"
      "s_cbranch_vccnz .Lpk_86l\n"
      ".Lpk_88:\n"
      "s_mov_b32 s80, s98\n"
      "s_branch .Lpr_87\n"
      ".La1_st:\n"
      ".La1_st_l:\n"
      "buffer_load_dwordx4 v[240:243], v1, s[20:23], s71 offen sc1\n"
      "buffer_load_dwordx4 v[244:247], v1, s[20:23], s71 offen offset:1024 sc1\n"
      "buffer_load_dwordx4 v[248:251], v1, s[20:23], s71 offen offset:2048 sc1\n"
      "s_mov_b32 exec_hi, 0\n"
      "buffer_load_dwordx4 v[252:255], v1, s[20:23], s71 offen offset:3072 sc1\n"
      "s_mov_b32 exec_hi, -1\n"
      "s_waitcnt vmcnt(3)\n"
      "v_max_u32 v239, v240, v241\n"
      "v_max3_u32 v239, v239, v242, v243\n"
      "s_waitcnt vmcnt(2)\n"
      "v_max3_u32 v239, v239, v244, v245\n"
      "v_max3_u32 v239, v239, v246, v247\n"
      "s_waitcnt vmcnt(1)\n"
      "v_max3_u32 v239, v239, v248, v249\n"
      "v_max3_u32 v239, v239, v250, v251\n"
      "s_waitcnt vmcnt(0)\n"
      "v_max3_u32 v239, v239, v252, v253\n"
      "v_max3_u32 v239, v239, v254, v255\n"
      "v_cmp_eq_u32 vcc, -1, v239\n"
      "s_sub_u32 s79, s79, 1\n"
      "s_cmp_lt_i32 s79, 0\n"
      "s_cbranch_scc1 .La1_st_d\n"
      "s_cbranch_vccnz .La1_st_l\n"
      ".La1_st_d:\n"
      "v_add_f32 v240, v240, v244\n"
      "v_add_f32 v241, v241, v245\n"
      "v_add_f32 v242, v242, v246\n"
      "v_add_f32 v243, v243, v247\n"
      "v_add_f32 v248, v248, v252\n"
      "v_add_f32 v249, v249, v253\n"
      "v_add_f32 v250, v250, v254\n"
      "v_add_f32 v251, v251, v255\n"
      "v_add_f32 v240, v240, v248\n"
      "v_add_f32 v241, v241, v249\n"
      "v_add_f32 v242, v242, v250\n"
      "v_add_f32 v243, v243, v251\n"
      "s_nop 0\n"
      "v_permlane16_swap_b32 v240, v242\n"
      "v_permlane16_swap_b32 v241, v243\n"
      "v_add_f32 v234, v240, v242\n"
      "v_add_f32 v235, v241, v243\n"
      "v_mov_b32 v236, v234\n"
      "v_mov_b32 v237, v235\n"
      "s_nop 0\n"
      "v_permlane32_swap_b32 v234, v236\n"
      "v_permlane32_swap_b32 v235, v237\n"
      "v_add_f32 v234, v234, v236\n"
      "v_add_f32 v235, v235, v237\n"
      "v_mul_f32 v234, 0xbb124925, v234\n"
      "v_mul_f32 v235, 0x3b124925, v235\n"
      "v_mul_f32 v236, v234, v234\n"
      "v_sub_f32 v235, v235, v236\n"
      "v_fma_f32 v234, v235, 0, v234\n"
      "v_max_f32 v235, 0, v235\n"
      "v_add_f32 v235, 0x3727c5ac, v235\n"
      "v_rsq_f32 v235, v235\n"
      "s_nop 0\n"
      "ds_write_b32 v14, v235 offset:128\n"
      "v_mul_f32 v234, v234, v235\n"
      "ds_write_b32 v14, v234\n"
      "s_waitcnt vmcnt(0) lgkmcnt(0)\n"
      "s_nop 15\n"
      "s_nop 7\n"
      "s_branch .La1_st_ok\n"
      ".La1_nan:\n"
      "s_cmp_lg_u32 s64, 0\n"
      "s_cbranch_scc1 .Lgu_89\n"
      "s_mov_b32 s64, 1\n"
      "s_mov_b32 s81, s70\n"
      "s_add_u32 s82, s81, 0x7000\n"
      ".Lvf_90:\n"
      "buffer_load_dwordx4 v[160:163], v1, s[20:23], s81 offen sc1\n"
      "buffer_load_dwordx4 v[164:167], v1, s[20:23], s81 offen offset:1024 sc1\n"
      "s_sub_u32 s79, s79, 1\n"
      "s_cmp_lt_i32 s79, 0\n"
      "s_cbranch_scc1 .Lvf_91\n"
      "s_waitcnt vmcnt(1)\n"
      "v_max_u32 v239, v160, v161\n"
      "v_max3_u32 v239, v239, v162, v163\n"
      "s_waitcnt vmcnt(0)\n"
      "v_max3_u32 v239, v239, v164, v165\n"
      "v_max3_u32 v239, v239, v166, v167\n"
      "v_cmp_eq_u32 vcc, -1, v239\n"
      "s_nop 0\n"
      "s_cbranch_vccnz .Lvf_90\n"
      "s_add_u32 s81, s81, 0x800\n"
      "s_cmp_lt_u32 s81, s82\n"
      "s_cbranch_scc1 .Lvf_90\n"
      ".Lvf_91:\n"
      "v_add_u32 v228, 0x24000, v30\n"
      "s_lshl_b32 s82, s52, 11\n"
      "v_add_u32 v228, s82, v228\n"
      "s_add_u32 s82, s82, 0x24000\n"
      "s_mov_b32 s80, s70\n"
      "s_waitcnt vmcnt(1)\n"
      "buffer_load_dwordx4 v[160:163], v1, s[20:23], s80 offen sc1\n"
      "s_waitcnt lgkmcnt(0)\n"
      "s_mov_b32 m0, s82\n"
      "s_add_u32 s78, s72, 0xa800\n"
      "buffer_load_dwordx4 v1, s[20:23], s78 offen sc1 lds\n"
      "s_waitcnt vmcnt(0)\n"
      "ds_read_b64_tr_b16 v[208:209], v228 offset:0\n"
      "ds_read_b64_tr_b16 v[210:211], v228 offset:512\n"
      "s_waitcnt lgkmcnt(0)\n"
      "v_mfma_f32_32x32x16_bf16 v[192:207], v[208:211], v[160:163], 0\n"
      "s_mov_b32 m0, s82\n"
      "s_add_u32 s78, s72, 0xe000\n"
      "buffer_load_dwordx4 v1, s[20:23], s78 offen sc1 lds\n"
      "s_waitcnt vmcnt(0)\n"
      "ds_read_b64_tr_b16 v[208:209], v228 offset:0\n"
      "ds_read_b64_tr_b16 v[210:211], v228 offset:512\n"
      "s_waitcnt lgkmcnt(0)\n"
      "v_mfma_f32_32x32x16_bf16 v[128:143], v[160:163], v[208:211], 0\n"
      "s_mov_b32 m0, s82\n"
      "s_add_u32 s78, s72, 0x11800\n"
      "buffer_load_dwordx4 v1, s[20:23], s78 offen sc1 lds\n"
      "s_waitcnt vmcnt(0)\n"
      "ds_read_b64_tr_b16 v[208:209], v228 offset:0\n"
      "ds_read_b64_tr_b16 v[210:211], v228 offset:512\n"
      "s_waitcnt lgkmcnt(0)\n"
      "v_mfma_f32_32x32x16_bf16 v[144:159], v[160:163], v[208:211], 0\n"
      "buffer_load_dwordx4 v[164:167], v1, s[20:23], s80 offen offset:1024 sc1\n"
      "s_mov_b32 m0, s82\n"
      "s_add_u32 s78, s72, 0xac00\n"
      "buffer_load_dwordx4 v1, s[20:23], s78 offen sc1 lds\n"
      "s_waitcnt vmcnt(0)\n"
      "ds_read_b64_tr_b16 v[208:209], v228 offset:0\n"
      "ds_read_b64_tr_b16 v[210:211], v228 offset:512\n"
      "s_waitcnt lgkmcnt(0)\n"
      "v_mfma_f32_32x32x16_bf16 v[192:207], v[208:211], v[164:167], v[192:207]\n"
      "s_mov_b32 m0, s82\n"
      "s_add_u32 s78, s72, 0xe400\n"
      "buffer_load_dwordx4 v1, s[20:23], s78 offen sc1 lds\n"
      "s_waitcnt vmcnt(0)\n"
      "ds_read_b64_tr_b16 v[208:209], v228 offset:0\n"
      "ds_read_b64_tr_b16 v[210:211], v228 offset:512\n"
      "s_waitcnt lgkmcnt(0)\n"
      "v_mfma_f32_32x32x16_bf16 v[128:143], v[164:167], v[208:211], v[128:143]\n"
      "s_mov_b32 m0, s82\n"
      "s_add_u32 s78, s72, 0x11c00\n"
      "buffer_load_dwordx4 v1, s[20:23], s78 offen sc1 lds\n"
      "s_waitcnt vmcnt(0)\n"
      "ds_read_b64_tr_b16 v[208:209], v228 offset:0\n"
      "ds_read_b64_tr_b16 v[210:211], v228 offset:512\n"
      "s_waitcnt lgkmcnt(0)\n"
      "v_mfma_f32_32x32x16_bf16 v[144:159], v[164:167], v[208:211], v[144:159]\n"
      "s_add_u32 s80, s70, 0x1000\n"
      "buffer_load_dwordx4 v[168:171], v1, s[20:23], s80 offen sc1\n"
      "s_mov_b32 m0, s82\n"
      "s_add_u32 s78, s72, 0xb000\n"
      "buffer_load_dwordx4 v1, s[20:23], s78 offen sc1 lds\n"
      "s_waitcnt vmcnt(0)\n"
      "ds_read_b64_tr_b16 v[208:209], v228 offset:0\n"
      "ds_read_b64_tr_b16 v[210:211], v228 offset:512\n"
      "s_waitcnt lgkmcnt(0)\n"
      "v_mfma_f32_32x32x16_bf16 v[192:207], v[208:211], v[168:171], v[192:207]\n"
      "s_mov_b32 m0, s82\n"
      "s_add_u32 s78, s72, 0xe800\n"
      "buffer_load_dwordx4 v1, s[20:23], s78 offen sc1 lds\n"
      "s_waitcnt vmcnt(0)\n"
      "ds_read_b64_tr_b16 v[208:209], v228 offset:0\n"
      "ds_read_b64_tr_b16 v[210:211], v228 offset:512\n"
      "s_waitcnt lgkmcnt(0)\n"
      "v_mfma_f32_32x32x16_bf16 v[128:143], v[168:171], v[208:211], v[128:143]\n"
      "s_mov_b32 m0, s82\n"
      "s_add_u32 s78, s72, 0x12000\n"
      "buffer_load_dwordx4 v1, s[20:23], s78 offen sc1 lds\n"
      "s_waitcnt vmcnt(0)\n"
      "ds_read_b64_tr_b16 v[208:209], v228 offset:0\n"
      "ds_read_b64_tr_b16 v[210:211], v228 offset:512\n"
      "s_waitcnt lgkmcnt(0)\n"
      "v_mfma_f32_32x32x16_bf16 v[144:159], v[168:171], v[208:211], v[144:159]\n"
      "buffer_load_dwordx4 v[172:175], v1, s[20:23], s80 offen offset:1024 sc1\n"
      "s_mov_b32 m0, s82\n"
      "s_add_u32 s78, s72, 0xb400\n"
      "buffer_load_dwordx4 v1, s[20:23], s78 offen sc1 lds\n"
      "s_waitcnt vmcnt(0)\n"
      "ds_read_b64_tr_b16 v[208:209], v228 offset:0\n"
      "ds_read_b64_tr_b16 v[210:211], v228 offset:512\n"
      "s_waitcnt lgkmcnt(0)\n"
      "v_mfma_f32_32x32x16_bf16 v[192:207], v[208:211], v[172:175], v[192:207]\n"
      "s_mov_b32 m0, s82\n"
      "s_add_u32 s78, s72, 0xec00\n"
      "buffer_load_dwordx4 v1, s[20:23], s78 offen sc1 lds\n"
      "s_waitcnt vmcnt(0)\n"
      "ds_read_b64_tr_b16 v[208:209], v228 offset:0\n"
      "ds_read_b64_tr_b16 v[210:211], v228 offset:512\n"
      "s_waitcnt lgkmcnt(0)\n"
      "v_mfma_f32_32x32x16_bf16 v[128:143], v[172:175], v[208:211], v[128:143]\n"
      "s_mov_b32 m0, s82\n"
      "s_add_u32 s78, s72, 0x12400\n"
      "buffer_load_dwordx4 v1, s[20:23], s78 offen sc1 lds\n"
      "s_waitcnt vmcnt(0)\n"
      "ds_read_b64_tr_b16 v[208:209], v228 offset:0\n"
      "ds_read_b64_tr_b16 v[210:211], v228 offset:512\n"
      "s_waitcnt lgkmcnt(0)\n"
      "v_mfma_f32_32x32x16_bf16 v[144:159], v[172:175], v[208:211], v[144:159]\n"
      "s_add_u32 s80, s70, 0x2000\n"
      "buffer_load_dwordx4 v[176:179], v1, s[20:23], s80 offen sc1\n"
      "s_mov_b32 m0, s82\n"
      "s_add_u32 s78, s72, 0xb800\n"
      "buffer_load_dwordx4 v1, s[20:23], s78 offen sc1 lds\n"
      "s_waitcnt vmcnt(0)\n"
      "ds_read_b64_tr_b16 v[208:209], v228 offset:0\n"
      "ds_read_b64_tr_b16 v[210:211], v228 offset:512\n"
      "s_waitcnt lgkmcnt(0)\n"
      "v_mfma_f32_32x32x16_bf16 v[192:207], v[208:211], v[176:179], v[192:207]\n"
      "s_mov_b32 m0, s82\n"
      "s_add_u32 s78, s72, 0xf000\n"
      "buffer_load_dwordx4 v1, s[20:23], s78 offen sc1 lds\n"
      "s_waitcnt vmcnt(0)\n"
      "ds_read_b64_tr_b16 v[208:209], v228 offset:0\n"
      "ds_read_b64_tr_b16 v[210:211], v228 offset:512\n"
      "s_waitcnt lgkmcnt(0)\n"
      "v_mfma_f32_32x32x16_bf16 v[128:143], v[176:179], v[208:211], v[128:143]\n"
      "s_mov_b32 m0, s82\n"
      "s_add_u32 s78, s72, 0x12800\n"
      "buffer_load_dwordx4 v1, s[20:23], s78 offen sc1 lds\n"
      "s_waitcnt vmcnt(0)\n"
      "ds_read_b64_tr_b16 v[208:209], v228 offset:0\n"
      "ds_read_b64_tr_b16 v[210:211], v228 offset:512\n"
      "s_waitcnt lgkmcnt(0)\n"
      "v_mfma_f32_32x32x16_bf16 v[144:159], v[176:179], v[208:211], v[144:159]\n"
      "buffer_load_dwordx4 v[180:183], v1, s[20:23], s80 offen offset:1024 sc1\n"
      "s_mov_b32 m0, s82\n"
      "s_add_u32 s78, s72, 0xbc00\n"
      "buffer_load_dwordx4 v1, s[20:23], s78 offen sc1 lds\n"
      "s_waitcnt vmcnt(0)\n"
      "ds_read_b64_tr_b16 v[208:209], v228 offset:0\n"
      "ds_read_b64_tr_b16 v[210:211], v228 offset:512\n"
      "s_waitcnt lgkmcnt(0)\n"
      "v_mfma_f32_32x32x16_bf16 v[192:207], v[208:211], v[180:183], v[192:207]\n"
      "s_mov_b32 m0, s82\n"
      "s_add_u32 s78, s72, 0xf400\n"
      "buffer_load_dwordx4 v1, s[20:23], s78 offen sc1 lds\n"
      "s_waitcnt vmcnt(0)\n"
      "ds_read_b64_tr_b16 v[208:209], v228 offset:0\n"
      "ds_read_b64_tr_b16 v[210:211], v228 offset:512\n"
      "s_waitcnt lgkmcnt(0)\n"
      "v_mfma_f32_32x32x16_bf16 v[128:143], v[180:183], v[208:211], v[128:143]\n"
      "s_mov_b32 m0, s82\n"
      "s_add_u32 s78, s72, 0x12c00\n"
      "buffer_load_dwordx4 v1, s[20:23], s78 offen sc1 lds\n"
      "s_waitcnt vmcnt(0)\n"
      "ds_read_b64_tr_b16 v[208:209], v228 offset:0\n"
      "ds_read_b64_tr_b16 v[210:211], v228 offset:512\n"
      "s_waitcnt lgkmcnt(0)\n"
      "v_mfma_f32_32x32x16_bf16 v[144:159], v[180:183], v[208:211], v[144:159]\n"
      "s_add_u32 s80, s70, 0x3000\n"
      "buffer_load_dwordx4 v[184:187], v1, s[20:23], s80 offen sc1\n"
      "s_mov_b32 m0, s82\n"
      "s_add_u32 s78, s72, 0xc000\n"
      "buffer_load_dwordx4 v1, s[20:23], s78 offen sc1 lds\n"
      "s_waitcnt vmcnt(0)\n"
      "ds_read_b64_tr_b16 v[208:209], v228 offset:0\n"
      "ds_read_b64_tr_b16 v[210:211], v228 offset:512\n"
      "s_waitcnt lgkmcnt(0)\n"
      "v_mfma_f32_32x32x16_bf16 v[192:207], v[208:211], v[184:187], v[192:207]\n"
      "s_mov_b32 m0, s82\n"
      "s_add_u32 s78, s72, 0xf800\n"
      "buffer_load_dwordx4 v1, s[20:23], s78 offen sc1 lds\n"
      "s_waitcnt vmcnt(0)\n"
      "ds_read_b64_tr_b16 v[208:209], v228 offset:0\n"
      "ds_read_b64_tr_b16 v[210:211], v228 offset:512\n"
      "s_waitcnt lgkmcnt(0)\n"
      "v_mfma_f32_32x32x16_bf16 v[128:143], v[184:187], v[208:211], v[128:143]\n"
      "s_mov_b32 m0, s82\n"
      "s_add_u32 s78, s72, 0x13000\n"
      "buffer_load_dwordx4 v1, s[20:23], s78 offen sc1 lds\n"
      "s_waitcnt vmcnt(0)\n"
      "ds_read_b64_tr_b16 v[208:209], v228 offset:0\n"
      "ds_read_b64_tr_b16 v[210:211], v228 offset:512\n"
      "s_waitcnt lgkmcnt(0)\n"
      "v_mfma_f32_32x32x16_bf16 v[144:159], v[184:187], v[208:211], v[144:159]\n"
      "buffer_load_dwordx4 v[188:191], v1, s[20:23], s80 offen offset:1024 sc1\n"
      "s_mov_b32 m0, s82\n"
      "s_add_u32 s78, s72, 0xc400\n"
      "buffer_load_dwordx4 v1, s[20:23], s78 offen sc1 lds\n"
      "s_waitcnt vmcnt(0)\n"
      "ds_read_b64_tr_b16 v[208:209], v228 offset:0\n"
      "ds_read_b64_tr_b16 v[210:211], v228 offset:512\n"
      "s_waitcnt lgkmcnt(0)\n"
      "v_mfma_f32_32x32x16_bf16 v[192:207], v[208:211], v[188:191], v[192:207]\n"
      "s_mov_b32 m0, s82\n"
      "s_add_u32 s78, s72, 0xfc00\n"
      "buffer_load_dwordx4 v1, s[20:23], s78 offen sc1 lds\n"
      "s_waitcnt vmcnt(0)\n"
      "ds_read_b64_tr_b16 v[208:209], v228 offset:0\n"
      "ds_read_b64_tr_b16 v[210:211], v228 offset:512\n"
      "s_waitcnt lgkmcnt(0)\n"
      "v_mfma_f32_32x32x16_bf16 v[128:143], v[188:191], v[208:211], v[128:143]\n"
      "s_mov_b32 m0, s82\n"
      "s_add_u32 s78, s72, 0x13400\n"
      "buffer_load_dwordx4 v1, s[20:23], s78 offen sc1 lds\n"
      "s_waitcnt vmcnt(0)\n"
      "ds_read_b64_tr_b16 v[208:209], v228 offset:0\n"
      "ds_read_b64_tr_b16 v[210:211], v228 offset:512\n"
      "s_waitcnt lgkmcnt(0)\n"
      "v_mfma_f32_32x32x16_bf16 v[144:159], v[188:191], v[208:211], v[144:159]\n"
      "s_add_u32 s80, s70, 0x4000\n"
      "buffer_load_dwordx4 v[160:163], v1, s[20:23], s80 offen sc1\n"
      "s_mov_b32 m0, s82\n"
      "s_add_u32 s78, s72, 0xc800\n"
      "buffer_load_dwordx4 v1, s[20:23], s78 offen sc1 lds\n"
      "s_waitcnt vmcnt(0)\n"
      "ds_read_b64_tr_b16 v[208:209], v228 offset:0\n"
      "ds_read_b64_tr_b16 v[210:211], v228 offset:512\n"
      "s_waitcnt lgkmcnt(0)\n"
      "v_mfma_f32_32x32x16_bf16 v[192:207], v[208:211], v[160:163], v[192:207]\n"
      "s_mov_b32 m0, s82\n"
      "s_add_u32 s78, s72, 0x10000\n"
      "buffer_load_dwordx4 v1, s[20:23], s78 offen sc1 lds\n"
      "s_waitcnt vmcnt(0)\n"
      "ds_read_b64_tr_b16 v[208:209], v228 offset:0\n"
      "ds_read_b64_tr_b16 v[210:211], v228 offset:512\n"
      "s_waitcnt lgkmcnt(0)\n"
      "v_mfma_f32_32x32x16_bf16 v[128:143], v[160:163], v[208:211], v[128:143]\n"
      "s_mov_b32 m0, s82\n"
      "s_add_u32 s78, s72, 0x13800\n"
      "buffer_load_dwordx4 v1, s[20:23], s78 offen sc1 lds\n"
      "s_waitcnt vmcnt(0)\n"
      "ds_read_b64_tr_b16 v[208:209], v228 offset:0\n"
      "ds_read_b64_tr_b16 v[210:211], v228 offset:512\n"
      "s_waitcnt lgkmcnt(0)\n"
      "v_mfma_f32_32x32x16_bf16 v[144:159], v[160:163], v[208:211], v[144:159]\n"
      "buffer_load_dwordx4 v[164:167], v1, s[20:23], s80 offen offset:1024 sc1\n"
      "s_mov_b32 m0, s82\n"
      "s_add_u32 s78, s72, 0xcc00\n"
      "buffer_load_dwordx4 v1, s[20:23], s78 offen sc1 lds\n"
      "s_waitcnt vmcnt(0)\n"
      "ds_read_b64_tr_b16 v[208:209], v228 offset:0\n"
      "ds_read_b64_tr_b16 v[210:211], v228 offset:512\n"
      "s_waitcnt lgkmcnt(0)\n"
      "v_mfma_f32_32x32x16_bf16 v[192:207], v[208:211], v[164:167], v[192:207]\n"
      "s_mov_b32 m0, s82\n"
      "s_add_u32 s78, s72, 0x10400\n"
      "buffer_load_dwordx4 v1, s[20:23], s78 offen sc1 lds\n"
      "s_waitcnt vmcnt(0)\n"
      "ds_read_b64_tr_b16 v[208:209], v228 offset:0\n"
      "ds_read_b64_tr_b16 v[210:211], v228 offset:512\n"
      "s_waitcnt lgkmcnt(0)\n"
      "v_mfma_f32_32x32x16_bf16 v[128:143], v[164:167], v[208:211], v[128:143]\n"
      "s_mov_b32 m0, s82\n"
      "s_add_u32 s78, s72, 0x13c00\n"
      "buffer_load_dwordx4 v1, s[20:23], s78 offen sc1 lds\n"
      "s_waitcnt vmcnt(0)\n"
      "ds_read_b64_tr_b16 v[208:209], v228 offset:0\n"
      "ds_read_b64_tr_b16 v[210:211], v228 offset:512\n"
      "s_waitcnt lgkmcnt(0)\n"
      "v_mfma_f32_32x32x16_bf16 v[144:159], v[164:167], v[208:211], v[144:159]\n"
      "s_add_u32 s80, s70, 0x5000\n"
      "buffer_load_dwordx4 v[168:171], v1, s[20:23], s80 offen sc1\n"
      "s_mov_b32 m0, s82\n"
      "s_add_u32 s78, s72, 0xd000\n"
      "buffer_load_dwordx4 v1, s[20:23], s78 offen sc1 lds\n"
      "s_waitcnt vmcnt(0)\n"
      "ds_read_b64_tr_b16 v[208:209], v228 offset:0\n"
      "ds_read_b64_tr_b16 v[210:211], v228 offset:512\n"
      "s_waitcnt lgkmcnt(0)\n"
      "v_mfma_f32_32x32x16_bf16 v[192:207], v[208:211], v[168:171], v[192:207]\n"
      "s_mov_b32 m0, s82\n"
      "s_add_u32 s78, s72, 0x10800\n"
      "buffer_load_dwordx4 v1, s[20:23], s78 offen sc1 lds\n"
      "s_waitcnt vmcnt(0)\n"
      "ds_read_b64_tr_b16 v[208:209], v228 offset:0\n"
      "ds_read_b64_tr_b16 v[210:211], v228 offset:512\n"
      "s_waitcnt lgkmcnt(0)\n"
      "v_mfma_f32_32x32x16_bf16 v[128:143], v[168:171], v[208:211], v[128:143]\n"
      "s_mov_b32 m0, s82\n"
      "s_add_u32 s78, s72, 0x14000\n"
      "buffer_load_dwordx4 v1, s[20:23], s78 offen sc1 lds\n"
      "s_waitcnt vmcnt(0)\n"
      "ds_read_b64_tr_b16 v[208:209], v228 offset:0\n"
      "ds_read_b64_tr_b16 v[210:211], v228 offset:512\n"
      "s_waitcnt lgkmcnt(0)\n"
      "v_mfma_f32_32x32x16_bf16 v[144:159], v[168:171], v[208:211], v[144:159]\n"
      "buffer_load_dwordx4 v[172:175], v1, s[20:23], s80 offen offset:1024 sc1\n"
      "s_mov_b32 m0, s82\n"
      "s_add_u32 s78, s72, 0xd400\n"
      "buffer_load_dwordx4 v1, s[20:23], s78 offen sc1 lds\n"
      "s_waitcnt vmcnt(0)\n"
      "ds_read_b64_tr_b16 v[208:209], v228 offset:0\n"
      "ds_read_b64_tr_b16 v[210:211], v228 offset:512\n"
      "s_waitcnt lgkmcnt(0)\n"
      "v_mfma_f32_32x32x16_bf16 v[192:207], v[208:211], v[172:175], v[192:207]\n"
      "s_mov_b32 m0, s82\n"
      "s_add_u32 s78, s72, 0x10c00\n"
      "buffer_load_dwordx4 v1, s[20:23], s78 offen sc1 lds\n"
      "s_waitcnt vmcnt(0)\n"
      "ds_read_b64_tr_b16 v[208:209], v228 offset:0\n"
      "ds_read_b64_tr_b16 v[210:211], v228 offset:512\n"
      "s_waitcnt lgkmcnt(0)\n"
      "v_mfma_f32_32x32x16_bf16 v[128:143], v[172:175], v[208:211], v[128:143]\n"
      "s_mov_b32 m0, s82\n"
      "s_add_u32 s78, s72, 0x14400\n"
      "buffer_load_dwordx4 v1, s[20:23], s78 offen sc1 lds\n"
      "s_waitcnt vmcnt(0)\n"
      "ds_read_b64_tr_b16 v[208:209], v228 offset:0\n"
      "ds_read_b64_tr_b16 v[210:211], v228 offset:512\n"
      "s_waitcnt lgkmcnt(0)\n"
      "v_mfma_f32_32x32x16_bf16 v[144:159], v[172:175], v[208:211], v[144:159]\n"
      "s_add_u32 s80, s70, 0x6000\n"
      "buffer_load_dwordx4 v[176:179], v1, s[20:23], s80 offen sc1\n"
      "s_mov_b32 m0, s82\n"
      "s_add_u32 s78, s72, 0xd800\n"
      "buffer_load_dwordx4 v1, s[20:23], s78 offen sc1 lds\n"
      "s_waitcnt vmcnt(0)\n"
      "ds_read_b64_tr_b16 v[208:209], v228 offset:0\n"
      "ds_read_b64_tr_b16 v[210:211], v228 offset:512\n"
      "s_waitcnt lgkmcnt(0)\n"
      "v_mfma_f32_32x32x16_bf16 v[192:207], v[208:211], v[176:179], v[192:207]\n"
      "s_mov_b32 m0, s82\n"
      "s_add_u32 s78, s72, 0x11000\n"
      "buffer_load_dwordx4 v1, s[20:23], s78 offen sc1 lds\n"
      "s_waitcnt vmcnt(0)\n"
      "ds_read_b64_tr_b16 v[208:209], v228 offset:0\n"
      "ds_read_b64_tr_b16 v[210:211], v228 offset:512\n"
      "s_waitcnt lgkmcnt(0)\n"
      "v_mfma_f32_32x32x16_bf16 v[128:143], v[176:179], v[208:211], v[128:143]\n"
      "s_mov_b32 m0, s82\n"
      "s_add_u32 s78, s72, 0x14800\n"
      "buffer_load_dwordx4 v1, s[20:23], s78 offen sc1 lds\n"
      "s_waitcnt vmcnt(0)\n"
      "ds_read_b64_tr_b16 v[208:209], v228 offset:0\n"
      "ds_read_b64_tr_b16 v[210:211], v228 offset:512\n"
      "s_waitcnt lgkmcnt(0)\n"
      "v_mfma_f32_32x32x16_bf16 v[144:159], v[176:179], v[208:211], v[144:159]\n"
      "buffer_load_dwordx4 v[180:183], v1, s[20:23], s80 offen offset:1024 sc1\n"
      "s_mov_b32 m0, s82\n"
      "s_add_u32 s78, s72, 0xdc00\n"
      "buffer_load_dwordx4 v1, s[20:23], s78 offen sc1 lds\n"
      "s_waitcnt vmcnt(0)\n"
      "ds_read_b64_tr_b16 v[208:209], v228 offset:0\n"
      "ds_read_b64_tr_b16 v[210:211], v228 offset:512\n"
      "s_waitcnt lgkmcnt(0)\n"
      "v_mfma_f32_32x32x16_bf16 v[192:207], v[208:211], v[180:183], v[192:207]\n"
      "s_mov_b32 m0, s82\n"
      "s_add_u32 s78, s72, 0x11400\n"
      "buffer_load_dwordx4 v1, s[20:23], s78 offen sc1 lds\n"
      "s_waitcnt vmcnt(0)\n"
      "ds_read_b64_tr_b16 v[208:209], v228 offset:0\n"
      "ds_read_b64_tr_b16 v[210:211], v228 offset:512\n"
      "s_waitcnt lgkmcnt(0)\n"
      "v_mfma_f32_32x32x16_bf16 v[128:143], v[180:183], v[208:211], v[128:143]\n"
      "s_mov_b32 m0, s82\n"
      "s_add_u32 s78, s72, 0x14c00\n"
      "buffer_load_dwordx4 v1, s[20:23], s78 offen sc1 lds\n"
      "s_waitcnt vmcnt(0)\n"
      "ds_read_b64_tr_b16 v[208:209], v228 offset:0\n"
      "ds_read_b64_tr_b16 v[210:211], v228 offset:512\n"
      "s_waitcnt lgkmcnt(0)\n"
      "v_mfma_f32_32x32x16_bf16 v[144:159], v[180:183], v[208:211], v[144:159]\n"
      "s_mov_b32 s80, s70\n"
      "buffer_load_dwordx4 v[184:187], v1, s[20:23], s80 offen offset:2048 sc1\n"
      "s_mov_b32 m0, s82\n"
      "s_mov_b32 s78, s72\n"
      "buffer_load_dwordx4 v1, s[20:23], s78 offen sc1 lds\n"
      "s_waitcnt vmcnt(0)\n"
      "ds_read_b64_tr_b16 v[208:209], v228 offset:0\n"
      "ds_read_b64_tr_b16 v[210:211], v228 offset:512\n"
      "s_waitcnt lgkmcnt(0)\n"
      "v_mfma_f32_32x32x16_bf16 v[192:207], v[208:211], v[184:187], v[192:207]\n"
      "s_mov_b32 m0, s82\n"
      "s_add_u32 s78, s72, 0x3800\n"
      "buffer_load_dwordx4 v1, s[20:23], s78 offen sc1 lds\n"
      "s_waitcnt vmcnt(0)\n"
      "ds_read_b64_tr_b16 v[208:209], v228 offset:0\n"
      "ds_read_b64_tr_b16 v[210:211], v228 offset:512\n"
      "s_waitcnt lgkmcnt(0)\n"
      "v_mfma_f32_32x32x16_bf16 v[128:143], v[184:187], v[208:211], v[128:143]\n"
      "s_mov_b32 m0, s82\n"
      "s_add_u32 s78, s72, 0x7000\n"
      "buffer_load_dwordx4 v1, s[20:23], s78 offen sc1 lds\n"
      "s_waitcnt vmcnt(0)\n"
      "ds_read_b64_tr_b16 v[208:209], v228 offset:0\n"
      "ds_read_b64_tr_b16 v[210:211], v228 offset:512\n"
      "s_waitcnt lgkmcnt(0)\n"
      "v_mfma_f32_32x32x16_bf16 v[144:159], v[184:187], v[208:211], v[144:159]\n"
      "buffer_load_dwordx4 v[188:191], v1, s[20:23], s80 offen offset:3072 sc1\n"
      "s_mov_b32 m0, s82\n"
      "s_add_u32 s78, s72, 0x400\n"
      "buffer_load_dwordx4 v1, s[20:23], s78 offen sc1 lds\n"
      "s_waitcnt vmcnt(0)\n"
      "ds_read_b64_tr_b16 v[208:209], v228 offset:0\n"
      "ds_read_b64_tr_b16 v[210:211], v228 offset:512\n"
      "s_waitcnt lgkmcnt(0)\n"
      "v_mfma_f32_32x32x16_bf16 v[192:207], v[208:211], v[188:191], v[192:207]\n"
      "s_mov_b32 m0, s82\n"
      "s_add_u32 s78, s72, 0x3c00\n"
      "buffer_load_dwordx4 v1, s[20:23], s78 offen sc1 lds\n"
      "s_waitcnt vmcnt(0)\n"
      "ds_read_b64_tr_b16 v[208:209], v228 offset:0\n"
      "ds_read_b64_tr_b16 v[210:211], v228 offset:512\n"
      "s_waitcnt lgkmcnt(0)\n"
      "v_mfma_f32_32x32x16_bf16 v[128:143], v[188:191], v[208:211], v[128:143]\n"
      "s_mov_b32 m0, s82\n"
      "s_add_u32 s78, s72, 0x7400\n"
      "buffer_load_dwordx4 v1, s[20:23], s78 offen sc1 lds\n"
      "s_waitcnt vmcnt(0)\n"
      "ds_read_b64_tr_b16 v[208:209], v228 offset:0\n"
      "ds_read_b64_tr_b16 v[210:211], v228 offset:512\n"
      "s_waitcnt lgkmcnt(0)\n"
      "v_mfma_f32_32x32x16_bf16 v[144:159], v[188:191], v[208:211], v[144:159]\n"
      "s_add_u32 s80, s70, 0x1000\n"
      "buffer_load_dwordx4 v[160:163], v1, s[20:23], s80 offen offset:2048 sc1\n"
      "s_mov_b32 m0, s82\n"
      "s_add_u32 s78, s72, 0x800\n"
      "buffer_load_dwordx4 v1, s[20:23], s78 offen sc1 lds\n"
      "s_waitcnt vmcnt(0)\n"
      "ds_read_b64_tr_b16 v[208:209], v228 offset:0\n"
      "ds_read_b64_tr_b16 v[210:211], v228 offset:512\n"
      "s_waitcnt lgkmcnt(0)\n"
      "v_mfma_f32_32x32x16_bf16 v[192:207], v[208:211], v[160:163], v[192:207]\n"
      "s_mov_b32 m0, s82\n"
      "s_add_u32 s78, s72, 0x4000\n"
      "buffer_load_dwordx4 v1, s[20:23], s78 offen sc1 lds\n"
      "s_waitcnt vmcnt(0)\n"
      "ds_read_b64_tr_b16 v[208:209], v228 offset:0\n"
      "ds_read_b64_tr_b16 v[210:211], v228 offset:512\n"
      "s_waitcnt lgkmcnt(0)\n"
      "v_mfma_f32_32x32x16_bf16 v[128:143], v[160:163], v[208:211], v[128:143]\n"
      "s_mov_b32 m0, s82\n"
      "s_add_u32 s78, s72, 0x7800\n"
      "buffer_load_dwordx4 v1, s[20:23], s78 offen sc1 lds\n"
      "s_waitcnt vmcnt(0)\n"
      "ds_read_b64_tr_b16 v[208:209], v228 offset:0\n"
      "ds_read_b64_tr_b16 v[210:211], v228 offset:512\n"
      "s_waitcnt lgkmcnt(0)\n"
      "v_mfma_f32_32x32x16_bf16 v[144:159], v[160:163], v[208:211], v[144:159]\n"
      "buffer_load_dwordx4 v[164:167], v1, s[20:23], s80 offen offset:3072 sc1\n"
      "s_mov_b32 m0, s82\n"
      "s_add_u32 s78, s72, 0xc00\n"
      "buffer_load_dwordx4 v1, s[20:23], s78 offen sc1 lds\n"
      "s_waitcnt vmcnt(0)\n"
      "ds_read_b64_tr_b16 v[208:209], v228 offset:0\n"
      "ds_read_b64_tr_b16 v[210:211], v228 offset:512\n"
      "s_waitcnt lgkmcnt(0)\n"
      "v_mfma_f32_32x32x16_bf16 v[192:207], v[208:211], v[164:167], v[192:207]\n"
      "s_mov_b32 m0, s82\n"
      "s_add_u32 s78, s72, 0x4400\n"
      "buffer_load_dwordx4 v1, s[20:23], s78 offen sc1 lds\n"
      "s_waitcnt vmcnt(0)\n"
      "ds_read_b64_tr_b16 v[208:209], v228 offset:0\n"
      "ds_read_b64_tr_b16 v[210:211], v228 offset:512\n"
      "s_waitcnt lgkmcnt(0)\n"
      "v_mfma_f32_32x32x16_bf16 v[128:143], v[164:167], v[208:211], v[128:143]\n"
      "s_mov_b32 m0, s82\n"
      "s_add_u32 s78, s72, 0x7c00\n"
      "buffer_load_dwordx4 v1, s[20:23], s78 offen sc1 lds\n"
      "s_waitcnt vmcnt(0)\n"
      "ds_read_b64_tr_b16 v[208:209], v228 offset:0\n"
      "ds_read_b64_tr_b16 v[210:211], v228 offset:512\n"
      "s_waitcnt lgkmcnt(0)\n"
      "v_mfma_f32_32x32x16_bf16 v[144:159], v[164:167], v[208:211], v[144:159]\n"
      "s_add_u32 s80, s70, 0x2000\n"
      "buffer_load_dwordx4 v[168:171], v1, s[20:23], s80 offen offset:2048 sc1\n"
      "s_mov_b32 m0, s82\n"
      "s_add_u32 s78, s72, 0x1000\n"
      "buffer_load_dwordx4 v1, s[20:23], s78 offen sc1 lds\n"
      "s_waitcnt vmcnt(0)\n"
      "ds_read_b64_tr_b16 v[208:209], v228 offset:0\n"
      "ds_read_b64_tr_b16 v[210:211], v228 offset:512\n"
      "s_waitcnt lgkmcnt(0)\n"
      "v_mfma_f32_32x32x16_bf16 v[192:207], v[208:211], v[168:171], v[192:207]\n"
      "s_mov_b32 m0, s82\n"
      "s_add_u32 s78, s72, 0x4800\n"
      "buffer_load_dwordx4 v1, s[20:23], s78 offen sc1 lds\n"
      "s_waitcnt vmcnt(0)\n"
      "ds_read_b64_tr_b16 v[208:209], v228 offset:0\n"
      "ds_read_b64_tr_b16 v[210:211], v228 offset:512\n"
      "s_waitcnt lgkmcnt(0)\n"
      "v_mfma_f32_32x32x16_bf16 v[128:143], v[168:171], v[208:211], v[128:143]\n"
      "s_mov_b32 m0, s82\n"
      "s_add_u32 s78, s72, 0x8000\n"
      "buffer_load_dwordx4 v1, s[20:23], s78 offen sc1 lds\n"
      "s_waitcnt vmcnt(0)\n"
      "ds_read_b64_tr_b16 v[208:209], v228 offset:0\n"
      "ds_read_b64_tr_b16 v[210:211], v228 offset:512\n"
      "s_waitcnt lgkmcnt(0)\n"
      "v_mfma_f32_32x32x16_bf16 v[144:159], v[168:171], v[208:211], v[144:159]\n"
      "buffer_load_dwordx4 v[172:175], v1, s[20:23], s80 offen offset:3072 sc1\n"
      "s_mov_b32 m0, s82\n"
      "s_add_u32 s78, s72, 0x1400\n"
      "buffer_load_dwordx4 v1, s[20:23], s78 offen sc1 lds\n"
      "s_waitcnt vmcnt(0)\n"
      "ds_read_b64_tr_b16 v[208:209], v228 offset:0\n"
      "ds_read_b64_tr_b16 v[210:211], v228 offset:512\n"
      "s_waitcnt lgkmcnt(0)\n"
      "v_mfma_f32_32x32x16_bf16 v[192:207], v[208:211], v[172:175], v[192:207]\n"
      "s_mov_b32 m0, s82\n"
      "s_add_u32 s78, s72, 0x4c00\n"
      "buffer_load_dwordx4 v1, s[20:23], s78 offen sc1 lds\n"
      "s_waitcnt vmcnt(0)\n"
      "ds_read_b64_tr_b16 v[208:209], v228 offset:0\n"
      "ds_read_b64_tr_b16 v[210:211], v228 offset:512\n"
      "s_waitcnt lgkmcnt(0)\n"
      "v_mfma_f32_32x32x16_bf16 v[128:143], v[172:175], v[208:211], v[128:143]\n"
      "s_mov_b32 m0, s82\n"
      "s_add_u32 s78, s72, 0x8400\n"
      "buffer_load_dwordx4 v1, s[20:23], s78 offen sc1 lds\n"
      "s_waitcnt vmcnt(0)\n"
      "ds_read_b64_tr_b16 v[208:209], v228 offset:0\n"
      "ds_read_b64_tr_b16 v[210:211], v228 offset:512\n"
      "s_waitcnt lgkmcnt(0)\n"
      "v_mfma_f32_32x32x16_bf16 v[144:159], v[172:175], v[208:211], v[144:159]\n"
      "s_add_u32 s80, s70, 0x3000\n"
      "buffer_load_dwordx4 v[176:179], v1, s[20:23], s80 offen offset:2048 sc1\n"
      "s_mov_b32 m0, s82\n"
      "s_add_u32 s78, s72, 0x5000\n"
      "buffer_load_dwordx4 v1, s[20:23], s78 offen sc1 lds\n"
      "s_waitcnt vmcnt(0)\n"
      "ds_read_b64_tr_b16 v[208:209], v228 offset:0\n"
      "ds_read_b64_tr_b16 v[210:211], v228 offset:512\n"
      "s_waitcnt lgkmcnt(0)\n"
      "v_mfma_f32_32x32x16_bf16 v[128:143], v[176:179], v[208:211], v[128:143]\n"
      "s_mov_b32 m0, s82\n"
      "s_add_u32 s78, s72, 0x8800\n"
      "buffer_load_dwordx4 v1, s[20:23], s78 offen sc1 lds\n"
      "s_waitcnt vmcnt(0)\n"
      "ds_read_b64_tr_b16 v[208:209], v228 offset:0\n"
      "ds_read_b64_tr_b16 v[210:211], v228 offset:512\n"
      "s_waitcnt lgkmcnt(0)\n"
      "v_mfma_f32_32x32x16_bf16 v[144:159], v[176:179], v[208:211], v[144:159]\n"
      "buffer_load_dwordx4 v[180:183], v1, s[20:23], s80 offen offset:3072 sc1\n"
      "s_mov_b32 m0, s82\n"
      "s_add_u32 s78, s72, 0x5400\n"
      "buffer_load_dwordx4 v1, s[20:23], s78 offen sc1 lds\n"
      "s_waitcnt vmcnt(0)\n"
      "ds_read_b64_tr_b16 v[208:209], v228 offset:0\n"
      "ds_read_b64_tr_b16 v[210:211], v228 offset:512\n"
      "s_waitcnt lgkmcnt(0)\n"
      "v_mfma_f32_32x32x16_bf16 v[128:143], v[180:183], v[208:211], v[128:143]\n"
      "s_mov_b32 m0, s82\n"
      "s_add_u32 s78, s72, 0x8c00\n"
      "buffer_load_dwordx4 v1, s[20:23], s78 offen sc1 lds\n"
      "s_waitcnt vmcnt(0)\n"
      "ds_read_b64_tr_b16 v[208:209], v228 offset:0\n"
      "ds_read_b64_tr_b16 v[210:211], v228 offset:512\n"
      "s_waitcnt lgkmcnt(0)\n"
      "v_mfma_f32_32x32x16_bf16 v[144:159], v[180:183], v[208:211], v[144:159]\n"
      "s_add_u32 s80, s70, 0x4000\n"
      "buffer_load_dwordx4 v[184:187], v1, s[20:23], s80 offen offset:2048 sc1\n"
      "s_mov_b32 m0, s82\n"
      "s_add_u32 s78, s72, 0x5800\n"
      "buffer_load_dwordx4 v1, s[20:23], s78 offen sc1 lds\n"
      "s_waitcnt vmcnt(0)\n"
      "ds_read_b64_tr_b16 v[208:209], v228 offset:0\n"
      "ds_read_b64_tr_b16 v[210:211], v228 offset:512\n"
      "s_waitcnt lgkmcnt(0)\n"
      "v_mfma_f32_32x32x16_bf16 v[128:143], v[184:187], v[208:211], v[128:143]\n"
      "s_mov_b32 m0, s82\n"
      "s_add_u32 s78, s72, 0x9000\n"
      "buffer_load_dwordx4 v1, s[20:23], s78 offen sc1 lds\n"
      "s_waitcnt vmcnt(0)\n"
      "ds_read_b64_tr_b16 v[208:209], v228 offset:0\n"
      "ds_read_b64_tr_b16 v[210:211], v228 offset:512\n"
      "s_waitcnt lgkmcnt(0)\n"
      "v_mfma_f32_32x32x16_bf16 v[144:159], v[184:187], v[208:211], v[144:159]\n"
      "buffer_load_dwordx4 v[188:191], v1, s[20:23], s80 offen offset:3072 sc1\n"
      "s_mov_b32 m0, s82\n"
      "s_add_u32 s78, s72, 0x5c00\n"
      "buffer_load_dwordx4 v1, s[20:23], s78 offen sc1 lds\n"
      "s_waitcnt vmcnt(0)\n"
      "ds_read_b64_tr_b16 v[208:209], v228 offset:0\n"
      "ds_read_b64_tr_b16 v[210:211], v228 offset:512\n"
      "s_waitcnt lgkmcnt(0)\n"
      "v_mfma_f32_32x32x16_bf16 v[128:143], v[188:191], v[208:211], v[128:143]\n"
      "s_mov_b32 m0, s82\n"
      "s_add_u32 s78, s72, 0x9400\n"
      "buffer_load_dwordx4 v1, s[20:23], s78 offen sc1 lds\n"
      "s_waitcnt vmcnt(0)\n"
      "ds_read_b64_tr_b16 v[208:209], v228 offset:0\n"
      "ds_read_b64_tr_b16 v[210:211], v228 offset:512\n"
      "s_waitcnt lgkmcnt(0)\n"
      "v_mfma_f32_32x32x16_bf16 v[144:159], v[188:191], v[208:211], v[144:159]\n"
      "s_add_u32 s80, s70, 0x5000\n"
      "buffer_load_dwordx4 v[160:163], v1, s[20:23], s80 offen offset:2048 sc1\n"
      "s_mov_b32 m0, s82\n"
      "s_add_u32 s78, s72, 0x6000\n"
      "buffer_load_dwordx4 v1, s[20:23], s78 offen sc1 lds\n"
      "s_waitcnt vmcnt(0)\n"
      "ds_read_b64_tr_b16 v[208:209], v228 offset:0\n"
      "ds_read_b64_tr_b16 v[210:211], v228 offset:512\n"
      "s_waitcnt lgkmcnt(0)\n"
      "v_mfma_f32_32x32x16_bf16 v[128:143], v[160:163], v[208:211], v[128:143]\n"
      "s_mov_b32 m0, s82\n"
      "s_add_u32 s78, s72, 0x9800\n"
      "buffer_load_dwordx4 v1, s[20:23], s78 offen sc1 lds\n"
      "s_waitcnt vmcnt(0)\n"
      "ds_read_b64_tr_b16 v[208:209], v228 offset:0\n"
      "ds_read_b64_tr_b16 v[210:211], v228 offset:512\n"
      "s_waitcnt lgkmcnt(0)\n"
      "v_mfma_f32_32x32x16_bf16 v[144:159], v[160:163], v[208:211], v[144:159]\n"
      "buffer_load_dwordx4 v[164:167], v1, s[20:23], s80 offen offset:3072 sc1\n"
      "s_mov_b32 m0, s82\n"
      "s_add_u32 s78, s72, 0x6400\n"
      "buffer_load_dwordx4 v1, s[20:23], s78 offen sc1 lds\n"
      "s_waitcnt vmcnt(0)\n"
      "ds_read_b64_tr_b16 v[208:209], v228 offset:0\n"
      "ds_read_b64_tr_b16 v[210:211], v228 offset:512\n"
      "s_waitcnt lgkmcnt(0)\n"
      "v_mfma_f32_32x32x16_bf16 v[128:143], v[164:167], v[208:211], v[128:143]\n"
      "s_mov_b32 m0, s82\n"
      "s_add_u32 s78, s72, 0x9c00\n"
      "buffer_load_dwordx4 v1, s[20:23], s78 offen sc1 lds\n"
      "s_waitcnt vmcnt(0)\n"
      "ds_read_b64_tr_b16 v[208:209], v228 offset:0\n"
      "ds_read_b64_tr_b16 v[210:211], v228 offset:512\n"
      "s_waitcnt lgkmcnt(0)\n"
      "v_mfma_f32_32x32x16_bf16 v[144:159], v[164:167], v[208:211], v[144:159]\n"
      "s_add_u32 s80, s70, 0x6000\n"
      "buffer_load_dwordx4 v[168:171], v1, s[20:23], s80 offen offset:2048 sc1\n"
      "s_mov_b32 m0, s82\n"
      "s_add_u32 s78, s72, 0x6800\n"
      "buffer_load_dwordx4 v1, s[20:23], s78 offen sc1 lds\n"
      "s_waitcnt vmcnt(0)\n"
      "ds_read_b64_tr_b16 v[208:209], v228 offset:0\n"
      "ds_read_b64_tr_b16 v[210:211], v228 offset:512\n"
      "s_waitcnt lgkmcnt(0)\n"
      "v_mfma_f32_32x32x16_bf16 v[128:143], v[168:171], v[208:211], v[128:143]\n"
      "s_mov_b32 m0, s82\n"
      "s_add_u32 s78, s72, 0xa000\n"
      "buffer_load_dwordx4 v1, s[20:23], s78 offen sc1 lds\n"
      "s_waitcnt vmcnt(0)\n"
      "ds_read_b64_tr_b16 v[208:209], v228 offset:0\n"
      "ds_read_b64_tr_b16 v[210:211], v228 offset:512\n"
      "s_waitcnt lgkmcnt(0)\n"
      "v_mfma_f32_32x32x16_bf16 v[144:159], v[168:171], v[208:211], v[144:159]\n"
      "buffer_load_dwordx4 v[172:175], v1, s[20:23], s80 offen offset:3072 sc1\n"
      "s_mov_b32 m0, s82\n"
      "s_add_u32 s78, s72, 0x6c00\n"
      "buffer_load_dwordx4 v1, s[20:23], s78 offen sc1 lds\n"
      "s_waitcnt vmcnt(0)\n"
      "ds_read_b64_tr_b16 v[208:209], v228 offset:0\n"
      "ds_read_b64_tr_b16 v[210:211], v228 offset:512\n"
      "s_waitcnt lgkmcnt(0)\n"
      "v_mfma_f32_32x32x16_bf16 v[128:143], v[172:175], v[208:211], v[128:143]\n"
      "s_mov_b32 m0, s82\n"
      "s_add_u32 s78, s72, 0xa400\n"
      "buffer_load_dwordx4 v1, s[20:23], s78 offen sc1 lds\n"
      "s_waitcnt vmcnt(0)\n"
      "ds_read_b64_tr_b16 v[208:209], v228 offset:0\n"
      "ds_read_b64_tr_b16 v[210:211], v228 offset:512\n"
      "s_waitcnt lgkmcnt(0)\n"
      "v_mfma_f32_32x32x16_bf16 v[144:159], v[172:175], v[208:211], v[144:159]\n"
      "ds_read_b128 v[240:243], v15 offset:128\n"
      "ds_read_b128 v[244:247], v15 offset:160\n"
      "ds_read_b128 v[248:251], v15 offset:192\n"
      "ds_read_b128 v[252:255], v15 offset:224\n"
      "s_waitcnt vmcnt(0) lgkmcnt(0)\n"
      "s_nop 15\n"
      "s_nop 7\n"
      "s_branch .La1_epi\n"
      ".Lkv_bad:\n"
      ".Lkv_retry:\n"
      "v_min_u32 v172, 15, v0\n"
      "v_mul_u32_u24 v172, 0x800, v172\n"
      ".Lcan_94:\n"
      "buffer_load_dword v173, v172, s[20:23], s65 offen sc1\n"
      "s_waitcnt vmcnt(0)\n"
      "v_cmp_eq_u32 vcc, -1, v173\n"
      "s_sub_u32 s79, s79, 1\n"
      "s_cmp_lt_i32 s79, 0\n"
      "s_cbranch_scc1 .Lcand_95\n"
      "s_cbranch_vccnz .Lcan_94\n"
      ".Lcand_95:\n"
      "s_mov_b32 s80, s65\n"
      "buffer_load_dwordx4 v[128:131], v170, s[20:23], s80 offen sc1\n"
      "s_add_u32 s80, s65, 0x1000\n"
      "buffer_load_dwordx4 v[132:135], v170, s[20:23], s80 offen sc1\n"
      "s_add_u32 s80, s65, 0x2000\n"
      "buffer_load_dwordx4 v[136:139], v170, s[20:23], s80 offen sc1\n"
      "s_add_u32 s80, s65, 0x3000\n"
      "buffer_load_dwordx4 v[140:143], v170, s[20:23], s80 offen sc1\n"
      "s_add_u32 s80, s65, 0x4000\n"
      "buffer_load_dwordx4 v[144:147], v170, s[20:23], s80 offen sc1\n"
      "s_add_u32 s80, s65, 0x5000\n"
      "buffer_load_dwordx4 v[148:151], v170, s[20:23], s80 offen sc1\n"
      "s_add_u32 s80, s65, 0x6000\n"
      "buffer_load_dwordx4 v[152:155], v170, s[20:23], s80 offen sc1\n"
      "s_add_u32 s80, s65, 0x7000\n"
      "buffer_load_dwordx4 v[156:159], v170, s[20:23], s80 offen sc1\n"
      "v_pk_mov_b32 v[160:161], 0, 0\n"
      "v_pk_mov_b32 v[162:163], 0, 0\n"
      "v_pk_mov_b32 v[164:165], 0, 0\n"
      "v_pk_mov_b32 v[166:167], 0, 0\n"
      "s_waitcnt vmcnt(7)\n"
      "v_dot2c_f32_bf16 v160, s82, v128\n"
      "v_dot2c_f32_bf16 v161, s83, v128\n"
      "v_dot2c_f32_bf16 v162, s82, v129\n"
      "v_dot2c_f32_bf16 v163, s83, v129\n"
      "v_dot2c_f32_bf16 v164, s82, v130\n"
      "v_dot2c_f32_bf16 v165, s83, v130\n"
      "v_dot2c_f32_bf16 v166, s82, v131\n"
      "v_dot2c_f32_bf16 v167, s83, v131\n"
      "s_waitcnt vmcnt(6)\n"
      "v_dot2c_f32_bf16 v160, s82, v132\n"
      "v_dot2c_f32_bf16 v161, s83, v132\n"
      "v_dot2c_f32_bf16 v162, s82, v133\n"
      "v_dot2c_f32_bf16 v163, s83, v133\n"
      "v_dot2c_f32_bf16 v164, s82, v134\n"
      "v_dot2c_f32_bf16 v165, s83, v134\n"
      "v_dot2c_f32_bf16 v166, s82, v135\n"
      "v_dot2c_f32_bf16 v167, s83, v135\n"
      "s_waitcnt vmcnt(5)\n"
      "v_dot2c_f32_bf16 v160, s82, v136\n"
      "v_dot2c_f32_bf16 v161, s83, v136\n"
      "v_dot2c_f32_bf16 v162, s82, v137\n"
      "v_dot2c_f32_bf16 v163, s83, v137\n"
      "v_dot2c_f32_bf16 v164, s82, v138\n"
      "v_dot2c_f32_bf16 v165, s83, v138\n"
      "v_dot2c_f32_bf16 v166, s82, v139\n"
      "v_dot2c_f32_bf16 v167, s83, v139\n"
      "s_waitcnt vmcnt(4)\n"
      "v_dot2c_f32_bf16 v160, s82, v140\n"
      "v_dot2c_f32_bf16 v161, s83, v140\n"
      "v_dot2c_f32_bf16 v162, s82, v141\n"
      "v_dot2c_f32_bf16 v163, s83, v141\n"
      "v_dot2c_f32_bf16 v164, s82, v142\n"
      "v_dot2c_f32_bf16 v165, s83, v142\n"
      "v_dot2c_f32_bf16 v166, s82, v143\n"
      "v_dot2c_f32_bf16 v167, s83, v143\n"
      "s_waitcnt vmcnt(3)\n"
      "v_dot2c_f32_bf16 v160, s82, v144\n"
      "v_dot2c_f32_bf16 v161, s83, v144\n"
      "v_dot2c_f32_bf16 v162, s82, v145\n"
      "v_dot2c_f32_bf16 v163, s83, v145\n"
      "v_dot2c_f32_bf16 v164, s82, v146\n"
      "v_dot2c_f32_bf16 v165, s83, v146\n"
      "v_dot2c_f32_bf16 v166, s82, v147\n"
      "v_dot2c_f32_bf16 v167, s83, v147\n"
      "s_waitcnt vmcnt(2)\n"
      "v_dot2c_f32_bf16 v160, s82, v148\n"
      "v_dot2c_f32_bf16 v161, s83, v148\n"
      "v_dot2c_f32_bf16 v162, s82, v149\n"
      "v_dot2c_f32_bf16 v163, s83, v149\n"
      "v_dot2c_f32_bf16 v164, s82, v150\n"
      "v_dot2c_f32_bf16 v165, s83, v150\n"
      "v_dot2c_f32_bf16 v166, s82, v151\n"
      "v_dot2c_f32_bf16 v167, s83, v151\n"
      "s_waitcnt vmcnt(1)\n"
      "v_dot2c_f32_bf16 v160, s82, v152\n"
      "v_dot2c_f32_bf16 v161, s83, v152\n"
      "v_dot2c_f32_bf16 v162, s82, v153\n"
      "v_dot2c_f32_bf16 v163, s83, v153\n"
      "v_dot2c_f32_bf16 v164, s82, v154\n"
      "v_dot2c_f32_bf16 v165, s83, v154\n"
      "v_dot2c_f32_bf16 v166, s82, v155\n"
      "v_dot2c_f32_bf16 v167, s83, v155\n"
      "s_waitcnt vmcnt(0)\n"
      "v_dot2c_f32_bf16 v160, s82, v156\n"
      "v_dot2c_f32_bf16 v161, s83, v156\n"
      "v_dot2c_f32_bf16 v162, s82, v157\n"
      "v_dot2c_f32_bf16 v163, s83, v157\n"
      "v_dot2c_f32_bf16 v164, s82, v158\n"
      "v_dot2c_f32_bf16 v165, s83, v158\n"
      "v_dot2c_f32_bf16 v166, s82, v159\n"
      "v_dot2c_f32_bf16 v167, s83, v159\n"
      "v_permlane32_swap_b32 v160, v164\n"
      "v_permlane32_swap_b32 v161, v165\n"
      "v_permlane32_swap_b32 v162, v166\n"
      "v_permlane32_swap_b32 v163, v167\n"
      "v_pk_add_f32 v[160:161], v[160:161], v[164:165]\n"
      "v_pk_add_f32 v[162:163], v[162:163], v[166:167]\n"
      "v_cmp_u_f32 vcc, v160, v162\n"
      "s_nop 0\n"
      "s_cbranch_vccz .Lkv_done\n"
      "v_max_u32 v239, v128, v129\n"
      "v_max3_u32 v239, v239, v130, v131\n"
      "v_max3_u32 v239, v239, v132, v133\n"
      "v_max3_u32 v239, v239, v134, v135\n"
      "v_max3_u32 v239, v239, v136, v137\n"
      "v_max3_u32 v239, v239, v138, v139\n"
      "v_max3_u32 v239, v239, v140, v141\n"
      "v_max3_u32 v239, v239, v142, v143\n"
      "v_max3_u32 v239, v239, v144, v145\n"
      "v_max3_u32 v239, v239, v146, v147\n"
      "v_max3_u32 v239, v239, v148, v149\n"
      "v_max3_u32 v239, v239, v150, v151\n"
      "v_max3_u32 v239, v239, v152, v153\n"
      "v_max3_u32 v239, v239, v154, v155\n"
      "v_max3_u32 v239, v239, v156, v157\n"
      "v_max3_u32 v239, v239, v158, v159\n"
      "v_cmp_eq_u32 vcc, -1, v239\n"
      "s_cmp_lt_i32 s79, 0\n"
      "s_cbranch_scc1 .Lkv_done\n"
      "s_cbranch_vccnz .Lkv_retry\n"
      ".Lkv_done:\n"
      "v_cvt_pk_bf16_f32 v164, v160, v161\n"
      "v_cvt_pk_bf16_f32 v165, v162, v163\n"
      "s_branch .Lkv_ok\n"
      ".Lfa_wait:\n"
      "buffer_load_dword v169, v168, s[20:23], s85 offen sc1\n"
      "s_waitcnt vmcnt(0)\n"
      "v_cmp_ne_u32 vcc, s56, v169\n"
      "s_sub_u32 s79, s79, 1\n"
      "s_cmp_lt_i32 s79, 0\n"
      "s_cbranch_scc1 .Lfa_ok\n"
      "s_cbranch_vccnz .Lfa_wait\n"
      "s_branch .Lfa_ok\n"
      ".Lpk_98:\n"
      "s_mov_b32 s98, s80\n"
      ".Lpk_98l:\n"
      "s_mov_b32 s80, s66\n"
      "buffer_load_dwordx4 v[128:131], v1, s[20:23], s80 offen sc1\n"
      "s_waitcnt vmcnt(5)\n"
      "buffer_load_dwordx4 v[132:135], v1, s[20:23], s80 offen offset:1024 sc1\n"
      "s_waitcnt vmcnt(0)\n"
      "v_cmp_eq_u32 vcc, -1, v128\n"
      "s_sub_u32 s79, s79, 1\n"
      "s_cmp_lt_i32 s79, 0\n"
      "s_cbranch_scc1 .Lpk_100\n"
      "s_cbranch_vccnz .Lpk_98l\n"
      ".Lpk_100:\n"
      "s_mov_b32 s80, s98\n"
      "s_branch .Lpr_99\n"
      ".Lpk_101:\n"
      "s_mov_b32 s98, s80\n"
      ".Lpk_101l:\n"
      "s_mov_b32 s80, s66\n"
      "buffer_load_dwordx4 v[132:135], v1, s[20:23], s80 offen offset:1024 sc1\n"
      "s_waitcnt vmcnt(0)\n"
      "v_cmp_eq_u32 vcc, -1, v132\n"
      "s_sub_u32 s79, s79, 1\n"
      "s_cmp_lt_i32 s79, 0\n"
      "s_cbranch_scc1 .Lpk_103\n"
      "s_cbranch_vccnz .Lpk_101l\n"
      ".Lpk_103:\n"
      "s_mov_b32 s80, s98\n"
      "s_branch .Lpr_102\n"
      ".Lpk_104:\n"
      "s_mov_b32 s98, s80\n"
      ".Lpk_104l:\n"
      "s_mov_b32 s80, s66\n"
      "buffer_load_dwordx4 v[136:139], v1, s[20:23], s80 offen offset:2048 sc1\n"
      "s_waitcnt vmcnt(6)\n"
      "buffer_load_dwordx4 v[140:143], v1, s[20:23], s80 offen offset:3072 sc1\n"
      "s_waitcnt vmcnt(0)\n"
      "v_cmp_eq_u32 vcc, -1, v136\n"
      "s_sub_u32 s79, s79, 1\n"
      "s_cmp_lt_i32 s79, 0\n"
      "s_cbranch_scc1 .Lpk_106\n"
      "s_cbranch_vccnz .Lpk_104l\n"
      ".Lpk_106:\n"
      "s_mov_b32 s80, s98\n"
      "s_branch .Lpr_105\n"
      ".Lpk_107:\n"
      "s_mov_b32 s98, s80\n"
      ".Lpk_107l:\n"
      "s_mov_b32 s80, s66\n"
      "buffer_load_dwordx4 v[140:143], v1, s[20:23], s80 offen offset:3072 sc1\n"
      "s_waitcnt vmcnt(0)\n"
      "v_cmp_eq_u32 vcc, -1, v140\n"
      "s_sub_u32 s79, s79, 1\n"
      "s_cmp_lt_i32 s79, 0\n"
      "s_cbranch_scc1 .Lpk_109\n"
      "s_cbranch_vccnz .Lpk_107l\n"
      ".Lpk_109:\n"
      "s_mov_b32 s80, s98\n"
      "s_branch .Lpr_108\n"
      ".Lpk_110:\n"
      "s_mov_b32 s98, s80\n"
      ".Lpk_110l:\n"
      "s_add_u32 s80, s66, 0x1000\n"
      "buffer_load_dwordx4 v[144:147], v1, s[20:23], s80 offen sc1\n"
      "s_waitcnt vmcnt(8)\n"
      "buffer_load_dwordx4 v[148:151], v1, s[20:23], s80 offen offset:1024 sc1\n"
      "s_waitcnt vmcnt(0)\n"
      "v_cmp_eq_u32 vcc, -1, v144\n"
      "s_sub_u32 s79, s79, 1\n"
      "s_cmp_lt_i32 s79, 0\n"
      "s_cbranch_scc1 .Lpk_112\n"
      "s_cbranch_vccnz .Lpk_110l\n"
      ".Lpk_112:\n"
      "s_mov_b32 s80, s98\n"
      "s_branch .Lpr_111\n"
      ".Lpk_113:\n"
      "s_mov_b32 s98, s80\n"
      ".Lpk_113l:\n"
      "s_add_u32 s80, s66, 0x1000\n"
      "buffer_load_dwordx4 v[148:151], v1, s[20:23], s80 offen offset:1024 sc1\n"
      "s_waitcnt vmcnt(0)\n"
      "v_cmp_eq_u32 vcc, -1, v148\n"
      "s_sub_u32 s79, s79, 1\n"
      "s_cmp_lt_i32 s79, 0\n"
      "s_cbranch_scc1 .Lpk_115\n"
      "s_cbranch_vccnz .Lpk_113l\n"
      ".Lpk_115:\n"
      "s_mov_b32 s80, s98\n"
      "s_branch .Lpr_114\n"
      ".Lpk_116:\n"
      "s_mov_b32 s98, s80\n"
      ".Lpk_116l:\n"
      "s_add_u32 s80, s66, 0x1000\n"
      "buffer_load_dwordx4 v[152:155], v1, s[20:23], s80 offen offset:2048 sc1\n"
      "s_waitcnt vmcnt(9)\n"
      "buffer_load_dwordx4 v[156:159], v1, s[20:23], s80 offen offset:3072 sc1\n"
      "s_waitcnt vmcnt(0)\n"
      "v_cmp_eq_u32 vcc, -1, v152\n"
      "s_sub_u32 s79, s79, 1\n"
      "s_cmp_lt_i32 s79, 0\n"
      "s_cbranch_scc1 .Lpk_118\n"
      "s_cbranch_vccnz .Lpk_116l\n"
      ".Lpk_118:\n"
      "s_mov_b32 s80, s98\n"
      "s_branch .Lpr_117\n"
      ".Lpk_119:\n"
      "s_mov_b32 s98, s80\n"
      ".Lpk_119l:\n"
      "s_add_u32 s80, s66, 0x1000\n"
      "buffer_load_dwordx4 v[156:159], v1, s[20:23], s80 offen offset:3072 sc1\n"
      "s_waitcnt vmcnt(0)\n"
      "v_cmp_eq_u32 vcc, -1, v156\n"
      "s_sub_u32 s79, s79, 1\n"
      "s_cmp_lt_i32 s79, 0\n"
      "s_cbranch_scc1 .Lpk_121\n"
      "s_cbranch_vccnz .Lpk_119l\n"
      ".Lpk_121:\n"
      "s_mov_b32 s80, s98\n"
      "s_branch .Lpr_120\n"
      ".Lpk_122:\n"
      "s_mov_b32 s98, s80\n"
      ".Lpk_122l:\n"
      "s_mov_b32 s80, s17\n"
      "buffer_load_dwordx4 v[160:163], v1, s[20:23], s80 offen sc1\n"
      "buffer_load_dwordx4 v[164:167], v1, s[20:23], s80 offen offset:1024 sc1\n"
      "s_waitcnt vmcnt(0)\n"
      "v_cmp_eq_u32 vcc, -1, v160\n"
      "s_sub_u32 s79, s79, 1\n"
      "s_cmp_lt_i32 s79, 0\n"
      "s_cbranch_scc1 .Lpk_124\n"
      "s_cbranch_vccnz .Lpk_122l\n"
      ".Lpk_124:\n"
      "s_mov_b32 s80, s98\n"
      "s_branch .Lpr_123\n"
      ".Lpk_125:\n"
      "s_mov_b32 s98, s80\n"
      ".Lpk_125l:\n"
      "s_mov_b32 s80, s17\n"
      "buffer_load_dwordx4 v[164:167], v1, s[20:23], s80 offen offset:1024 sc1\n"
      "s_waitcnt vmcnt(0)\n"
      "v_cmp_eq_u32 vcc, -1, v164\n"
      "s_sub_u32 s79, s79, 1\n"
      "s_cmp_lt_i32 s79, 0\n"
      "s_cbranch_scc1 .Lpk_127\n"
      "s_cbranch_vccnz .Lpk_125l\n"
      ".Lpk_127:\n"
      "s_mov_b32 s80, s98\n"
      "s_branch .Lpr_126\n"
      ".Lpk_128:\n"
      "s_mov_b32 s98, s80\n"
      ".Lpk_128l:\n"
      "s_mov_b32 s80, s17\n"
      "buffer_load_dwordx4 v[168:171], v1, s[20:23], s80 offen offset:2048 sc1\n"
      "buffer_load_dwordx4 v[172:175], v1, s[20:23], s80 offen offset:3072 sc1\n"
      "s_waitcnt vmcnt(0)\n"
      "v_cmp_eq_u32 vcc, -1, v168\n"
      "s_sub_u32 s79, s79, 1\n"
      "s_cmp_lt_i32 s79, 0\n"
      "s_cbranch_scc1 .Lpk_130\n"
      "s_cbranch_vccnz .Lpk_128l\n"
      ".Lpk_130:\n"
      "s_mov_b32 s80, s98\n"
      "s_branch .Lpr_129\n"
      ".Lpk_131:\n"
      "s_mov_b32 s98, s80\n"
      ".Lpk_131l:\n"
      "s_mov_b32 s80, s17\n"
      "buffer_load_dwordx4 v[172:175], v1, s[20:23], s80 offen offset:3072 sc1\n"
      "s_waitcnt vmcnt(0)\n"
      "v_cmp_eq_u32 vcc, -1, v172\n"
      "s_sub_u32 s79, s79, 1\n"
      "s_cmp_lt_i32 s79, 0\n"
      "s_cbranch_scc1 .Lpk_133\n"
      "s_cbranch_vccnz .Lpk_131l\n"
      ".Lpk_133:\n"
      "s_mov_b32 s80, s98\n"
      "s_branch .Lpr_132\n"
      ".Lpk_134:\n"
      "s_mov_b32 s98, s80\n"
      ".Lpk_134l:\n"
      "s_add_u32 s80, s17, 0x1000\n"
      "buffer_load_dwordx4 v[176:179], v1, s[20:23], s80 offen sc1\n"
      "buffer_load_dwordx4 v[180:183], v1, s[20:23], s80 offen offset:1024 sc1\n"
      "s_waitcnt vmcnt(0)\n"
      "v_cmp_eq_u32 vcc, -1, v176\n"
      "s_sub_u32 s79, s79, 1\n"
      "s_cmp_lt_i32 s79, 0\n"
      "s_cbranch_scc1 .Lpk_136\n"
      "s_cbranch_vccnz .Lpk_134l\n"
      ".Lpk_136:\n"
      "s_mov_b32 s80, s98\n"
      "s_branch .Lpr_135\n"
      ".Lpk_137:\n"
      "s_mov_b32 s98, s80\n"
      ".Lpk_137l:\n"
      "s_add_u32 s80, s17, 0x1000\n"
      "buffer_load_dwordx4 v[180:183], v1, s[20:23], s80 offen offset:1024 sc1\n"
      "s_waitcnt vmcnt(0)\n"
      "v_cmp_eq_u32 vcc, -1, v180\n"
      "s_sub_u32 s79, s79, 1\n"
      "s_cmp_lt_i32 s79, 0\n"
      "s_cbranch_scc1 .Lpk_139\n"
      "s_cbranch_vccnz .Lpk_137l\n"
      ".Lpk_139:\n"
      "s_mov_b32 s80, s98\n"
      "s_branch .Lpr_138\n"
      ".Lpk_140:\n"
      "s_mov_b32 s98, s80\n"
      ".Lpk_140l:\n"
      "s_add_u32 s80, s17, 0x1000\n"
      "buffer_load_dwordx4 v[184:187], v1, s[20:23], s80 offen offset:2048 sc1\n"
      "buffer_load_dwordx4 v[188:191], v1, s[20:23], s80 offen offset:3072 sc1\n"
      "s_waitcnt vmcnt(0)\n"
      "v_cmp_eq_u32 vcc, -1, v184\n"
      "s_sub_u32 s79, s79, 1\n"
      "s_cmp_lt_i32 s79, 0\n"
      "s_cbranch_scc1 .Lpk_142\n"
      "s_cbranch_vccnz .Lpk_140l\n"
      ".Lpk_142:\n"
      "s_mov_b32 s80, s98\n"
      "s_branch .Lpr_141\n"
      ".Lpk_143:\n"
      "s_mov_b32 s98, s80\n"
      ".Lpk_143l:\n"
      "s_add_u32 s80, s17, 0x1000\n"
      "buffer_load_dwordx4 v[188:191], v1, s[20:23], s80 offen offset:3072 sc1\n"
      "s_waitcnt vmcnt(0)\n"
      "v_cmp_eq_u32 vcc, -1, v188\n"
      "s_sub_u32 s79, s79, 1\n"
      "s_cmp_lt_i32 s79, 0\n"
      "s_cbranch_scc1 .Lpk_145\n"
      "s_cbranch_vccnz .Lpk_143l\n"
      ".Lpk_145:\n"
      "s_mov_b32 s80, s98\n"
      "s_branch .Lpr_144\n"
      ".La2_nan:\n"
      "s_cmp_lg_u32 s64, 0\n"
      "s_cbranch_scc1 .Lgu_146\n"
      "s_mov_b32 s64, 1\n"
      "s_mov_b32 s81, s66\n"
      "s_add_u32 s82, s81, 0x2000\n"
      ".Lvf_147:\n"
      "buffer_load_dwordx4 v[128:131], v1, s[20:23], s81 offen sc1\n"
      "buffer_load_dwordx4 v[132:135], v1, s[20:23], s81 offen offset:1024 sc1\n"
      "s_sub_u32 s79, s79, 1\n"
      "s_cmp_lt_i32 s79, 0\n"
      "s_cbranch_scc1 .Lvf_148\n"
      "s_waitcnt vmcnt(1)\n"
      "v_max_u32 v254, v128, v129\n"
      "v_max3_u32 v254, v254, v130, v131\n"
      "s_waitcnt vmcnt(0)\n"
      "v_max3_u32 v254, v254, v132, v133\n"
      "v_max3_u32 v254, v254, v134, v135\n"
      "v_cmp_eq_u32 vcc, -1, v254\n"
      "s_nop 0\n"
      "s_cbranch_vccnz .Lvf_147\n"
      "s_add_u32 s81, s81, 0x800\n"
      "s_cmp_lt_u32 s81, s82\n"
      "s_cbranch_scc1 .Lvf_147\n"
      ".Lvf_148:\n"
      "s_mov_b32 s81, s17\n"
      "s_add_u32 s82, s81, 0x2000\n"
      ".Lvf_149:\n"
      "s_waitcnt vmcnt(1)\n"
      "buffer_load_dwordx4 v[128:131], v1, s[20:23], s81 offen sc1\n"
      "s_waitcnt vmcnt(1)\n"
      "buffer_load_dwordx4 v[132:135], v1, s[20:23], s81 offen offset:1024 sc1\n"
      "s_sub_u32 s79, s79, 1\n"
      "s_cmp_lt_i32 s79, 0\n"
      "s_cbranch_scc1 .Lvf_150\n"
      "s_waitcnt vmcnt(1)\n"
      "v_max_u32 v254, v128, v129\n"
      "v_max3_u32 v254, v254, v130, v131\n"
      "s_waitcnt vmcnt(0)\n"
      "v_max3_u32 v254, v254, v132, v133\n"
      "v_max3_u32 v254, v254, v134, v135\n"
      "v_cmp_eq_u32 vcc, -1, v254\n"
      "s_nop 0\n"
      "s_cbranch_vccnz .Lvf_149\n"
      "s_add_u32 s81, s81, 0x800\n"
      "s_cmp_lt_u32 s81, s82\n"
      "s_cbranch_scc1 .Lvf_149\n"
      ".Lvf_150:\n"
      "s_mov_b32 s80, s66\n"
      "s_waitcnt vmcnt(1)\n"
      "buffer_load_dwordx4 v[128:131], v1, s[20:23], s80 offen sc1\n"
      "s_waitcnt vmcnt(1)\n"
      "buffer_load_dwordx4 v[132:135], v1, s[20:23], s80 offen offset:1024 sc1\n"
      "buffer_load_dwordx4 v[136:139], v1, s[20:23], s80 offen offset:2048 sc1\n"
      "buffer_load_dwordx4 v[140:143], v1, s[20:23], s80 offen offset:3072 sc1\n"
      "s_add_u32 s80, s66, 0x1000\n"
      "buffer_load_dwordx4 v[144:147], v1, s[20:23], s80 offen sc1\n"
      "buffer_load_dwordx4 v[148:151], v1, s[20:23], s80 offen offset:1024 sc1\n"
      "s_branch .La2_go\n"
      ".Lst_bad:\n"
      "buffer_load_dwordx4 v[160:163], v1, s[20:23], s67 offen sc1\n"
      "buffer_load_dwordx4 v[164:167], v1, s[20:23], s67 offen offset:1024 sc1\n"
      "buffer_load_dwordx4 v[168:171], v1, s[20:23], s67 offen offset:2048 sc1\n"
      "s_mov_b32 exec_hi, 0\n"
      "buffer_load_dwordx4 v[172:175], v1, s[20:23], s67 offen offset:3072 sc1\n"
      "s_mov_b32 exec_hi, -1\n"
      "s_waitcnt vmcnt(3)\n"
      "v_max_u32 v152, v160, v161\n"
      "v_max3_u32 v152, v152, v162, v163\n"
      "s_waitcnt vmcnt(2)\n"
      "v_max3_u32 v152, v152, v164, v165\n"
      "v_max3_u32 v152, v152, v166, v167\n"
      "s_waitcnt vmcnt(1)\n"
      "v_max3_u32 v152, v152, v168, v169\n"
      "v_max3_u32 v152, v152, v170, v171\n"
      "s_waitcnt vmcnt(0)\n"
      "v_max3_u32 v152, v152, v172, v173\n"
      "v_max3_u32 v152, v152, v174, v175\n"
      "v_cmp_eq_u32 vcc, -1, v152\n"
      "s_sub_u32 s79, s79, 1\n"
      "s_cmp_lt_i32 s79, 0\n"
      "s_cbranch_scc1 .Lst_done\n"
      "s_cbranch_vccnz .Lst_bad\n"
      ".Lst_done:\n"
      "v_pk_add_f32 v[160:161], v[160:161], v[164:165]\n"
      "v_pk_add_f32 v[162:163], v[162:163], v[166:167]\n"
      "v_pk_add_f32 v[168:169], v[168:169], v[172:173]\n"
      "v_pk_add_f32 v[170:171], v[170:171], v[174:175]\n"
      "v_pk_add_f32 v[160:161], v[160:161], v[168:169]\n"
      "v_pk_add_f32 v[162:163], v[162:163], v[170:171]\n"
      "s_nop 1\n"
      "v_permlane16_swap_b32 v160, v162\n"
      "v_permlane16_swap_b32 v161, v163\n"
      "v_pk_add_f32 v[144:145], v[160:161], v[162:163]\n"
      "v_mov_b32 v146, v144\n"
      "v_mov_b32 v147, v145\n"
      "s_nop 0\n"
      "v_permlane32_swap_b32 v144, v146\n"
      "v_permlane32_swap_b32 v145, v147\n"
      "v_pk_add_f32 v[144:145], v[144:145], v[146:147]\n"
      "v_mul_f32 v144, 0xbb124925, v144\n"
      "v_mul_f32 v145, 0x3b124925, v145\n"
      "s_branch .Lst_ok\n"
      ".Lpk_155:\n"
      "s_mov_b32 s98, s80\n"
      ".Lpk_155l:\n"
      "s_add_u32 s80, s68, s2\n"
      "buffer_load_dwordx4 v[144:147], v1, s[20:23], s80 offen sc1\n"
      "s_waitcnt vmcnt(3)\n"
      "buffer_load_dwordx4 v[148:151], v1, s[20:23], s80 offen offset:1024 sc1\n"
      "s_waitcnt vmcnt(3)\n"
      "buffer_load_dwordx4 v[152:155], v1, s[20:23], s80 offen offset:2048 sc1\n"
      "s_waitcnt vmcnt(3)\n"
      "buffer_load_dwordx4 v[156:159], v1, s[20:23], s80 offen offset:3072 sc1\n"
      "s_waitcnt vmcnt(0)\n"
      "v_cmp_eq_u32 vcc, -1, v144\n"
      "s_sub_u32 s79, s79, 1\n"
      "s_cmp_lt_i32 s79, 0\n"
      "s_cbranch_scc1 .Lpk_157\n"
      "s_cbranch_vccnz .Lpk_155l\n"
      ".Lpk_157:\n"
      "s_mov_b32 s80, s98\n"
      "s_branch .Lpr_156\n"
      ".Lpk_158:\n"
      "s_mov_b32 s98, s80\n"
      ".Lpk_158l:\n"
      "s_add_u32 s80, s68, s2\n"
      "buffer_load_dwordx4 v[148:151], v1, s[20:23], s80 offen offset:1024 sc1\n"
      "s_waitcnt vmcnt(3)\n"
      "buffer_load_dwordx4 v[152:155], v1, s[20:23], s80 offen offset:2048 sc1\n"
      "s_waitcnt vmcnt(3)\n"
      "buffer_load_dwordx4 v[156:159], v1, s[20:23], s80 offen offset:3072 sc1\n"
      "s_waitcnt vmcnt(0)\n"
      "v_cmp_eq_u32 vcc, -1, v148\n"
      "s_sub_u32 s79, s79, 1\n"
      "s_cmp_lt_i32 s79, 0\n"
      "s_cbranch_scc1 .Lpk_160\n"
      "s_cbranch_vccnz .Lpk_158l\n"
      ".Lpk_160:\n"
      "s_mov_b32 s80, s98\n"
      "s_branch .Lpr_159\n"
      ".Lpk_161:\n"
      "s_mov_b32 s98, s80\n"
      ".Lpk_161l:\n"
      "s_add_u32 s80, s68, s2\n"
      "buffer_load_dwordx4 v[152:155], v1, s[20:23], s80 offen offset:2048 sc1\n"
      "s_waitcnt vmcnt(4)\n"
      "buffer_load_dwordx4 v[156:159], v1, s[20:23], s80 offen offset:3072 sc1\n"
      "s_waitcnt vmcnt(0)\n"
      "v_cmp_eq_u32 vcc, -1, v152\n"
      "s_sub_u32 s79, s79, 1\n"
      "s_cmp_lt_i32 s79, 0\n"
      "s_cbranch_scc1 .Lpk_163\n"
      "s_cbranch_vccnz .Lpk_161l\n"
      ".Lpk_163:\n"
      "s_mov_b32 s80, s98\n"
      "s_branch .Lpr_162\n"
      ".Lpk_164:\n"
      "s_mov_b32 s98, s80\n"
      ".Lpk_164l:\n"
      "s_add_u32 s80, s68, s2\n"
      "buffer_load_dwordx4 v[156:159], v1, s[20:23], s80 offen offset:3072 sc1\n"
      "s_waitcnt vmcnt(0)\n"
      "v_cmp_eq_u32 vcc, -1, v156\n"
      "s_sub_u32 s79, s79, 1\n"
      "s_cmp_lt_i32 s79, 0\n"
      "s_cbranch_scc1 .Lpk_166\n"
      "s_cbranch_vccnz .Lpk_164l\n"
      ".Lpk_166:\n"
      "s_mov_b32 s80, s98\n"
      "s_branch .Lpr_165\n"
      ".Lpk_167:\n"
      "s_mov_b32 s98, s80\n"
      ".Lpk_167l:\n"
      "s_add_u32 s80, s68, s3\n"
      "buffer_load_dwordx4 v[12:15], v1, s[20:23], s80 offen sc1\n"
      "s_waitcnt vmcnt(6)\n"
      "buffer_load_dwordx4 v[244:247], v1, s[20:23], s80 offen offset:1024 sc1\n"
      "s_waitcnt vmcnt(6)\n"
      "buffer_load_dwordx4 v[248:251], v1, s[20:23], s80 offen offset:2048 sc1\n"
      "s_waitcnt vmcnt(6)\n"
      "buffer_load_dwordx4 v[252:255], v1, s[20:23], s80 offen offset:3072 sc1\n"
      "s_waitcnt vmcnt(0)\n"
      "v_cmp_eq_u32 vcc, -1, v12\n"
      "s_sub_u32 s79, s79, 1\n"
      "s_cmp_lt_i32 s79, 0\n"
      "s_cbranch_scc1 .Lpk_169\n"
      "s_cbranch_vccnz .Lpk_167l\n"
      ".Lpk_169:\n"
      "s_mov_b32 s80, s98\n"
      "s_branch .Lpr_168\n"
      ".Lpk_170:\n"
      "s_mov_b32 s98, s80\n"
      ".Lpk_170l:\n"
      "s_add_u32 s80, s68, s3\n"
      "buffer_load_dwordx4 v[244:247], v1, s[20:23], s80 offen offset:1024 sc1\n"
      "s_waitcnt vmcnt(7)\n"
      "buffer_load_dwordx4 v[248:251], v1, s[20:23], s80 offen offset:2048 sc1\n"
      "s_waitcnt vmcnt(7)\n"
      "buffer_load_dwordx4 v[252:255], v1, s[20:23], s80 offen offset:3072 sc1\n"
      "s_waitcnt vmcnt(0)\n"
      "v_cmp_eq_u32 vcc, -1, v244\n"
      "s_sub_u32 s79, s79, 1\n"
      "s_cmp_lt_i32 s79, 0\n"
      "s_cbranch_scc1 .Lpk_172\n"
      "s_cbranch_vccnz .Lpk_170l\n"
      ".Lpk_172:\n"
      "s_mov_b32 s80, s98\n"
      "s_branch .Lpr_171\n"
      ".Lpk_173:\n"
      "s_mov_b32 s98, s80\n"
      ".Lpk_173l:\n"
      "s_add_u32 s80, s68, s3\n"
      "buffer_load_dwordx4 v[248:251], v1, s[20:23], s80 offen offset:2048 sc1\n"
      "s_waitcnt vmcnt(8)\n"
      "buffer_load_dwordx4 v[252:255], v1, s[20:23], s80 offen offset:3072 sc1\n"
      "s_waitcnt vmcnt(0)\n"
      "v_cmp_eq_u32 vcc, -1, v248\n"
      "s_sub_u32 s79, s79, 1\n"
      "s_cmp_lt_i32 s79, 0\n"
      "s_cbranch_scc1 .Lpk_175\n"
      "s_cbranch_vccnz .Lpk_173l\n"
      ".Lpk_175:\n"
      "s_mov_b32 s80, s98\n"
      "s_branch .Lpr_174\n"
      ".Lpk_176:\n"
      "s_mov_b32 s98, s80\n"
      ".Lpk_176l:\n"
      "s_add_u32 s80, s68, s3\n"
      "buffer_load_dwordx4 v[252:255], v1, s[20:23], s80 offen offset:3072 sc1\n"
      "s_waitcnt vmcnt(0)\n"
      "v_cmp_eq_u32 vcc, -1, v252\n"
      "s_sub_u32 s79, s79, 1\n"
      "s_cmp_lt_i32 s79, 0\n"
      "s_cbranch_scc1 .Lpk_178\n"
      "s_cbranch_vccnz .Lpk_176l\n"
      ".Lpk_178:\n"
      "s_mov_b32 s80, s98\n"
      "s_branch .Lpr_177\n"
      ".Lpk_179:\n"
      "s_mov_b32 s98, s80\n"
      ".Lpk_179l:\n"
      "s_add_u32 s80, s68, s4\n"
      "buffer_load_dwordx4 v[224:227], v1, s[20:23], s80 offen sc1\n"
      "s_waitcnt vmcnt(10)\n"
      "buffer_load_dwordx4 v[228:231], v1, s[20:23], s80 offen offset:1024 sc1\n"
      "s_waitcnt vmcnt(10)\n"
      "buffer_load_dwordx4 v[232:235], v1, s[20:23], s80 offen offset:2048 sc1\n"
      "s_waitcnt vmcnt(10)\n"
      "buffer_load_dwordx4 v[236:239], v1, s[20:23], s80 offen offset:3072 sc1\n"
      "s_waitcnt vmcnt(0)\n"
      "v_cmp_eq_u32 vcc, -1, v224\n"
      "s_sub_u32 s79, s79, 1\n"
      "s_cmp_lt_i32 s79, 0\n"
      "s_cbranch_scc1 .Lpk_181\n"
      "s_cbranch_vccnz .Lpk_179l\n"
      ".Lpk_181:\n"
      "s_mov_b32 s80, s98\n"
      "s_branch .Lpr_180\n"
      ".Lpk_182:\n"
      "s_mov_b32 s98, s80\n"
      ".Lpk_182l:\n"
      "s_add_u32 s80, s68, s4\n"
      "buffer_load_dwordx4 v[228:231], v1, s[20:23], s80 offen offset:1024 sc1\n"
      "s_waitcnt vmcnt(11)\n"
      "buffer_load_dwordx4 v[232:235], v1, s[20:23], s80 offen offset:2048 sc1\n"
      "s_waitcnt vmcnt(11)\n"
      "buffer_load_dwordx4 v[236:239], v1, s[20:23], s80 offen offset:3072 sc1\n"
      "s_waitcnt vmcnt(0)\n"
      "v_cmp_eq_u32 vcc, -1, v228\n"
      "s_sub_u32 s79, s79, 1\n"
      "s_cmp_lt_i32 s79, 0\n"
      "s_cbranch_scc1 .Lpk_184\n"
      "s_cbranch_vccnz .Lpk_182l\n"
      ".Lpk_184:\n"
      "s_mov_b32 s80, s98\n"
      "s_branch .Lpr_183\n"
      ".Lpk_185:\n"
      "s_mov_b32 s98, s80\n"
      ".Lpk_185l:\n"
      "s_add_u32 s80, s68, s4\n"
      "buffer_load_dwordx4 v[232:235], v1, s[20:23], s80 offen offset:2048 sc1\n"
      "s_waitcnt vmcnt(12)\n"
      "buffer_load_dwordx4 v[236:239], v1, s[20:23], s80 offen offset:3072 sc1\n"
      "s_waitcnt vmcnt(0)\n"
      "v_cmp_eq_u32 vcc, -1, v232\n"
      "s_sub_u32 s79, s79, 1\n"
      "s_cmp_lt_i32 s79, 0\n"
      "s_cbranch_scc1 .Lpk_187\n"
      "s_cbranch_vccnz .Lpk_185l\n"
      ".Lpk_187:\n"
      "s_mov_b32 s80, s98\n"
      "s_branch .Lpr_186\n"
      ".Lpk_188:\n"
      "s_mov_b32 s98, s80\n"
      ".Lpk_188l:\n"
      "s_add_u32 s80, s68, s4\n"
      "buffer_load_dwordx4 v[236:239], v1, s[20:23], s80 offen offset:3072 sc1\n"
      "s_waitcnt vmcnt(0)\n"
      "v_cmp_eq_u32 vcc, -1, v236\n"
      "s_sub_u32 s79, s79, 1\n"
      "s_cmp_lt_i32 s79, 0\n"
      "s_cbranch_scc1 .Lpk_190\n"
      "s_cbranch_vccnz .Lpk_188l\n"
      ".Lpk_190:\n"
      "s_mov_b32 s80, s98\n"
      "s_branch .Lpr_189\n"
      ".Lpk_191:\n"
      "s_mov_b32 s98, s80\n"
      ".Lpk_191l:\n"
      "s_add_u32 s80, s68, s5\n"
      "buffer_load_dwordx4 v[240:243], v1, s[20:23], s80 offen sc1\n"
      "s_waitcnt vmcnt(12)\n"
      "buffer_load_dwordx4 v[128:131], v1, s[20:23], s80 offen offset:1024 sc1\n"
      "s_waitcnt vmcnt(12)\n"
      "buffer_load_dwordx4 v[132:135], v1, s[20:23], s80 offen offset:2048 sc1\n"
      "s_waitcnt vmcnt(12)\n"
      "buffer_load_dwordx4 v[136:139], v1, s[20:23], s80 offen offset:3072 sc1\n"
      "s_waitcnt vmcnt(0)\n"
      "v_cmp_eq_u32 vcc, -1, v240\n"
      "s_sub_u32 s79, s79, 1\n"
      "s_cmp_lt_i32 s79, 0\n"
      "s_cbranch_scc1 .Lpk_193\n"
      "s_cbranch_vccnz .Lpk_191l\n"
      ".Lpk_193:\n"
      "s_mov_b32 s80, s98\n"
      "s_branch .Lpr_192\n"
      ".Lpk_194:\n"
      "s_mov_b32 s98, s80\n"
      ".Lpk_194l:\n"
      "s_add_u32 s80, s68, s5\n"
      "buffer_load_dwordx4 v[128:131], v1, s[20:23], s80 offen offset:1024 sc1\n"
      "s_waitcnt vmcnt(12)\n"
      "buffer_load_dwordx4 v[132:135], v1, s[20:23], s80 offen offset:2048 sc1\n"
      "s_waitcnt vmcnt(12)\n"
      "buffer_load_dwordx4 v[136:139], v1, s[20:23], s80 offen offset:3072 sc1\n"
      "s_waitcnt vmcnt(0)\n"
      "v_cmp_eq_u32 vcc, -1, v128\n"
      "s_sub_u32 s79, s79, 1\n"
      "s_cmp_lt_i32 s79, 0\n"
      "s_cbranch_scc1 .Lpk_196\n"
      "s_cbranch_vccnz .Lpk_194l\n"
      ".Lpk_196:\n"
      "s_mov_b32 s80, s98\n"
      "s_branch .Lpr_195\n"
      ".Lpk_197:\n"
      "s_mov_b32 s98, s80\n"
      ".Lpk_197l:\n"
      "s_add_u32 s80, s68, s5\n"
      "buffer_load_dwordx4 v[132:135], v1, s[20:23], s80 offen offset:2048 sc1\n"
      "s_waitcnt vmcnt(12)\n"
      "buffer_load_dwordx4 v[136:139], v1, s[20:23], s80 offen offset:3072 sc1\n"
      "s_waitcnt vmcnt(0)\n"
      "v_cmp_eq_u32 vcc, -1, v132\n"
      "s_sub_u32 s79, s79, 1\n"
      "s_cmp_lt_i32 s79, 0\n"
      "s_cbranch_scc1 .Lpk_199\n"
      "s_cbranch_vccnz .Lpk_197l\n"
      ".Lpk_199:\n"
      "s_mov_b32 s80, s98\n"
      "s_branch .Lpr_198\n"
      ".Lpk_200:\n"
      "s_mov_b32 s98, s80\n"
      ".Lpk_200l:\n"
      "s_add_u32 s80, s68, s5\n"
      "buffer_load_dwordx4 v[136:139], v1, s[20:23], s80 offen offset:3072 sc1\n"
      "s_waitcnt vmcnt(0)\n"
      "v_cmp_eq_u32 vcc, -1, v136\n"
      "s_sub_u32 s79, s79, 1\n"
      "s_cmp_lt_i32 s79, 0\n"
      "s_cbranch_scc1 .Lpk_202\n"
      "s_cbranch_vccnz .Lpk_200l\n"
      ".Lpk_202:\n"
      "s_mov_b32 s80, s98\n"
      "s_branch .Lpr_201\n"
      ".Lpk_203:\n"
      "s_mov_b32 s98, s80\n"
      ".Lpk_203l:\n"
      "s_add_u32 s80, s68, s6\n"
      "buffer_load_dwordx4 v[140:143], v1, s[20:23], s80 offen sc1\n"
      "s_waitcnt vmcnt(12)\n"
      "buffer_load_dwordx4 v[144:147], v1, s[20:23], s80 offen offset:1024 sc1\n"
      "s_waitcnt vmcnt(12)\n"
      "buffer_load_dwordx4 v[148:151], v1, s[20:23], s80 offen offset:2048 sc1\n"
      "s_waitcnt vmcnt(12)\n"
      "buffer_load_dwordx4 v[152:155], v1, s[20:23], s80 offen offset:3072 sc1\n"
      "s_waitcnt vmcnt(0)\n"
      "v_cmp_eq_u32 vcc, -1, v140\n"
      "s_sub_u32 s79, s79, 1\n"
      "s_cmp_lt_i32 s79, 0\n"
      "s_cbranch_scc1 .Lpk_205\n"
      "s_cbranch_vccnz .Lpk_203l\n"
      ".Lpk_205:\n"
      "s_mov_b32 s80, s98\n"
      "s_branch .Lpr_204\n"
      ".Lpk_206:\n"
      "s_mov_b32 s98, s80\n"
      ".Lpk_206l:\n"
      "s_add_u32 s80, s68, s6\n"
      "buffer_load_dwordx4 v[144:147], v1, s[20:23], s80 offen offset:1024 sc1\n"
      "s_waitcnt vmcnt(12)\n"
      "buffer_load_dwordx4 v[148:151], v1, s[20:23], s80 offen offset:2048 sc1\n"
      "s_waitcnt vmcnt(12)\n"
      "buffer_load_dwordx4 v[152:155], v1, s[20:23], s80 offen offset:3072 sc1\n"
      "s_waitcnt vmcnt(0)\n"
      "v_cmp_eq_u32 vcc, -1, v144\n"
      "s_sub_u32 s79, s79, 1\n"
      "s_cmp_lt_i32 s79, 0\n"
      "s_cbranch_scc1 .Lpk_208\n"
      "s_cbranch_vccnz .Lpk_206l\n"
      ".Lpk_208:\n"
      "s_mov_b32 s80, s98\n"
      "s_branch .Lpr_207\n"
      ".Lpk_209:\n"
      "s_mov_b32 s98, s80\n"
      ".Lpk_209l:\n"
      "s_add_u32 s80, s68, s6\n"
      "buffer_load_dwordx4 v[148:151], v1, s[20:23], s80 offen offset:2048 sc1\n"
      "s_waitcnt vmcnt(12)\n"
      "buffer_load_dwordx4 v[152:155], v1, s[20:23], s80 offen offset:3072 sc1\n"
      "s_waitcnt vmcnt(0)\n"
      "v_cmp_eq_u32 vcc, -1, v148\n"
      "s_sub_u32 s79, s79, 1\n"
      "s_cmp_lt_i32 s79, 0\n"
      "s_cbranch_scc1 .Lpk_211\n"
      "s_cbranch_vccnz .Lpk_209l\n"
      ".Lpk_211:\n"
      "s_mov_b32 s80, s98\n"
      "s_branch .Lpr_210\n"
      ".Lpk_212:\n"
      "s_mov_b32 s98, s80\n"
      ".Lpk_212l:\n"
      "s_add_u32 s80, s68, s6\n"
      "buffer_load_dwordx4 v[152:155], v1, s[20:23], s80 offen offset:3072 sc1\n"
      "s_waitcnt vmcnt(0)\n"
      "v_cmp_eq_u32 vcc, -1, v152\n"
      "s_sub_u32 s79, s79, 1\n"
      "s_cmp_lt_i32 s79, 0\n"
      "s_cbranch_scc1 .Lpk_214\n"
      "s_cbranch_vccnz .Lpk_212l\n"
      ".Lpk_214:\n"
      "s_mov_b32 s80, s98\n"
      "s_branch .Lpr_213\n"
      ".Lpk_215:\n"
      "s_mov_b32 s98, s80\n"
      ".Lpk_215l:\n"
      "s_add_u32 s80, s68, s7\n"
      "buffer_load_dwordx4 v[156:159], v1, s[20:23], s80 offen sc1\n"
      "s_waitcnt vmcnt(12)\n"
      "buffer_load_dwordx4 v[12:15], v1, s[20:23], s80 offen offset:1024 sc1\n"
      "s_waitcnt vmcnt(12)\n"
      "buffer_load_dwordx4 v[244:247], v1, s[20:23], s80 offen offset:2048 sc1\n"
      "s_waitcnt vmcnt(12)\n"
      "buffer_load_dwordx4 v[248:251], v1, s[20:23], s80 offen offset:3072 sc1\n"
      "s_waitcnt vmcnt(0)\n"
      "v_cmp_eq_u32 vcc, -1, v156\n"
      "s_sub_u32 s79, s79, 1\n"
      "s_cmp_lt_i32 s79, 0\n"
      "s_cbranch_scc1 .Lpk_217\n"
      "s_cbranch_vccnz .Lpk_215l\n"
      ".Lpk_217:\n"
      "s_mov_b32 s80, s98\n"
      "s_branch .Lpr_216\n"
      ".Lpk_218:\n"
      "s_mov_b32 s98, s80\n"
      ".Lpk_218l:\n"
      "s_add_u32 s80, s68, s7\n"
      "buffer_load_dwordx4 v[12:15], v1, s[20:23], s80 offen offset:1024 sc1\n"
      "s_waitcnt vmcnt(12)\n"
      "buffer_load_dwordx4 v[244:247], v1, s[20:23], s80 offen offset:2048 sc1\n"
      "s_waitcnt vmcnt(12)\n"
      "buffer_load_dwordx4 v[248:251], v1, s[20:23], s80 offen offset:3072 sc1\n"
      "s_waitcnt vmcnt(0)\n"
      "v_cmp_eq_u32 vcc, -1, v12\n"
      "s_sub_u32 s79, s79, 1\n"
      "s_cmp_lt_i32 s79, 0\n"
      "s_cbranch_scc1 .Lpk_220\n"
      "s_cbranch_vccnz .Lpk_218l\n"
      ".Lpk_220:\n"
      "s_mov_b32 s80, s98\n"
      "s_branch .Lpr_219\n"
      ".Lpk_221:\n"
      "s_mov_b32 s98, s80\n"
      ".Lpk_221l:\n"
      "s_add_u32 s80, s68, s7\n"
      "buffer_load_dwordx4 v[244:247], v1, s[20:23], s80 offen offset:2048 sc1\n"
      "s_waitcnt vmcnt(12)\n"
      "buffer_load_dwordx4 v[248:251], v1, s[20:23], s80 offen offset:3072 sc1\n"
      "s_waitcnt vmcnt(0)\n"
      "v_cmp_eq_u32 vcc, -1, v244\n"
      "s_sub_u32 s79, s79, 1\n"
      "s_cmp_lt_i32 s79, 0\n"
      "s_cbranch_scc1 .Lpk_223\n"
      "s_cbranch_vccnz .Lpk_221l\n"
      ".Lpk_223:\n"
      "s_mov_b32 s80, s98\n"
      "s_branch .Lpr_222\n"
      ".Lpk_224:\n"
      "s_mov_b32 s98, s80\n"
      ".Lpk_224l:\n"
      "s_add_u32 s80, s68, s7\n"
      "buffer_load_dwordx4 v[248:251], v1, s[20:23], s80 offen offset:3072 sc1\n"
      "s_waitcnt vmcnt(0)\n"
      "v_cmp_eq_u32 vcc, -1, v248\n"
      "s_sub_u32 s79, s79, 1\n"
      "s_cmp_lt_i32 s79, 0\n"
      "s_cbranch_scc1 .Lpk_226\n"
      "s_cbranch_vccnz .Lpk_224l\n"
      ".Lpk_226:\n"
      "s_mov_b32 s80, s98\n"
      "s_branch .Lpr_225\n"
      ".Lpk_227:\n"
      "s_mov_b32 s98, s80\n"
      ".Lpk_227l:\n"
      "s_add_u32 s80, s68, s8\n"
      "buffer_load_dwordx4 v[252:255], v1, s[20:23], s80 offen sc1\n"
      "s_waitcnt vmcnt(12)\n"
      "buffer_load_dwordx4 v[224:227], v1, s[20:23], s80 offen offset:1024 sc1\n"
      "s_waitcnt vmcnt(12)\n"
      "buffer_load_dwordx4 v[228:231], v1, s[20:23], s80 offen offset:2048 sc1\n"
      "s_waitcnt vmcnt(12)\n"
      "buffer_load_dwordx4 v[232:235], v1, s[20:23], s80 offen offset:3072 sc1\n"
      "s_waitcnt vmcnt(0)\n"
      "v_cmp_eq_u32 vcc, -1, v252\n"
      "s_sub_u32 s79, s79, 1\n"
      "s_cmp_lt_i32 s79, 0\n"
      "s_cbranch_scc1 .Lpk_229\n"
      "s_cbranch_vccnz .Lpk_227l\n"
      ".Lpk_229:\n"
      "s_mov_b32 s80, s98\n"
      "s_branch .Lpr_228\n"
      ".Lpk_230:\n"
      "s_mov_b32 s98, s80\n"
      ".Lpk_230l:\n"
      "s_add_u32 s80, s68, s8\n"
      "buffer_load_dwordx4 v[224:227], v1, s[20:23], s80 offen offset:1024 sc1\n"
      "s_waitcnt vmcnt(12)\n"
      "buffer_load_dwordx4 v[228:231], v1, s[20:23], s80 offen offset:2048 sc1\n"
      "s_waitcnt vmcnt(12)\n"
      "buffer_load_dwordx4 v[232:235], v1, s[20:23], s80 offen offset:3072 sc1\n"
      "s_waitcnt vmcnt(0)\n"
      "v_cmp_eq_u32 vcc, -1, v224\n"
      "s_sub_u32 s79, s79, 1\n"
      "s_cmp_lt_i32 s79, 0\n"
      "s_cbranch_scc1 .Lpk_232\n"
      "s_cbranch_vccnz .Lpk_230l\n"
      ".Lpk_232:\n"
      "s_mov_b32 s80, s98\n"
      "s_branch .Lpr_231\n"
      ".Lpk_233:\n"
      "s_mov_b32 s98, s80\n"
      ".Lpk_233l:\n"
      "s_add_u32 s80, s68, s8\n"
      "buffer_load_dwordx4 v[228:231], v1, s[20:23], s80 offen offset:2048 sc1\n"
      "s_waitcnt vmcnt(12)\n"
      "buffer_load_dwordx4 v[232:235], v1, s[20:23], s80 offen offset:3072 sc1\n"
      "s_waitcnt vmcnt(0)\n"
      "v_cmp_eq_u32 vcc, -1, v228\n"
      "s_sub_u32 s79, s79, 1\n"
      "s_cmp_lt_i32 s79, 0\n"
      "s_cbranch_scc1 .Lpk_235\n"
      "s_cbranch_vccnz .Lpk_233l\n"
      ".Lpk_235:\n"
      "s_mov_b32 s80, s98\n"
      "s_branch .Lpr_234\n"
      ".Lpk_236:\n"
      "s_mov_b32 s98, s80\n"
      ".Lpk_236l:\n"
      "s_add_u32 s80, s68, s8\n"
      "buffer_load_dwordx4 v[232:235], v1, s[20:23], s80 offen offset:3072 sc1\n"
      "s_waitcnt vmcnt(0)\n"
      "v_cmp_eq_u32 vcc, -1, v232\n"
      "s_sub_u32 s79, s79, 1\n"
      "s_cmp_lt_i32 s79, 0\n"
      "s_cbranch_scc1 .Lpk_238\n"
      "s_cbranch_vccnz .Lpk_236l\n"
      ".Lpk_238:\n"
      "s_mov_b32 s80, s98\n"
      "s_branch .Lpr_237\n"
      ".Lpk_239:\n"
      "s_mov_b32 s98, s80\n"
      ".Lpk_239l:\n"
      "s_add_u32 s80, s68, s9\n"
      "buffer_load_dwordx4 v[236:239], v1, s[20:23], s80 offen sc1\n"
      "s_waitcnt vmcnt(12)\n"
      "buffer_load_dwordx4 v[240:243], v1, s[20:23], s80 offen offset:1024 sc1\n"
      "s_waitcnt vmcnt(12)\n"
      "buffer_load_dwordx4 v[128:131], v1, s[20:23], s80 offen offset:2048 sc1\n"
      "s_waitcnt vmcnt(12)\n"
      "buffer_load_dwordx4 v[132:135], v1, s[20:23], s80 offen offset:3072 sc1\n"
      "s_waitcnt vmcnt(0)\n"
      "v_cmp_eq_u32 vcc, -1, v236\n"
      "s_sub_u32 s79, s79, 1\n"
      "s_cmp_lt_i32 s79, 0\n"
      "s_cbranch_scc1 .Lpk_241\n"
      "s_cbranch_vccnz .Lpk_239l\n"
      ".Lpk_241:\n"
      "s_mov_b32 s80, s98\n"
      "s_branch .Lpr_240\n"
      ".Lpk_242:\n"
      "s_mov_b32 s98, s80\n"
      ".Lpk_242l:\n"
      "s_add_u32 s80, s68, s9\n"
      "buffer_load_dwordx4 v[240:243], v1, s[20:23], s80 offen offset:1024 sc1\n"
      "buffer_load_dwordx4 v[128:131], v1, s[20:23], s80 offen offset:2048 sc1\n"
      "s_waitcnt vmcnt(12)\n"
      "buffer_load_dwordx4 v[132:135], v1, s[20:23], s80 offen offset:3072 sc1\n"
      "s_waitcnt vmcnt(0)\n"
      "v_cmp_eq_u32 vcc, -1, v240\n"
      "s_sub_u32 s79, s79, 1\n"
      "s_cmp_lt_i32 s79, 0\n"
      "s_cbranch_scc1 .Lpk_244\n"
      "s_cbranch_vccnz .Lpk_242l\n"
      ".Lpk_244:\n"
      "s_mov_b32 s80, s98\n"
      "s_branch .Lpr_243\n"
      ".Lpk_245:\n"
      "s_mov_b32 s98, s80\n"
      ".Lpk_245l:\n"
      "s_add_u32 s80, s68, s9\n"
      "buffer_load_dwordx4 v[128:131], v1, s[20:23], s80 offen offset:2048 sc1\n"
      "buffer_load_dwordx4 v[132:135], v1, s[20:23], s80 offen offset:3072 sc1\n"
      "s_waitcnt vmcnt(0)\n"
      "v_cmp_eq_u32 vcc, -1, v128\n"
      "s_sub_u32 s79, s79, 1\n"
      "s_cmp_lt_i32 s79, 0\n"
      "s_cbranch_scc1 .Lpk_247\n"
      "s_cbranch_vccnz .Lpk_245l\n"
      ".Lpk_247:\n"
      "s_mov_b32 s80, s98\n"
      "s_branch .Lpr_246\n"
      ".Lpk_248:\n"
      "s_mov_b32 s98, s80\n"
      ".Lpk_248l:\n"
      "s_add_u32 s80, s68, s9\n"
      "buffer_load_dwordx4 v[132:135], v1, s[20:23], s80 offen offset:3072 sc1\n"
      "s_waitcnt vmcnt(0)\n"
      "v_cmp_eq_u32 vcc, -1, v132\n"
      "s_sub_u32 s79, s79, 1\n"
      "s_cmp_lt_i32 s79, 0\n"
      "s_cbranch_scc1 .Lpk_250\n"
      "s_cbranch_vccnz .Lpk_248l\n"
      ".Lpk_250:\n"
      "s_mov_b32 s80, s98\n"
      "s_branch .Lpr_249\n"
      ".Lb1_fix0:\n"
      "s_mov_b32 s81, s68\n"
      "s_add_u32 s82, s81, 0x8000\n"
      ".Lvf_251:\n"
      "buffer_load_dwordx4 v[208:211], v1, s[20:23], s81 offen sc1\n"
      "buffer_load_dwordx4 v[212:215], v1, s[20:23], s81 offen offset:1024 sc1\n"
      "s_sub_u32 s79, s79, 1\n"
      "s_cmp_lt_i32 s79, 0\n"
      "s_cbranch_scc1 .Lvf_252\n"
      "s_waitcnt vmcnt(1)\n"
      "v_max_u32 v216, v208, v209\n"
      "v_max3_u32 v216, v216, v210, v211\n"
      "s_waitcnt vmcnt(0)\n"
      "v_max3_u32 v216, v216, v212, v213\n"
      "v_max3_u32 v216, v216, v214, v215\n"
      "v_cmp_eq_u32 vcc, -1, v216\n"
      "s_nop 0\n"
      "s_cbranch_vccnz .Lvf_251\n"
      "s_add_u32 s81, s81, 0x800\n"
      "s_cmp_lt_u32 s81, s82\n"
      "s_cbranch_scc1 .Lvf_251\n"
      ".Lvf_252:\n"
      "s_lshl_b32 s81, s52, 6\n"
      "s_add_u32 s81, s81, 0x27280\n"
      "s_nop 0\n"
      "v_and_b32 v168, 32, v0\n"
      "v_add_u32 v168, s81, v168\n"
      "ds_read_b128 v[160:163], v168\n"
      "ds_read_b128 v[164:167], v168 offset:16\n"
      "v_mov_b32 v168, 0\n"
      "v_mov_b32 v169, 0\n"
      "v_mov_b32 v170, 0\n"
      "v_mov_b32 v171, 0\n"
      "v_mov_b32 v172, 0\n"
      "v_mov_b32 v173, 0\n"
      "v_mov_b32 v174, 0\n"
      "v_mov_b32 v175, 0\n"
      "s_add_u32 s81, s68, s2\n"
      "s_waitcnt vmcnt(1)\n"
      "buffer_load_dwordx4 v[208:211], v1, s[20:23], s81 offen sc1\n"
      "s_waitcnt vmcnt(1)\n"
      "buffer_load_dwordx4 v[212:215], v1, s[20:23], s81 offen offset:1024 sc1\n"
      "buffer_load_dwordx4 v[216:219], v1, s[20:23], s81 offen offset:2048 sc1\n"
      "buffer_load_dwordx4 v[220:223], v1, s[20:23], s81 offen offset:3072 sc1\n"
      "s_waitcnt vmcnt(3)\n"
      "v_mfma_f32_32x32x16_bf16 v[176:191], a[4:7], v[208:211], 0\n"
      "s_waitcnt lgkmcnt(0)\n"
      "v_mfma_f32_32x32x16_bf16 v[160:175], a[0:3], v[208:211], v[160:175]\n"
      "s_waitcnt vmcnt(2)\n"
      "v_mfma_f32_32x32x16_bf16 v[176:191], a[12:15], v[212:215], v[176:191]\n"
      "v_mfma_f32_32x32x16_bf16 v[160:175], a[8:11], v[212:215], v[160:175]\n"
      "s_waitcnt vmcnt(1)\n"
      "v_mfma_f32_32x32x16_bf16 v[176:191], a[20:23], v[216:219], v[176:191]\n"
      "v_mfma_f32_32x32x16_bf16 v[160:175], a[16:19], v[216:219], v[160:175]\n"
      "s_waitcnt vmcnt(0)\n"
      "v_mfma_f32_32x32x16_bf16 v[176:191], a[28:31], v[220:223], v[176:191]\n"
      "v_mfma_f32_32x32x16_bf16 v[160:175], a[24:27], v[220:223], v[160:175]\n"
      "s_add_u32 s81, s68, s3\n"
      "buffer_load_dwordx4 v[208:211], v1, s[20:23], s81 offen sc1\n"
      "buffer_load_dwordx4 v[212:215], v1, s[20:23], s81 offen offset:1024 sc1\n"
      "buffer_load_dwordx4 v[216:219], v1, s[20:23], s81 offen offset:2048 sc1\n"
      "buffer_load_dwordx4 v[220:223], v1, s[20:23], s81 offen offset:3072 sc1\n"
      "s_waitcnt vmcnt(3)\n"
      "v_mfma_f32_32x32x16_bf16 v[176:191], a[36:39], v[208:211], v[176:191]\n"
      "v_mfma_f32_32x32x16_bf16 v[160:175], a[32:35], v[208:211], v[160:175]\n"
      "s_waitcnt vmcnt(2)\n"
      "v_mfma_f32_32x32x16_bf16 v[176:191], a[44:47], v[212:215], v[176:191]\n"
      "v_mfma_f32_32x32x16_bf16 v[160:175], a[40:43], v[212:215], v[160:175]\n"
      "s_waitcnt vmcnt(1)\n"
      "v_mfma_f32_32x32x16_bf16 v[176:191], a[52:55], v[216:219], v[176:191]\n"
      "v_mfma_f32_32x32x16_bf16 v[160:175], a[48:51], v[216:219], v[160:175]\n"
      "s_waitcnt vmcnt(0)\n"
      "v_mfma_f32_32x32x16_bf16 v[176:191], a[60:63], v[220:223], v[176:191]\n"
      "v_mfma_f32_32x32x16_bf16 v[160:175], a[56:59], v[220:223], v[160:175]\n"
      "s_add_u32 s81, s68, s4\n"
      "buffer_load_dwordx4 v[208:211], v1, s[20:23], s81 offen sc1\n"
      "buffer_load_dwordx4 v[212:215], v1, s[20:23], s81 offen offset:1024 sc1\n"
      "buffer_load_dwordx4 v[216:219], v1, s[20:23], s81 offen offset:2048 sc1\n"
      "buffer_load_dwordx4 v[220:223], v1, s[20:23], s81 offen offset:3072 sc1\n"
      "s_waitcnt vmcnt(3)\n"
      "v_mfma_f32_32x32x16_bf16 v[176:191], a[68:71], v[208:211], v[176:191]\n"
      "v_mfma_f32_32x32x16_bf16 v[160:175], a[64:67], v[208:211], v[160:175]\n"
      "s_waitcnt vmcnt(2)\n"
      "v_mfma_f32_32x32x16_bf16 v[176:191], a[76:79], v[212:215], v[176:191]\n"
      "v_mfma_f32_32x32x16_bf16 v[160:175], a[72:75], v[212:215], v[160:175]\n"
      "s_waitcnt vmcnt(1)\n"
      "v_mfma_f32_32x32x16_bf16 v[176:191], a[84:87], v[216:219], v[176:191]\n"
      "v_mfma_f32_32x32x16_bf16 v[160:175], a[80:83], v[216:219], v[160:175]\n"
      "s_waitcnt vmcnt(0)\n"
      "v_mfma_f32_32x32x16_bf16 v[176:191], a[92:95], v[220:223], v[176:191]\n"
      "v_mfma_f32_32x32x16_bf16 v[160:175], a[88:91], v[220:223], v[160:175]\n"
      "s_add_u32 s81, s68, s5\n"
      "buffer_load_dwordx4 v[208:211], v1, s[20:23], s81 offen sc1\n"
      "buffer_load_dwordx4 v[212:215], v1, s[20:23], s81 offen offset:1024 sc1\n"
      "buffer_load_dwordx4 v[216:219], v1, s[20:23], s81 offen offset:2048 sc1\n"
      "buffer_load_dwordx4 v[220:223], v1, s[20:23], s81 offen offset:3072 sc1\n"
      "s_waitcnt vmcnt(3)\n"
      "v_mfma_f32_32x32x16_bf16 v[176:191], a[100:103], v[208:211], v[176:191]\n"
      "v_mfma_f32_32x32x16_bf16 v[160:175], a[96:99], v[208:211], v[160:175]\n"
      "s_waitcnt vmcnt(2)\n"
      "v_mfma_f32_32x32x16_bf16 v[176:191], a[108:111], v[212:215], v[176:191]\n"
      "v_mfma_f32_32x32x16_bf16 v[160:175], a[104:107], v[212:215], v[160:175]\n"
      "s_waitcnt vmcnt(1)\n"
      "v_mfma_f32_32x32x16_bf16 v[176:191], a[116:119], v[216:219], v[176:191]\n"
      "v_mfma_f32_32x32x16_bf16 v[160:175], a[112:115], v[216:219], v[160:175]\n"
      "s_waitcnt vmcnt(0)\n"
      "v_mfma_f32_32x32x16_bf16 v[176:191], a[124:127], v[220:223], v[176:191]\n"
      "v_mfma_f32_32x32x16_bf16 v[160:175], a[120:123], v[220:223], v[160:175]\n"
      "s_add_u32 s81, s68, s6\n"
      "buffer_load_dwordx4 v[208:211], v1, s[20:23], s81 offen sc1\n"
      "buffer_load_dwordx4 v[212:215], v1, s[20:23], s81 offen offset:1024 sc1\n"
      "buffer_load_dwordx4 v[216:219], v1, s[20:23], s81 offen offset:2048 sc1\n"
      "buffer_load_dwordx4 v[220:223], v1, s[20:23], s81 offen offset:3072 sc1\n"
      "s_waitcnt vmcnt(3)\n"
      "v_mfma_f32_32x32x16_bf16 v[176:191], a[132:135], v[208:211], v[176:191]\n"
      "v_mfma_f32_32x32x16_bf16 v[160:175], a[128:131], v[208:211], v[160:175]\n"
      "s_waitcnt vmcnt(2)\n"
      "v_mfma_f32_32x32x16_bf16 v[176:191], a[140:143], v[212:215], v[176:191]\n"
      "v_mfma_f32_32x32x16_bf16 v[160:175], a[136:139], v[212:215], v[160:175]\n"
      "s_waitcnt vmcnt(1)\n"
      "v_mfma_f32_32x32x16_bf16 v[176:191], a[148:151], v[216:219], v[176:191]\n"
      "v_mfma_f32_32x32x16_bf16 v[160:175], a[144:147], v[216:219], v[160:175]\n"
      "s_waitcnt vmcnt(0)\n"
      "v_mfma_f32_32x32x16_bf16 v[176:191], a[156:159], v[220:223], v[176:191]\n"
      "v_mfma_f32_32x32x16_bf16 v[160:175], a[152:155], v[220:223], v[160:175]\n"
      "s_add_u32 s81, s68, s7\n"
      "buffer_load_dwordx4 v[208:211], v1, s[20:23], s81 offen sc1\n"
      "buffer_load_dwordx4 v[212:215], v1, s[20:23], s81 offen offset:1024 sc1\n"
      "buffer_load_dwordx4 v[216:219], v1, s[20:23], s81 offen offset:2048 sc1\n"
      "buffer_load_dwordx4 v[220:223], v1, s[20:23], s81 offen offset:3072 sc1\n"
      "s_waitcnt vmcnt(3)\n"
      "v_mfma_f32_32x32x16_bf16 v[176:191], a[164:167], v[208:211], v[176:191]\n"
      "v_mfma_f32_32x32x16_bf16 v[160:175], a[160:163], v[208:211], v[160:175]\n"
      "s_waitcnt vmcnt(2)\n"
      "v_mfma_f32_32x32x16_bf16 v[176:191], a[172:175], v[212:215], v[176:191]\n"
      "v_mfma_f32_32x32x16_bf16 v[160:175], a[168:171], v[212:215], v[160:175]\n"
      "s_waitcnt vmcnt(1)\n"
      "v_mfma_f32_32x32x16_bf16 v[176:191], a[180:183], v[216:219], v[176:191]\n"
      "v_mfma_f32_32x32x16_bf16 v[160:175], a[176:179], v[216:219], v[160:175]\n"
      "s_waitcnt vmcnt(0)\n"
      "v_mfma_f32_32x32x16_bf16 v[176:191], a[188:191], v[220:223], v[176:191]\n"
      "v_mfma_f32_32x32x16_bf16 v[160:175], a[184:187], v[220:223], v[160:175]\n"
      "s_add_u32 s81, s68, s8\n"
      "buffer_load_dwordx4 v[208:211], v1, s[20:23], s81 offen sc1\n"
      "buffer_load_dwordx4 v[212:215], v1, s[20:23], s81 offen offset:1024 sc1\n"
      "buffer_load_dwordx4 v[216:219], v1, s[20:23], s81 offen offset:2048 sc1\n"
      "buffer_load_dwordx4 v[220:223], v1, s[20:23], s81 offen offset:3072 sc1\n"
      "s_waitcnt vmcnt(3)\n"
      "v_mfma_f32_32x32x16_bf16 v[176:191], a[196:199], v[208:211], v[176:191]\n"
      "v_mfma_f32_32x32x16_bf16 v[160:175], a[192:195], v[208:211], v[160:175]\n"
      "s_waitcnt vmcnt(2)\n"
      "v_mfma_f32_32x32x16_bf16 v[176:191], a[204:207], v[212:215], v[176:191]\n"
      "v_mfma_f32_32x32x16_bf16 v[160:175], a[200:203], v[212:215], v[160:175]\n"
      "s_waitcnt vmcnt(1)\n"
      "v_mfma_f32_32x32x16_bf16 v[176:191], a[212:215], v[216:219], v[176:191]\n"
      "v_mfma_f32_32x32x16_bf16 v[160:175], a[208:211], v[216:219], v[160:175]\n"
      "s_waitcnt vmcnt(0)\n"
      "v_mfma_f32_32x32x16_bf16 v[176:191], a[220:223], v[220:223], v[176:191]\n"
      "v_mfma_f32_32x32x16_bf16 v[160:175], a[216:219], v[220:223], v[160:175]\n"
      "s_add_u32 s81, s68, s9\n"
      "buffer_load_dwordx4 v[208:211], v1, s[20:23], s81 offen sc1\n"
      "buffer_load_dwordx4 v[212:215], v1, s[20:23], s81 offen offset:1024 sc1\n"
      "buffer_load_dwordx4 v[216:219], v1, s[20:23], s81 offen offset:2048 sc1\n"
      "buffer_load_dwordx4 v[220:223], v1, s[20:23], s81 offen offset:3072 sc1\n"
      "s_waitcnt vmcnt(3)\n"
      "v_mfma_f32_32x32x16_bf16 v[176:191], a[228:231], v[208:211], v[176:191]\n"
      "v_mfma_f32_32x32x16_bf16 v[160:175], a[224:227], v[208:211], v[160:175]\n"
      "s_waitcnt vmcnt(2)\n"
      "v_mfma_f32_32x32x16_bf16 v[176:191], a[236:239], v[212:215], v[176:191]\n"
      "v_mfma_f32_32x32x16_bf16 v[160:175], a[232:235], v[212:215], v[160:175]\n"
      "s_waitcnt vmcnt(1)\n"
      "v_mfma_f32_32x32x16_bf16 v[176:191], a[244:247], v[216:219], v[176:191]\n"
      "v_mfma_f32_32x32x16_bf16 v[160:175], a[240:243], v[216:219], v[160:175]\n"
      "s_waitcnt vmcnt(0)\n"
      "v_mfma_f32_32x32x16_bf16 v[176:191], a[252:255], v[220:223], v[176:191]\n"
      "v_mfma_f32_32x32x16_bf16 v[160:175], a[248:251], v[220:223], v[160:175]\n"
      "s_nop 10\n"
      "ds_write_b128 v9, v[184:187]\n"
      "s_lshl_b32 s81, s52, 6\n"
      "s_add_u32 s81, s81, 0x27280\n"
      "v_and_b32 v200, 32, v0\n"
      "v_add_u32 v200, s81, v200\n"
      "ds_read_b128 v[192:195], v200\n"
      "ds_read_b128 v[196:199], v200 offset:16\n"
      "v_mov_b32 v200, 0\n"
      "v_mov_b32 v201, 0\n"
      "v_mov_b32 v202, 0\n"
      "v_mov_b32 v203, 0\n"
      "v_mov_b32 v204, 0\n"
      "v_mov_b32 v205, 0\n"
      "v_mov_b32 v206, 0\n"
      "v_mov_b32 v207, 0\n"
      "s_waitcnt vmcnt(0) lgkmcnt(0)\n"
      "s_nop 15\n"
      "s_nop 7\n"
      "s_branch .Lb1_ok0\n"
      ".Lb1_fix1:\n"
      "s_add_u32 s81, s68, 0x20000\n"
      "s_add_u32 s82, s81, 0x8000\n"
      ".Lvf_253:\n"
      "buffer_load_dwordx4 v[176:179], v1, s[20:23], s81 offen sc1\n"
      "buffer_load_dwordx4 v[180:183], v1, s[20:23], s81 offen offset:1024 sc1\n"
      "s_sub_u32 s79, s79, 1\n"
      "s_cmp_lt_i32 s79, 0\n"
      "s_cbranch_scc1 .Lvf_254\n"
      "s_waitcnt vmcnt(1)\n"
      "v_max_u32 v184, v176, v177\n"
      "v_max3_u32 v184, v184, v178, v179\n"
      "s_waitcnt vmcnt(0)\n"
      "v_max3_u32 v184, v184, v180, v181\n"
      "v_max3_u32 v184, v184, v182, v183\n"
      "v_cmp_eq_u32 vcc, -1, v184\n"
      "s_nop 0\n"
      "s_cbranch_vccnz .Lvf_253\n"
      "s_add_u32 s81, s81, 0x800\n"
      "s_cmp_lt_u32 s81, s82\n"
      "s_cbranch_scc1 .Lvf_253\n"
      ".Lvf_254:\n"
      "s_lshl_b32 s81, s52, 6\n"
      "s_add_u32 s81, s81, 0x27280\n"
      "s_nop 0\n"
      "v_and_b32 v200, 32, v0\n"
      "v_add_u32 v200, s81, v200\n"
      "ds_read_b128 v[192:195], v200\n"
      "ds_read_b128 v[196:199], v200 offset:16\n"
      "v_mov_b32 v200, 0\n"
      "v_mov_b32 v201, 0\n"
      "v_mov_b32 v202, 0\n"
      "v_mov_b32 v203, 0\n"
      "v_mov_b32 v204, 0\n"
      "v_mov_b32 v205, 0\n"
      "v_mov_b32 v206, 0\n"
      "v_mov_b32 v207, 0\n"
      "s_add_u32 s81, s68, s2\n"
      "s_add_u32 s81, s81, 0x20000\n"
      "s_waitcnt vmcnt(1)\n"
      "buffer_load_dwordx4 v[176:179], v1, s[20:23], s81 offen sc1\n"
      "s_waitcnt vmcnt(1)\n"
      "buffer_load_dwordx4 v[180:183], v1, s[20:23], s81 offen offset:1024 sc1\n"
      "buffer_load_dwordx4 v[184:187], v1, s[20:23], s81 offen offset:2048 sc1\n"
      "buffer_load_dwordx4 v[188:191], v1, s[20:23], s81 offen offset:3072 sc1\n"
      "s_waitcnt vmcnt(3)\n"
      "v_mfma_f32_32x32x16_bf16 v[208:223], a[4:7], v[176:179], 0\n"
      "s_waitcnt lgkmcnt(0)\n"
      "v_mfma_f32_32x32x16_bf16 v[192:207], a[0:3], v[176:179], v[192:207]\n"
      "s_waitcnt vmcnt(2)\n"
      "v_mfma_f32_32x32x16_bf16 v[208:223], a[12:15], v[180:183], v[208:223]\n"
      "v_mfma_f32_32x32x16_bf16 v[192:207], a[8:11], v[180:183], v[192:207]\n"
      "s_waitcnt vmcnt(1)\n"
      "v_mfma_f32_32x32x16_bf16 v[208:223], a[20:23], v[184:187], v[208:223]\n"
      "v_mfma_f32_32x32x16_bf16 v[192:207], a[16:19], v[184:187], v[192:207]\n"
      "s_waitcnt vmcnt(0)\n"
      "v_mfma_f32_32x32x16_bf16 v[208:223], a[28:31], v[188:191], v[208:223]\n"
      "v_mfma_f32_32x32x16_bf16 v[192:207], a[24:27], v[188:191], v[192:207]\n"
      "s_add_u32 s81, s68, s3\n"
      "s_add_u32 s81, s81, 0x20000\n"
      "buffer_load_dwordx4 v[176:179], v1, s[20:23], s81 offen sc1\n"
      "buffer_load_dwordx4 v[180:183], v1, s[20:23], s81 offen offset:1024 sc1\n"
      "buffer_load_dwordx4 v[184:187], v1, s[20:23], s81 offen offset:2048 sc1\n"
      "buffer_load_dwordx4 v[188:191], v1, s[20:23], s81 offen offset:3072 sc1\n"
      "s_waitcnt vmcnt(3)\n"
      "v_mfma_f32_32x32x16_bf16 v[208:223], a[36:39], v[176:179], v[208:223]\n"
      "v_mfma_f32_32x32x16_bf16 v[192:207], a[32:35], v[176:179], v[192:207]\n"
      "s_waitcnt vmcnt(2)\n"
      "v_mfma_f32_32x32x16_bf16 v[208:223], a[44:47], v[180:183], v[208:223]\n"
      "v_mfma_f32_32x32x16_bf16 v[192:207], a[40:43], v[180:183], v[192:207]\n"
      "s_waitcnt vmcnt(1)\n"
      "v_mfma_f32_32x32x16_bf16 v[208:223], a[52:55], v[184:187], v[208:223]\n"
      "v_mfma_f32_32x32x16_bf16 v[192:207], a[48:51], v[184:187], v[192:207]\n"
      "s_waitcnt vmcnt(0)\n"
      "v_mfma_f32_32x32x16_bf16 v[208:223], a[60:63], v[188:191], v[208:223]\n"
      "v_mfma_f32_32x32x16_bf16 v[192:207], a[56:59], v[188:191], v[192:207]\n"
      "s_add_u32 s81, s68, s4\n"
      "s_add_u32 s81, s81, 0x20000\n"
      "buffer_load_dwordx4 v[176:179], v1, s[20:23], s81 offen sc1\n"
      "buffer_load_dwordx4 v[180:183], v1, s[20:23], s81 offen offset:1024 sc1\n"
      "buffer_load_dwordx4 v[184:187], v1, s[20:23], s81 offen offset:2048 sc1\n"
      "buffer_load_dwordx4 v[188:191], v1, s[20:23], s81 offen offset:3072 sc1\n"
      "s_waitcnt vmcnt(3)\n"
      "v_mfma_f32_32x32x16_bf16 v[208:223], a[68:71], v[176:179], v[208:223]\n"
      "v_mfma_f32_32x32x16_bf16 v[192:207], a[64:67], v[176:179], v[192:207]\n"
      "s_waitcnt vmcnt(2)\n"
      "v_mfma_f32_32x32x16_bf16 v[208:223], a[76:79], v[180:183], v[208:223]\n"
      "v_mfma_f32_32x32x16_bf16 v[192:207], a[72:75], v[180:183], v[192:207]\n"
      "s_waitcnt vmcnt(1)\n"
      "v_mfma_f32_32x32x16_bf16 v[208:223], a[84:87], v[184:187], v[208:223]\n"
      "v_mfma_f32_32x32x16_bf16 v[192:207], a[80:83], v[184:187], v[192:207]\n"
      "s_waitcnt vmcnt(0)\n"
      "v_mfma_f32_32x32x16_bf16 v[208:223], a[92:95], v[188:191], v[208:223]\n"
      "v_mfma_f32_32x32x16_bf16 v[192:207], a[88:91], v[188:191], v[192:207]\n"
      "s_add_u32 s81, s68, s5\n"
      "s_add_u32 s81, s81, 0x20000\n"
      "buffer_load_dwordx4 v[176:179], v1, s[20:23], s81 offen sc1\n"
      "buffer_load_dwordx4 v[180:183], v1, s[20:23], s81 offen offset:1024 sc1\n"
      "buffer_load_dwordx4 v[184:187], v1, s[20:23], s81 offen offset:2048 sc1\n"
      "buffer_load_dwordx4 v[188:191], v1, s[20:23], s81 offen offset:3072 sc1\n"
      "s_waitcnt vmcnt(3)\n"
      "v_mfma_f32_32x32x16_bf16 v[208:223], a[100:103], v[176:179], v[208:223]\n"
      "v_mfma_f32_32x32x16_bf16 v[192:207], a[96:99], v[176:179], v[192:207]\n"
      "s_waitcnt vmcnt(2)\n"
      "v_mfma_f32_32x32x16_bf16 v[208:223], a[108:111], v[180:183], v[208:223]\n"
      "v_mfma_f32_32x32x16_bf16 v[192:207], a[104:107], v[180:183], v[192:207]\n"
      "s_waitcnt vmcnt(1)\n"
      "v_mfma_f32_32x32x16_bf16 v[208:223], a[116:119], v[184:187], v[208:223]\n"
      "v_mfma_f32_32x32x16_bf16 v[192:207], a[112:115], v[184:187], v[192:207]\n"
      "s_waitcnt vmcnt(0)\n"
      "v_mfma_f32_32x32x16_bf16 v[208:223], a[124:127], v[188:191], v[208:223]\n"
      "v_mfma_f32_32x32x16_bf16 v[192:207], a[120:123], v[188:191], v[192:207]\n"
      "s_add_u32 s81, s68, s6\n"
      "s_add_u32 s81, s81, 0x20000\n"
      "buffer_load_dwordx4 v[176:179], v1, s[20:23], s81 offen sc1\n"
      "buffer_load_dwordx4 v[180:183], v1, s[20:23], s81 offen offset:1024 sc1\n"
      "buffer_load_dwordx4 v[184:187], v1, s[20:23], s81 offen offset:2048 sc1\n"
      "buffer_load_dwordx4 v[188:191], v1, s[20:23], s81 offen offset:3072 sc1\n"
      "s_waitcnt vmcnt(3)\n"
      "v_mfma_f32_32x32x16_bf16 v[208:223], a[132:135], v[176:179], v[208:223]\n"
      "v_mfma_f32_32x32x16_bf16 v[192:207], a[128:131], v[176:179], v[192:207]\n"
      "s_waitcnt vmcnt(2)\n"
      "v_mfma_f32_32x32x16_bf16 v[208:223], a[140:143], v[180:183], v[208:223]\n"
      "v_mfma_f32_32x32x16_bf16 v[192:207], a[136:139], v[180:183], v[192:207]\n"
      "s_waitcnt vmcnt(1)\n"
      "v_mfma_f32_32x32x16_bf16 v[208:223], a[148:151], v[184:187], v[208:223]\n"
      "v_mfma_f32_32x32x16_bf16 v[192:207], a[144:147], v[184:187], v[192:207]\n"
      "s_waitcnt vmcnt(0)\n"
      "v_mfma_f32_32x32x16_bf16 v[208:223], a[156:159], v[188:191], v[208:223]\n"
      "v_mfma_f32_32x32x16_bf16 v[192:207], a[152:155], v[188:191], v[192:207]\n"
      "s_add_u32 s81, s68, s7\n"
      "s_add_u32 s81, s81, 0x20000\n"
      "buffer_load_dwordx4 v[176:179], v1, s[20:23], s81 offen sc1\n"
      "buffer_load_dwordx4 v[180:183], v1, s[20:23], s81 offen offset:1024 sc1\n"
      "buffer_load_dwordx4 v[184:187], v1, s[20:23], s81 offen offset:2048 sc1\n"
      "buffer_load_dwordx4 v[188:191], v1, s[20:23], s81 offen offset:3072 sc1\n"
      "s_waitcnt vmcnt(3)\n"
      "v_mfma_f32_32x32x16_bf16 v[208:223], a[164:167], v[176:179], v[208:223]\n"
      "v_mfma_f32_32x32x16_bf16 v[192:207], a[160:163], v[176:179], v[192:207]\n"
      "s_waitcnt vmcnt(2)\n"
      "v_mfma_f32_32x32x16_bf16 v[208:223], a[172:175], v[180:183], v[208:223]\n"
      "v_mfma_f32_32x32x16_bf16 v[192:207], a[168:171], v[180:183], v[192:207]\n"
      "s_waitcnt vmcnt(1)\n"
      "v_mfma_f32_32x32x16_bf16 v[208:223], a[180:183], v[184:187], v[208:223]\n"
      "v_mfma_f32_32x32x16_bf16 v[192:207], a[176:179], v[184:187], v[192:207]\n"
      "s_waitcnt vmcnt(0)\n"
      "v_mfma_f32_32x32x16_bf16 v[208:223], a[188:191], v[188:191], v[208:223]\n"
      "v_mfma_f32_32x32x16_bf16 v[192:207], a[184:187], v[188:191], v[192:207]\n"
      "s_add_u32 s81, s68, s8\n"
      "s_add_u32 s81, s81, 0x20000\n"
      "buffer_load_dwordx4 v[176:179], v1, s[20:23], s81 offen sc1\n"
      "buffer_load_dwordx4 v[180:183], v1, s[20:23], s81 offen offset:1024 sc1\n"
      "buffer_load_dwordx4 v[184:187], v1, s[20:23], s81 offen offset:2048 sc1\n"
      "buffer_load_dwordx4 v[188:191], v1, s[20:23], s81 offen offset:3072 sc1\n"
      "s_waitcnt vmcnt(3)\n"
      "v_mfma_f32_32x32x16_bf16 v[208:223], a[196:199], v[176:179], v[208:223]\n"
      "v_mfma_f32_32x32x16_bf16 v[192:207], a[192:195], v[176:179], v[192:207]\n"
      "s_waitcnt vmcnt(2)\n"
      "v_mfma_f32_32x32x16_bf16 v[208:223], a[204:207], v[180:183], v[208:223]\n"
      "v_mfma_f32_32x32x16_bf16 v[192:207], a[200:203], v[180:183], v[192:207]\n"
      "s_waitcnt vmcnt(1)\n"
      "v_mfma_f32_32x32x16_bf16 v[208:223], a[212:215], v[184:187], v[208:223]\n"
      "v_mfma_f32_32x32x16_bf16 v[192:207], a[208:211], v[184:187], v[192:207]\n"
      "s_waitcnt vmcnt(0)\n"
      "v_mfma_f32_32x32x16_bf16 v[208:223], a[220:223], v[188:191], v[208:223]\n"
      "v_mfma_f32_32x32x16_bf16 v[192:207], a[216:219], v[188:191], v[192:207]\n"
      "s_add_u32 s81, s68, s9\n"
      "s_add_u32 s81, s81, 0x20000\n"
      "buffer_load_dwordx4 v[176:179], v1, s[20:23], s81 offen sc1\n"
      "buffer_load_dwordx4 v[180:183], v1, s[20:23], s81 offen offset:1024 sc1\n"
      "buffer_load_dwordx4 v[184:187], v1, s[20:23], s81 offen offset:2048 sc1\n"
      "buffer_load_dwordx4 v[188:191], v1, s[20:23], s81 offen offset:3072 sc1\n"
      "s_waitcnt vmcnt(3)\n"
      "v_mfma_f32_32x32x16_bf16 v[208:223], a[228:231], v[176:179], v[208:223]\n"
      "v_mfma_f32_32x32x16_bf16 v[192:207], a[224:227], v[176:179], v[192:207]\n"
      "s_waitcnt vmcnt(2)\n"
      "v_mfma_f32_32x32x16_bf16 v[208:223], a[236:239], v[180:183], v[208:223]\n"
      "v_mfma_f32_32x32x16_bf16 v[192:207], a[232:235], v[180:183], v[192:207]\n"
      "s_waitcnt vmcnt(1)\n"
      "v_mfma_f32_32x32x16_bf16 v[208:223], a[244:247], v[184:187], v[208:223]\n"
      "v_mfma_f32_32x32x16_bf16 v[192:207], a[240:243], v[184:187], v[192:207]\n"
      "s_waitcnt vmcnt(0)\n"
      "v_mfma_f32_32x32x16_bf16 v[208:223], a[252:255], v[188:191], v[208:223]\n"
      "v_mfma_f32_32x32x16_bf16 v[192:207], a[248:251], v[188:191], v[192:207]\n"
      "s_nop 10\n"
      "ds_write_b128 v9, v[216:219]\n"
      "ds_write_b128 v9, v[220:223] offset:1024\n"
      "ds_write_b128 v8, v[208:211]\n"
      "ds_write_b128 v8, v[212:215] offset:1024\n"
      "v_lshlrev_b32 v160, 16, v18\n"
      "v_and_b32 v161, 0xffff0000, v18\n"
      "v_lshlrev_b32 v162, 16, v19\n"
      "v_and_b32 v163, 0xffff0000, v19\n"
      "v_lshlrev_b32 v164, 16, v20\n"
      "v_and_b32 v165, 0xffff0000, v20\n"
      "v_lshlrev_b32 v166, 16, v21\n"
      "v_and_b32 v167, 0xffff0000, v21\n"
      "v_pk_add_f32 v[160:161], v[160:161], v[26:27] op_sel_hi:[1,0]\n"
      "v_pk_add_f32 v[162:163], v[162:163], v[26:27] op_sel_hi:[1,0]\n"
      "v_pk_add_f32 v[164:165], v[164:165], v[26:27] op_sel_hi:[1,0]\n"
      "v_pk_add_f32 v[166:167], v[166:167], v[26:27] op_sel_hi:[1,0]\n"
      "v_mov_b32 v168, 0\n"
      "v_mov_b32 v169, 0\n"
      "v_mov_b32 v170, 0\n"
      "v_mov_b32 v171, 0\n"
      "v_mov_b32 v172, 0\n"
      "v_mov_b32 v173, 0\n"
      "v_mov_b32 v174, 0\n"
      "v_mov_b32 v175, 0\n"
      "s_waitcnt vmcnt(0) lgkmcnt(0)\n"
      "s_nop 15\n"
      "s_nop 7\n"
      "s_branch .Lb1_ok1\n"
      ".Lpk_259:\n"
      "s_mov_b32 s98, s80\n"
      ".Lpk_259l:\n"
      "s_add_u32 s80, s69, s10\n"
      "buffer_load_dwordx4 v[128:131], v28, s[20:23], s80 offen sc1\n"
      "s_waitcnt vmcnt(5)\n"
      "buffer_load_dwordx4 v[132:135], v28, s[20:23], s80 offen offset:1024 sc1\n"
      "s_waitcnt vmcnt(5)\n"
      "buffer_load_dwordx4 v[136:139], v28, s[20:23], s80 offen offset:2048 sc1\n"
      "s_waitcnt vmcnt(5)\n"
      "buffer_load_dwordx4 v[140:143], v28, s[20:23], s80 offen offset:3072 sc1\n"
      "s_waitcnt vmcnt(0)\n"
      "v_cmp_eq_u32 vcc, -1, v128\n"
      "s_sub_u32 s79, s79, 1\n"
      "s_cmp_lt_i32 s79, 0\n"
      "s_cbranch_scc1 .Lpk_261\n"
      "s_cbranch_vccnz .Lpk_259l\n"
      ".Lpk_261:\n"
      "s_mov_b32 s80, s98\n"
      "s_branch .Lpr_260\n"
      ".Lpk_262:\n"
      "s_mov_b32 s98, s80\n"
      ".Lpk_262l:\n"
      "s_add_u32 s80, s69, s10\n"
      "buffer_load_dwordx4 v[132:135], v28, s[20:23], s80 offen offset:1024 sc1\n"
      "s_waitcnt vmcnt(5)\n"
      "buffer_load_dwordx4 v[136:139], v28, s[20:23], s80 offen offset:2048 sc1\n"
      "s_waitcnt vmcnt(5)\n"
      "buffer_load_dwordx4 v[140:143], v28, s[20:23], s80 offen offset:3072 sc1\n"
      "s_waitcnt vmcnt(0)\n"
      "v_cmp_eq_u32 vcc, -1, v132\n"
      "s_sub_u32 s79, s79, 1\n"
      "s_cmp_lt_i32 s79, 0\n"
      "s_cbranch_scc1 .Lpk_264\n"
      "s_cbranch_vccnz .Lpk_262l\n"
      ".Lpk_264:\n"
      "s_mov_b32 s80, s98\n"
      "s_branch .Lpr_263\n"
      ".Lpk_265:\n"
      "s_mov_b32 s98, s80\n"
      ".Lpk_265l:\n"
      "s_add_u32 s80, s69, s10\n"
      "buffer_load_dwordx4 v[136:139], v28, s[20:23], s80 offen offset:2048 sc1\n"
      "s_waitcnt vmcnt(6)\n"
      "buffer_load_dwordx4 v[140:143], v28, s[20:23], s80 offen offset:3072 sc1\n"
      "s_waitcnt vmcnt(0)\n"
      "v_cmp_eq_u32 vcc, -1, v136\n"
      "s_sub_u32 s79, s79, 1\n"
      "s_cmp_lt_i32 s79, 0\n"
      "s_cbranch_scc1 .Lpk_267\n"
      "s_cbranch_vccnz .Lpk_265l\n"
      ".Lpk_267:\n"
      "s_mov_b32 s80, s98\n"
      "s_branch .Lpr_266\n"
      ".Lpk_268:\n"
      "s_mov_b32 s98, s80\n"
      ".Lpk_268l:\n"
      "s_add_u32 s80, s69, s10\n"
      "buffer_load_dwordx4 v[140:143], v28, s[20:23], s80 offen offset:3072 sc1\n"
      "s_waitcnt vmcnt(0)\n"
      "v_cmp_eq_u32 vcc, -1, v140\n"
      "s_sub_u32 s79, s79, 1\n"
      "s_cmp_lt_i32 s79, 0\n"
      "s_cbranch_scc1 .Lpk_270\n"
      "s_cbranch_vccnz .Lpk_268l\n"
      ".Lpk_270:\n"
      "s_mov_b32 s80, s98\n"
      "s_branch .Lpr_269\n"
      ".Lpk_271:\n"
      "s_mov_b32 s98, s80\n"
      ".Lpk_271l:\n"
      "s_add_u32 s80, s69, s11\n"
      "buffer_load_dwordx4 v[144:147], v28, s[20:23], s80 offen sc1\n"
      "s_waitcnt vmcnt(7)\n"
      "buffer_load_dwordx4 v[148:151], v28, s[20:23], s80 offen offset:1024 sc1\n"
      "s_waitcnt vmcnt(7)\n"
      "buffer_load_dwordx4 v[152:155], v28, s[20:23], s80 offen offset:2048 sc1\n"
      "s_waitcnt vmcnt(7)\n"
      "buffer_load_dwordx4 v[156:159], v28, s[20:23], s80 offen offset:3072 sc1\n"
      "s_waitcnt vmcnt(0)\n"
      "v_cmp_eq_u32 vcc, -1, v144\n"
      "s_sub_u32 s79, s79, 1\n"
      "s_cmp_lt_i32 s79, 0\n"
      "s_cbranch_scc1 .Lpk_273\n"
      "s_cbranch_vccnz .Lpk_271l\n"
      ".Lpk_273:\n"
      "s_mov_b32 s80, s98\n"
      "s_branch .Lpr_272\n"
      ".Lpk_274:\n"
      "s_mov_b32 s98, s80\n"
      ".Lpk_274l:\n"
      "s_add_u32 s80, s69, s11\n"
      "buffer_load_dwordx4 v[148:151], v28, s[20:23], s80 offen offset:1024 sc1\n"
      "s_waitcnt vmcnt(8)\n"
      "buffer_load_dwordx4 v[152:155], v28, s[20:23], s80 offen offset:2048 sc1\n"
      "s_waitcnt vmcnt(8)\n"
      "buffer_load_dwordx4 v[156:159], v28, s[20:23], s80 offen offset:3072 sc1\n"
      "s_waitcnt vmcnt(0)\n"
      "v_cmp_eq_u32 vcc, -1, v148\n"
      "s_sub_u32 s79, s79, 1\n"
      "s_cmp_lt_i32 s79, 0\n"
      "s_cbranch_scc1 .Lpk_276\n"
      "s_cbranch_vccnz .Lpk_274l\n"
      ".Lpk_276:\n"
      "s_mov_b32 s80, s98\n"
      "s_branch .Lpr_275\n"
      ".Lpk_277:\n"
      "s_mov_b32 s98, s80\n"
      ".Lpk_277l:\n"
      "s_add_u32 s80, s69, s11\n"
      "buffer_load_dwordx4 v[152:155], v28, s[20:23], s80 offen offset:2048 sc1\n"
      "s_waitcnt vmcnt(9)\n"
      "buffer_load_dwordx4 v[156:159], v28, s[20:23], s80 offen offset:3072 sc1\n"
      "s_waitcnt vmcnt(0)\n"
      "v_cmp_eq_u32 vcc, -1, v152\n"
      "s_sub_u32 s79, s79, 1\n"
      "s_cmp_lt_i32 s79, 0\n"
      "s_cbranch_scc1 .Lpk_279\n"
      "s_cbranch_vccnz .Lpk_277l\n"
      ".Lpk_279:\n"
      "s_mov_b32 s80, s98\n"
      "s_branch .Lpr_278\n"
      ".Lpk_280:\n"
      "s_mov_b32 s98, s80\n"
      ".Lpk_280l:\n"
      "s_add_u32 s80, s69, s11\n"
      "buffer_load_dwordx4 v[156:159], v28, s[20:23], s80 offen offset:3072 sc1\n"
      "s_waitcnt vmcnt(0)\n"
      "v_cmp_eq_u32 vcc, -1, v156\n"
      "s_sub_u32 s79, s79, 1\n"
      "s_cmp_lt_i32 s79, 0\n"
      "s_cbranch_scc1 .Lpk_282\n"
      "s_cbranch_vccnz .Lpk_280l\n"
      ".Lpk_282:\n"
      "s_mov_b32 s80, s98\n"
      "s_branch .Lpr_281\n"
      ".Lpk_283:\n"
      "s_mov_b32 s98, s80\n"
      ".Lpk_283l:\n"
      "s_add_u32 s80, s69, s12\n"
      "buffer_load_dwordx4 v[12:15], v28, s[20:23], s80 offen sc1\n"
      "s_waitcnt vmcnt(9)\n"
      "buffer_load_dwordx4 v[244:247], v28, s[20:23], s80 offen offset:1024 sc1\n"
      "s_waitcnt vmcnt(9)\n"
      "buffer_load_dwordx4 v[248:251], v28, s[20:23], s80 offen offset:2048 sc1\n"
      "s_waitcnt vmcnt(9)\n"
      "buffer_load_dwordx4 v[252:255], v28, s[20:23], s80 offen offset:3072 sc1\n"
      "s_waitcnt vmcnt(0)\n"
      "v_cmp_eq_u32 vcc, -1, v12\n"
      "s_sub_u32 s79, s79, 1\n"
      "s_cmp_lt_i32 s79, 0\n"
      "s_cbranch_scc1 .Lpk_285\n"
      "s_cbranch_vccnz .Lpk_283l\n"
      ".Lpk_285:\n"
      "s_mov_b32 s80, s98\n"
      "s_branch .Lpr_284\n"
      ".Lpk_286:\n"
      "s_mov_b32 s98, s80\n"
      ".Lpk_286l:\n"
      "s_add_u32 s80, s69, s12\n"
      "buffer_load_dwordx4 v[244:247], v28, s[20:23], s80 offen offset:1024 sc1\n"
      "s_waitcnt vmcnt(9)\n"
      "buffer_load_dwordx4 v[248:251], v28, s[20:23], s80 offen offset:2048 sc1\n"
      "s_waitcnt vmcnt(9)\n"
      "buffer_load_dwordx4 v[252:255], v28, s[20:23], s80 offen offset:3072 sc1\n"
      "s_waitcnt vmcnt(0)\n"
      "v_cmp_eq_u32 vcc, -1, v244\n"
      "s_sub_u32 s79, s79, 1\n"
      "s_cmp_lt_i32 s79, 0\n"
      "s_cbranch_scc1 .Lpk_288\n"
      "s_cbranch_vccnz .Lpk_286l\n"
      ".Lpk_288:\n"
      "s_mov_b32 s80, s98\n"
      "s_branch .Lpr_287\n"
      ".Lpk_289:\n"
      "s_mov_b32 s98, s80\n"
      ".Lpk_289l:\n"
      "s_add_u32 s80, s69, s12\n"
      "buffer_load_dwordx4 v[248:251], v28, s[20:23], s80 offen offset:2048 sc1\n"
      "s_waitcnt vmcnt(9)\n"
      "buffer_load_dwordx4 v[252:255], v28, s[20:23], s80 offen offset:3072 sc1\n"
      "s_waitcnt vmcnt(0)\n"
      "v_cmp_eq_u32 vcc, -1, v248\n"
      "s_sub_u32 s79, s79, 1\n"
      "s_cmp_lt_i32 s79, 0\n"
      "s_cbranch_scc1 .Lpk_291\n"
      "s_cbranch_vccnz .Lpk_289l\n"
      ".Lpk_291:\n"
      "s_mov_b32 s80, s98\n"
      "s_branch .Lpr_290\n"
      ".Lpk_292:\n"
      "s_mov_b32 s98, s80\n"
      ".Lpk_292l:\n"
      "s_add_u32 s80, s69, s12\n"
      "buffer_load_dwordx4 v[252:255], v28, s[20:23], s80 offen offset:3072 sc1\n"
      "s_waitcnt vmcnt(0)\n"
      "v_cmp_eq_u32 vcc, -1, v252\n"
      "s_sub_u32 s79, s79, 1\n"
      "s_cmp_lt_i32 s79, 0\n"
      "s_cbranch_scc1 .Lpk_294\n"
      "s_cbranch_vccnz .Lpk_292l\n"
      ".Lpk_294:\n"
      "s_mov_b32 s80, s98\n"
      "s_branch .Lpr_293\n"
      ".Lpk_295:\n"
      "s_mov_b32 s98, s80\n"
      ".Lpk_295l:\n"
      "s_add_u32 s80, s69, s13\n"
      "buffer_load_dwordx4 v[128:131], v28, s[20:23], s80 offen sc1\n"
      "s_waitcnt vmcnt(9)\n"
      "buffer_load_dwordx4 v[132:135], v28, s[20:23], s80 offen offset:1024 sc1\n"
      "s_waitcnt vmcnt(9)\n"
      "buffer_load_dwordx4 v[136:139], v28, s[20:23], s80 offen offset:2048 sc1\n"
      "s_waitcnt vmcnt(9)\n"
      "buffer_load_dwordx4 v[140:143], v28, s[20:23], s80 offen offset:3072 sc1\n"
      "s_waitcnt vmcnt(0)\n"
      "v_cmp_eq_u32 vcc, -1, v128\n"
      "s_sub_u32 s79, s79, 1\n"
      "s_cmp_lt_i32 s79, 0\n"
      "s_cbranch_scc1 .Lpk_297\n"
      "s_cbranch_vccnz .Lpk_295l\n"
      ".Lpk_297:\n"
      "s_mov_b32 s80, s98\n"
      "s_branch .Lpr_296\n"
      ".Lpk_298:\n"
      "s_mov_b32 s98, s80\n"
      ".Lpk_298l:\n"
      "s_add_u32 s80, s69, s13\n"
      "buffer_load_dwordx4 v[132:135], v28, s[20:23], s80 offen offset:1024 sc1\n"
      "s_waitcnt vmcnt(9)\n"
      "buffer_load_dwordx4 v[136:139], v28, s[20:23], s80 offen offset:2048 sc1\n"
      "s_waitcnt vmcnt(9)\n"
      "buffer_load_dwordx4 v[140:143], v28, s[20:23], s80 offen offset:3072 sc1\n"
      "s_waitcnt vmcnt(0)\n"
      "v_cmp_eq_u32 vcc, -1, v132\n"
      "s_sub_u32 s79, s79, 1\n"
      "s_cmp_lt_i32 s79, 0\n"
      "s_cbranch_scc1 .Lpk_300\n"
      "s_cbranch_vccnz .Lpk_298l\n"
      ".Lpk_300:\n"
      "s_mov_b32 s80, s98\n"
      "s_branch .Lpr_299\n"
      ".Lpk_301:\n"
      "s_mov_b32 s98, s80\n"
      ".Lpk_301l:\n"
      "s_add_u32 s80, s69, s13\n"
      "buffer_load_dwordx4 v[136:139], v28, s[20:23], s80 offen offset:2048 sc1\n"
      "s_waitcnt vmcnt(9)\n"
      "buffer_load_dwordx4 v[140:143], v28, s[20:23], s80 offen offset:3072 sc1\n"
      "s_waitcnt vmcnt(0)\n"
      "v_cmp_eq_u32 vcc, -1, v136\n"
      "s_sub_u32 s79, s79, 1\n"
      "s_cmp_lt_i32 s79, 0\n"
      "s_cbranch_scc1 .Lpk_303\n"
      "s_cbranch_vccnz .Lpk_301l\n"
      ".Lpk_303:\n"
      "s_mov_b32 s80, s98\n"
      "s_branch .Lpr_302\n"
      ".Lpk_304:\n"
      "s_mov_b32 s98, s80\n"
      ".Lpk_304l:\n"
      "s_add_u32 s80, s69, s13\n"
      "buffer_load_dwordx4 v[140:143], v28, s[20:23], s80 offen offset:3072 sc1\n"
      "s_waitcnt vmcnt(0)\n"
      "v_cmp_eq_u32 vcc, -1, v140\n"
      "s_sub_u32 s79, s79, 1\n"
      "s_cmp_lt_i32 s79, 0\n"
      "s_cbranch_scc1 .Lpk_306\n"
      "s_cbranch_vccnz .Lpk_304l\n"
      ".Lpk_306:\n"
      "s_mov_b32 s80, s98\n"
      "s_branch .Lpr_305\n"
      ".Lpk_307:\n"
      "s_mov_b32 s98, s80\n"
      ".Lpk_307l:\n"
      "s_add_u32 s80, s69, s19\n"
      "buffer_load_dwordx4 v[144:147], v28, s[20:23], s80 offen sc1\n"
      "s_waitcnt vmcnt(9)\n"
      "buffer_load_dwordx4 v[148:151], v28, s[20:23], s80 offen offset:1024 sc1\n"
      "s_waitcnt vmcnt(9)\n"
      "buffer_load_dwordx4 v[152:155], v28, s[20:23], s80 offen offset:2048 sc1\n"
      "s_waitcnt vmcnt(9)\n"
      "buffer_load_dwordx4 v[156:159], v28, s[20:23], s80 offen offset:3072 sc1\n"
      "s_waitcnt vmcnt(0)\n"
      "v_cmp_eq_u32 vcc, -1, v144\n"
      "s_sub_u32 s79, s79, 1\n"
      "s_cmp_lt_i32 s79, 0\n"
      "s_cbranch_scc1 .Lpk_309\n"
      "s_cbranch_vccnz .Lpk_307l\n"
      ".Lpk_309:\n"
      "s_mov_b32 s80, s98\n"
      "s_branch .Lpr_308\n"
      ".Lpk_310:\n"
      "s_mov_b32 s98, s80\n"
      ".Lpk_310l:\n"
      "s_add_u32 s80, s69, s19\n"
      "buffer_load_dwordx4 v[148:151], v28, s[20:23], s80 offen offset:1024 sc1\n"
      "s_waitcnt vmcnt(9)\n"
      "buffer_load_dwordx4 v[152:155], v28, s[20:23], s80 offen offset:2048 sc1\n"
      "s_waitcnt vmcnt(9)\n"
      "buffer_load_dwordx4 v[156:159], v28, s[20:23], s80 offen offset:3072 sc1\n"
      "s_waitcnt vmcnt(0)\n"
      "v_cmp_eq_u32 vcc, -1, v148\n"
      "s_sub_u32 s79, s79, 1\n"
      "s_cmp_lt_i32 s79, 0\n"
      "s_cbranch_scc1 .Lpk_312\n"
      "s_cbranch_vccnz .Lpk_310l\n"
      ".Lpk_312:\n"
      "s_mov_b32 s80, s98\n"
      "s_branch .Lpr_311\n"
      ".Lpk_313:\n"
      "s_mov_b32 s98, s80\n"
      ".Lpk_313l:\n"
      "s_add_u32 s80, s69, s19\n"
      "buffer_load_dwordx4 v[152:155], v28, s[20:23], s80 offen offset:2048 sc1\n"
      "s_waitcnt vmcnt(9)\n"
      "buffer_load_dwordx4 v[156:159], v28, s[20:23], s80 offen offset:3072 sc1\n"
      "s_waitcnt vmcnt(0)\n"
      "v_cmp_eq_u32 vcc, -1, v152\n"
      "s_sub_u32 s79, s79, 1\n"
      "s_cmp_lt_i32 s79, 0\n"
      "s_cbranch_scc1 .Lpk_315\n"
      "s_cbranch_vccnz .Lpk_313l\n"
      ".Lpk_315:\n"
      "s_mov_b32 s80, s98\n"
      "s_branch .Lpr_314\n"
      ".Lpk_316:\n"
      "s_mov_b32 s98, s80\n"
      ".Lpk_316l:\n"
      "s_add_u32 s80, s69, s19\n"
      "buffer_load_dwordx4 v[156:159], v28, s[20:23], s80 offen offset:3072 sc1\n"
      "s_waitcnt vmcnt(0)\n"
      "v_cmp_eq_u32 vcc, -1, v156\n"
      "s_sub_u32 s79, s79, 1\n"
      "s_cmp_lt_i32 s79, 0\n"
      "s_cbranch_scc1 .Lpk_318\n"
      "s_cbranch_vccnz .Lpk_316l\n"
      ".Lpk_318:\n"
      "s_mov_b32 s80, s98\n"
      "s_branch .Lpr_317\n"
      ".Lpk_319:\n"
      "s_mov_b32 s98, s80\n"
      ".Lpk_319l:\n"
      "s_add_u32 s80, s69, s99\n"
      "buffer_load_dwordx4 v[12:15], v28, s[20:23], s80 offen sc1\n"
      "s_waitcnt vmcnt(9)\n"
      "buffer_load_dwordx4 v[244:247], v28, s[20:23], s80 offen offset:1024 sc1\n"
      "s_waitcnt vmcnt(9)\n"
      "buffer_load_dwordx4 v[248:251], v28, s[20:23], s80 offen offset:2048 sc1\n"
      "s_waitcnt vmcnt(9)\n"
      "buffer_load_dwordx4 v[252:255], v28, s[20:23], s80 offen offset:3072 sc1\n"
      "s_waitcnt vmcnt(0)\n"
      "v_cmp_eq_u32 vcc, -1, v12\n"
      "s_sub_u32 s79, s79, 1\n"
      "s_cmp_lt_i32 s79, 0\n"
      "s_cbranch_scc1 .Lpk_321\n"
      "s_cbranch_vccnz .Lpk_319l\n"
      ".Lpk_321:\n"
      "s_mov_b32 s80, s98\n"
      "s_branch .Lpr_320\n"
      ".Lpk_322:\n"
      "s_mov_b32 s98, s80\n"
      ".Lpk_322l:\n"
      "s_add_u32 s80, s69, s99\n"
      "buffer_load_dwordx4 v[244:247], v28, s[20:23], s80 offen offset:1024 sc1\n"
      "s_waitcnt vmcnt(9)\n"
      "buffer_load_dwordx4 v[248:251], v28, s[20:23], s80 offen offset:2048 sc1\n"
      "s_waitcnt vmcnt(9)\n"
      "buffer_load_dwordx4 v[252:255], v28, s[20:23], s80 offen offset:3072 sc1\n"
      "s_waitcnt vmcnt(0)\n"
      "v_cmp_eq_u32 vcc, -1, v244\n"
      "s_sub_u32 s79, s79, 1\n"
      "s_cmp_lt_i32 s79, 0\n"
      "s_cbranch_scc1 .Lpk_324\n"
      "s_cbranch_vccnz .Lpk_322l\n"
      ".Lpk_324:\n"
      "s_mov_b32 s80, s98\n"
      "s_branch .Lpr_323\n"
      ".Lpk_325:\n"
      "s_mov_b32 s98, s80\n"
      ".Lpk_325l:\n"
      "s_add_u32 s80, s69, s99\n"
      "buffer_load_dwordx4 v[248:251], v28, s[20:23], s80 offen offset:2048 sc1\n"
      "s_waitcnt vmcnt(9)\n"
      "buffer_load_dwordx4 v[252:255], v28, s[20:23], s80 offen offset:3072 sc1\n"
      "s_waitcnt vmcnt(0)\n"
      "v_cmp_eq_u32 vcc, -1, v248\n"
      "s_sub_u32 s79, s79, 1\n"
      "s_cmp_lt_i32 s79, 0\n"
      "s_cbranch_scc1 .Lpk_327\n"
      "s_cbranch_vccnz .Lpk_325l\n"
      ".Lpk_327:\n"
      "s_mov_b32 s80, s98\n"
      "s_branch .Lpr_326\n"
      ".Lpk_328:\n"
      "s_mov_b32 s98, s80\n"
      ".Lpk_328l:\n"
      "s_add_u32 s80, s69, s99\n"
      "buffer_load_dwordx4 v[252:255], v28, s[20:23], s80 offen offset:3072 sc1\n"
      "s_waitcnt vmcnt(0)\n"
      "v_cmp_eq_u32 vcc, -1, v252\n"
      "s_sub_u32 s79, s79, 1\n"
      "s_cmp_lt_i32 s79, 0\n"
      "s_cbranch_scc1 .Lpk_330\n"
      "s_cbranch_vccnz .Lpk_328l\n"
      ".Lpk_330:\n"
      "s_mov_b32 s80, s98\n"
      "s_branch .Lpr_329\n"
      ".Lpk_331:\n"
      "s_mov_b32 s98, s80\n"
      ".Lpk_331l:\n"
      "s_add_u32 s80, s69, s16\n"
      "buffer_load_dwordx4 v[128:131], v28, s[20:23], s80 offen sc1\n"
      "s_waitcnt vmcnt(9)\n"
      "buffer_load_dwordx4 v[132:135], v28, s[20:23], s80 offen offset:1024 sc1\n"
      "s_waitcnt vmcnt(9)\n"
      "buffer_load_dwordx4 v[136:139], v28, s[20:23], s80 offen offset:2048 sc1\n"
      "s_waitcnt vmcnt(9)\n"
      "buffer_load_dwordx4 v[140:143], v28, s[20:23], s80 offen offset:3072 sc1\n"
      "s_waitcnt vmcnt(0)\n"
      "v_cmp_eq_u32 vcc, -1, v128\n"
      "s_sub_u32 s79, s79, 1\n"
      "s_cmp_lt_i32 s79, 0\n"
      "s_cbranch_scc1 .Lpk_333\n"
      "s_cbranch_vccnz .Lpk_331l\n"
      ".Lpk_333:\n"
      "s_mov_b32 s80, s98\n"
      "s_branch .Lpr_332\n"
      ".Lpk_334:\n"
      "s_mov_b32 s98, s80\n"
      ".Lpk_334l:\n"
      "s_add_u32 s80, s69, s16\n"
      "buffer_load_dwordx4 v[132:135], v28, s[20:23], s80 offen offset:1024 sc1\n"
      "buffer_load_dwordx4 v[136:139], v28, s[20:23], s80 offen offset:2048 sc1\n"
      "s_waitcnt vmcnt(9)\n"
      "buffer_load_dwordx4 v[140:143], v28, s[20:23], s80 offen offset:3072 sc1\n"
      "s_waitcnt vmcnt(0)\n"
      "v_cmp_eq_u32 vcc, -1, v132\n"
      "s_sub_u32 s79, s79, 1\n"
      "s_cmp_lt_i32 s79, 0\n"
      "s_cbranch_scc1 .Lpk_336\n"
      "s_cbranch_vccnz .Lpk_334l\n"
      ".Lpk_336:\n"
      "s_mov_b32 s80, s98\n"
      "s_branch .Lpr_335\n"
      ".Lpk_337:\n"
      "s_mov_b32 s98, s80\n"
      ".Lpk_337l:\n"
      "s_add_u32 s80, s69, s16\n"
      "buffer_load_dwordx4 v[136:139], v28, s[20:23], s80 offen offset:2048 sc1\n"
      "buffer_load_dwordx4 v[140:143], v28, s[20:23], s80 offen offset:3072 sc1\n"
      "s_waitcnt vmcnt(0)\n"
      "v_cmp_eq_u32 vcc, -1, v136\n"
      "s_sub_u32 s79, s79, 1\n"
      "s_cmp_lt_i32 s79, 0\n"
      "s_cbranch_scc1 .Lpk_339\n"
      "s_cbranch_vccnz .Lpk_337l\n"
      ".Lpk_339:\n"
      "s_mov_b32 s80, s98\n"
      "s_branch .Lpr_338\n"
      ".Lpk_340:\n"
      "s_mov_b32 s98, s80\n"
      ".Lpk_340l:\n"
      "s_add_u32 s80, s69, s16\n"
      "buffer_load_dwordx4 v[140:143], v28, s[20:23], s80 offen offset:3072 sc1\n"
      "s_waitcnt vmcnt(0)\n"
      "v_cmp_eq_u32 vcc, -1, v140\n"
      "s_sub_u32 s79, s79, 1\n"
      "s_cmp_lt_i32 s79, 0\n"
      "s_cbranch_scc1 .Lpk_342\n"
      "s_cbranch_vccnz .Lpk_340l\n"
      ".Lpk_342:\n"
      "s_mov_b32 s80, s98\n"
      "s_branch .Lpr_341\n"
      ".Lb2_fix0:\n"
      "s_mov_b32 s81, s69\n"
      "s_add_u32 s82, s81, 0x7000\n"
      ".Lvf_343:\n"
      "buffer_load_dwordx4 v[208:211], v1, s[20:23], s81 offen sc1\n"
      "buffer_load_dwordx4 v[212:215], v1, s[20:23], s81 offen offset:1024 sc1\n"
      "s_sub_u32 s79, s79, 1\n"
      "s_cmp_lt_i32 s79, 0\n"
      "s_cbranch_scc1 .Lvf_344\n"
      "s_waitcnt vmcnt(1)\n"
      "v_max_u32 v216, v208, v209\n"
      "v_max3_u32 v216, v216, v210, v211\n"
      "s_waitcnt vmcnt(0)\n"
      "v_max3_u32 v216, v216, v212, v213\n"
      "v_max3_u32 v216, v216, v214, v215\n"
      "v_cmp_eq_u32 vcc, -1, v216\n"
      "s_nop 0\n"
      "s_cbranch_vccnz .Lvf_343\n"
      "s_add_u32 s81, s81, 0x800\n"
      "s_cmp_lt_u32 s81, s82\n"
      "s_cbranch_scc1 .Lvf_343\n"
      ".Lvf_344:\n"
      "v_lshlrev_b32 v160, 16, v18\n"
      "v_and_b32 v161, 0xffff0000, v18\n"
      "v_lshlrev_b32 v162, 16, v19\n"
      "v_and_b32 v163, 0xffff0000, v19\n"
      "v_lshlrev_b32 v164, 16, v20\n"
      "v_and_b32 v165, 0xffff0000, v20\n"
      "v_lshlrev_b32 v166, 16, v21\n"
      "v_and_b32 v167, 0xffff0000, v21\n"
      "v_pk_add_f32 v[160:161], v[160:161], v[26:27] op_sel_hi:[1,0]\n"
      "v_pk_add_f32 v[162:163], v[162:163], v[26:27] op_sel_hi:[1,0]\n"
      "v_pk_add_f32 v[164:165], v[164:165], v[26:27] op_sel_hi:[1,0]\n"
      "v_pk_add_f32 v[166:167], v[166:167], v[26:27] op_sel_hi:[1,0]\n"
      "v_mov_b32 v168, 0\n"
      "v_mov_b32 v169, 0\n"
      "v_mov_b32 v170, 0\n"
      "v_mov_b32 v171, 0\n"
      "v_mov_b32 v172, 0\n"
      "v_mov_b32 v173, 0\n"
      "v_mov_b32 v174, 0\n"
      "v_mov_b32 v175, 0\n"
      "s_add_u32 s81, s69, s10\n"
      "s_waitcnt vmcnt(1)\n"
      "buffer_load_dwordx4 v[208:211], v28, s[20:23], s81 offen sc1\n"
      "s_waitcnt vmcnt(1)\n"
      "buffer_load_dwordx4 v[212:215], v28, s[20:23], s81 offen offset:1024 sc1\n"
      "buffer_load_dwordx4 v[216:219], v28, s[20:23], s81 offen offset:2048 sc1\n"
      "buffer_load_dwordx4 v[220:223], v28, s[20:23], s81 offen offset:3072 sc1\n"
      "ds_read_b64_tr_b16 v[196:197], v3 offset:49152\n"
      "ds_read_b64_tr_b16 v[198:199], v3 offset:50176\n"
      "s_waitcnt vmcnt(3) lgkmcnt(0)\n"
      "v_mfma_f32_32x32x16_bf16 v[176:191], v[208:211], v[196:199], 0\n"
      "ds_read_b64_tr_b16 v[192:193], v2 offset:49152\n"
      "ds_read_b64_tr_b16 v[194:195], v2 offset:50176\n"
      "s_waitcnt lgkmcnt(0)\n"
      "v_mfma_f32_32x32x16_bf16 v[160:175], v[208:211], v[192:195], v[160:175]\n"
      "ds_read_b64_tr_b16 v[196:197], v3 offset:51200\n"
      "ds_read_b64_tr_b16 v[198:199], v3 offset:52224\n"
      "s_waitcnt vmcnt(2) lgkmcnt(0)\n"
      "v_mfma_f32_32x32x16_bf16 v[176:191], v[212:215], v[196:199], v[176:191]\n"
      "ds_read_b64_tr_b16 v[192:193], v2 offset:51200\n"
      "ds_read_b64_tr_b16 v[194:195], v2 offset:52224\n"
      "s_waitcnt lgkmcnt(0)\n"
      "v_mfma_f32_32x32x16_bf16 v[160:175], v[212:215], v[192:195], v[160:175]\n"
      "s_waitcnt vmcnt(1)\n"
      "v_mfma_f32_32x32x16_bf16 v[176:191], v[216:219], v[36:39], v[176:191]\n"
      "v_mfma_f32_32x32x16_bf16 v[160:175], v[216:219], v[32:35], v[160:175]\n"
      "s_waitcnt vmcnt(0)\n"
      "v_mfma_f32_32x32x16_bf16 v[176:191], v[220:223], v[44:47], v[176:191]\n"
      "v_mfma_f32_32x32x16_bf16 v[160:175], v[220:223], v[40:43], v[160:175]\n"
      "s_add_u32 s81, s69, s11\n"
      "buffer_load_dwordx4 v[208:211], v28, s[20:23], s81 offen sc1\n"
      "buffer_load_dwordx4 v[212:215], v28, s[20:23], s81 offen offset:1024 sc1\n"
      "buffer_load_dwordx4 v[216:219], v28, s[20:23], s81 offen offset:2048 sc1\n"
      "buffer_load_dwordx4 v[220:223], v28, s[20:23], s81 offen offset:3072 sc1\n"
      "ds_read_b64_tr_b16 v[196:197], v5 offset:0\n"
      "ds_read_b64_tr_b16 v[198:199], v5 offset:1024\n"
      "s_waitcnt vmcnt(3) lgkmcnt(0)\n"
      "v_mfma_f32_32x32x16_bf16 v[176:191], v[208:211], v[196:199], v[176:191]\n"
      "ds_read_b64_tr_b16 v[192:193], v4 offset:0\n"
      "ds_read_b64_tr_b16 v[194:195], v4 offset:1024\n"
      "s_waitcnt lgkmcnt(0)\n"
      "v_mfma_f32_32x32x16_bf16 v[160:175], v[208:211], v[192:195], v[160:175]\n"
      "ds_read_b64_tr_b16 v[196:197], v5 offset:2048\n"
      "ds_read_b64_tr_b16 v[198:199], v5 offset:3072\n"
      "s_waitcnt vmcnt(2) lgkmcnt(0)\n"
      "v_mfma_f32_32x32x16_bf16 v[176:191], v[212:215], v[196:199], v[176:191]\n"
      "ds_read_b64_tr_b16 v[192:193], v4 offset:2048\n"
      "ds_read_b64_tr_b16 v[194:195], v4 offset:3072\n"
      "s_waitcnt lgkmcnt(0)\n"
      "v_mfma_f32_32x32x16_bf16 v[160:175], v[212:215], v[192:195], v[160:175]\n"
      "s_waitcnt vmcnt(1)\n"
      "v_mfma_f32_32x32x16_bf16 v[176:191], v[216:219], v[52:55], v[176:191]\n"
      "v_mfma_f32_32x32x16_bf16 v[160:175], v[216:219], v[48:51], v[160:175]\n"
      "s_waitcnt vmcnt(0)\n"
      "v_mfma_f32_32x32x16_bf16 v[176:191], v[220:223], v[60:63], v[176:191]\n"
      "v_mfma_f32_32x32x16_bf16 v[160:175], v[220:223], v[56:59], v[160:175]\n"
      "s_add_u32 s81, s69, s12\n"
      "buffer_load_dwordx4 v[208:211], v28, s[20:23], s81 offen sc1\n"
      "buffer_load_dwordx4 v[212:215], v28, s[20:23], s81 offen offset:1024 sc1\n"
      "buffer_load_dwordx4 v[216:219], v28, s[20:23], s81 offen offset:2048 sc1\n"
      "buffer_load_dwordx4 v[220:223], v28, s[20:23], s81 offen offset:3072 sc1\n"
      "ds_read_b64_tr_b16 v[196:197], v5 offset:16384\n"
      "ds_read_b64_tr_b16 v[198:199], v5 offset:17408\n"
      "s_waitcnt vmcnt(3) lgkmcnt(0)\n"
      "v_mfma_f32_32x32x16_bf16 v[176:191], v[208:211], v[196:199], v[176:191]\n"
      "ds_read_b64_tr_b16 v[192:193], v4 offset:16384\n"
      "ds_read_b64_tr_b16 v[194:195], v4 offset:17408\n"
      "s_waitcnt lgkmcnt(0)\n"
      "v_mfma_f32_32x32x16_bf16 v[160:175], v[208:211], v[192:195], v[160:175]\n"
      "ds_read_b64_tr_b16 v[196:197], v5 offset:18432\n"
      "ds_read_b64_tr_b16 v[198:199], v5 offset:19456\n"
      "s_waitcnt vmcnt(2) lgkmcnt(0)\n"
      "v_mfma_f32_32x32x16_bf16 v[176:191], v[212:215], v[196:199], v[176:191]\n"
      "ds_read_b64_tr_b16 v[192:193], v4 offset:18432\n"
      "ds_read_b64_tr_b16 v[194:195], v4 offset:19456\n"
      "s_waitcnt lgkmcnt(0)\n"
      "v_mfma_f32_32x32x16_bf16 v[160:175], v[212:215], v[192:195], v[160:175]\n"
      "ds_read_b64_tr_b16 v[196:197], v3 offset:32768\n"
      "ds_read_b64_tr_b16 v[198:199], v3 offset:33792\n"
      "s_waitcnt vmcnt(1) lgkmcnt(0)\n"
      "v_mfma_f32_32x32x16_bf16 v[176:191], v[216:219], v[196:199], v[176:191]\n"
      "ds_read_b64_tr_b16 v[192:193], v2 offset:32768\n"
      "ds_read_b64_tr_b16 v[194:195], v2 offset:33792\n"
      "s_waitcnt lgkmcnt(0)\n"
      "v_mfma_f32_32x32x16_bf16 v[160:175], v[216:219], v[192:195], v[160:175]\n"
      "ds_read_b64_tr_b16 v[196:197], v3 offset:34816\n"
      "ds_read_b64_tr_b16 v[198:199], v3 offset:35840\n"
      "s_waitcnt vmcnt(0) lgkmcnt(0)\n"
      "v_mfma_f32_32x32x16_bf16 v[176:191], v[220:223], v[196:199], v[176:191]\n"
      "ds_read_b64_tr_b16 v[192:193], v2 offset:34816\n"
      "ds_read_b64_tr_b16 v[194:195], v2 offset:35840\n"
      "s_waitcnt lgkmcnt(0)\n"
      "v_mfma_f32_32x32x16_bf16 v[160:175], v[220:223], v[192:195], v[160:175]\n"
      "s_add_u32 s81, s69, s13\n"
      "buffer_load_dwordx4 v[208:211], v28, s[20:23], s81 offen sc1\n"
      "buffer_load_dwordx4 v[212:215], v28, s[20:23], s81 offen offset:1024 sc1\n"
      "buffer_load_dwordx4 v[216:219], v28, s[20:23], s81 offen offset:2048 sc1\n"
      "buffer_load_dwordx4 v[220:223], v28, s[20:23], s81 offen offset:3072 sc1\n"
      "s_waitcnt vmcnt(3)\n"
      "v_mfma_f32_32x32x16_bf16 v[176:191], v[208:211], v[68:71], v[176:191]\n"
      "v_mfma_f32_32x32x16_bf16 v[160:175], v[208:211], v[64:67], v[160:175]\n"
      "s_waitcnt vmcnt(2)\n"
      "v_mfma_f32_32x32x16_bf16 v[176:191], v[212:215], v[76:79], v[176:191]\n"
      "v_mfma_f32_32x32x16_bf16 v[160:175], v[212:215], v[72:75], v[160:175]\n"
      "ds_read_b64_tr_b16 v[196:197], v3 offset:16384\n"
      "ds_read_b64_tr_b16 v[198:199], v3 offset:17408\n"
      "s_waitcnt vmcnt(1) lgkmcnt(0)\n"
      "v_mfma_f32_32x32x16_bf16 v[176:191], v[216:219], v[196:199], v[176:191]\n"
      "ds_read_b64_tr_b16 v[192:193], v2 offset:16384\n"
      "ds_read_b64_tr_b16 v[194:195], v2 offset:17408\n"
      "s_waitcnt lgkmcnt(0)\n"
      "v_mfma_f32_32x32x16_bf16 v[160:175], v[216:219], v[192:195], v[160:175]\n"
      "ds_read_b64_tr_b16 v[196:197], v3 offset:18432\n"
      "ds_read_b64_tr_b16 v[198:199], v3 offset:19456\n"
      "s_waitcnt vmcnt(0) lgkmcnt(0)\n"
      "v_mfma_f32_32x32x16_bf16 v[176:191], v[220:223], v[196:199], v[176:191]\n"
      "ds_read_b64_tr_b16 v[192:193], v2 offset:18432\n"
      "ds_read_b64_tr_b16 v[194:195], v2 offset:19456\n"
      "s_waitcnt lgkmcnt(0)\n"
      "v_mfma_f32_32x32x16_bf16 v[160:175], v[220:223], v[192:195], v[160:175]\n"
      "s_add_u32 s81, s69, s19\n"
      "buffer_load_dwordx4 v[208:211], v28, s[20:23], s81 offen sc1\n"
      "buffer_load_dwordx4 v[212:215], v28, s[20:23], s81 offen offset:1024 sc1\n"
      "buffer_load_dwordx4 v[216:219], v28, s[20:23], s81 offen offset:2048 sc1\n"
      "buffer_load_dwordx4 v[220:223], v28, s[20:23], s81 offen offset:3072 sc1\n"
      "ds_read_b64_tr_b16 v[196:197], v3 offset:0\n"
      "ds_read_b64_tr_b16 v[198:199], v3 offset:1024\n"
      "s_waitcnt vmcnt(3) lgkmcnt(0)\n"
      "v_mfma_f32_32x32x16_bf16 v[176:191], v[208:211], v[196:199], v[176:191]\n"
      "ds_read_b64_tr_b16 v[192:193], v2 offset:0\n"
      "ds_read_b64_tr_b16 v[194:195], v2 offset:1024\n"
      "s_waitcnt lgkmcnt(0)\n"
      "v_mfma_f32_32x32x16_bf16 v[160:175], v[208:211], v[192:195], v[160:175]\n"
      "ds_read_b64_tr_b16 v[196:197], v3 offset:2048\n"
      "ds_read_b64_tr_b16 v[198:199], v3 offset:3072\n"
      "s_waitcnt vmcnt(2) lgkmcnt(0)\n"
      "v_mfma_f32_32x32x16_bf16 v[176:191], v[212:215], v[196:199], v[176:191]\n"
      "ds_read_b64_tr_b16 v[192:193], v2 offset:2048\n"
      "ds_read_b64_tr_b16 v[194:195], v2 offset:3072\n"
      "s_waitcnt lgkmcnt(0)\n"
      "v_mfma_f32_32x32x16_bf16 v[160:175], v[212:215], v[192:195], v[160:175]\n"
      "s_waitcnt vmcnt(1)\n"
      "v_mfma_f32_32x32x16_bf16 v[176:191], v[216:219], v[84:87], v[176:191]\n"
      "v_mfma_f32_32x32x16_bf16 v[160:175], v[216:219], v[80:83], v[160:175]\n"
      "s_waitcnt vmcnt(0)\n"
      "v_mfma_f32_32x32x16_bf16 v[176:191], v[220:223], v[92:95], v[176:191]\n"
      "v_mfma_f32_32x32x16_bf16 v[160:175], v[220:223], v[88:91], v[160:175]\n"
      "s_add_u32 s81, s69, s99\n"
      "buffer_load_dwordx4 v[208:211], v28, s[20:23], s81 offen sc1\n"
      "buffer_load_dwordx4 v[212:215], v28, s[20:23], s81 offen offset:1024 sc1\n"
      "buffer_load_dwordx4 v[216:219], v28, s[20:23], s81 offen offset:2048 sc1\n"
      "buffer_load_dwordx4 v[220:223], v28, s[20:23], s81 offen offset:3072 sc1\n"
      "ds_read_b64_tr_b16 v[196:197], v5 offset:32768\n"
      "ds_read_b64_tr_b16 v[198:199], v5 offset:33792\n"
      "s_waitcnt vmcnt(3) lgkmcnt(0)\n"
      "v_mfma_f32_32x32x16_bf16 v[176:191], v[208:211], v[196:199], v[176:191]\n"
      "ds_read_b64_tr_b16 v[192:193], v4 offset:32768\n"
      "ds_read_b64_tr_b16 v[194:195], v4 offset:33792\n"
      "s_waitcnt lgkmcnt(0)\n"
      "v_mfma_f32_32x32x16_bf16 v[160:175], v[208:211], v[192:195], v[160:175]\n"
      "ds_read_b64_tr_b16 v[196:197], v5 offset:34816\n"
      "ds_read_b64_tr_b16 v[198:199], v5 offset:35840\n"
      "s_waitcnt vmcnt(2) lgkmcnt(0)\n"
      "v_mfma_f32_32x32x16_bf16 v[176:191], v[212:215], v[196:199], v[176:191]\n"
      "ds_read_b64_tr_b16 v[192:193], v4 offset:34816\n"
      "ds_read_b64_tr_b16 v[194:195], v4 offset:35840\n"
      "s_waitcnt lgkmcnt(0)\n"
      "v_mfma_f32_32x32x16_bf16 v[160:175], v[212:215], v[192:195], v[160:175]\n"
      "s_waitcnt vmcnt(1)\n"
      "v_mfma_f32_32x32x16_bf16 v[176:191], v[216:219], v[100:103], v[176:191]\n"
      "v_mfma_f32_32x32x16_bf16 v[160:175], v[216:219], v[96:99], v[160:175]\n"
      "s_waitcnt vmcnt(0)\n"
      "v_mfma_f32_32x32x16_bf16 v[176:191], v[220:223], v[108:111], v[176:191]\n"
      "v_mfma_f32_32x32x16_bf16 v[160:175], v[220:223], v[104:107], v[160:175]\n"
      "s_add_u32 s81, s69, s16\n"
      "buffer_load_dwordx4 v[208:211], v28, s[20:23], s81 offen sc1\n"
      "buffer_load_dwordx4 v[212:215], v28, s[20:23], s81 offen offset:1024 sc1\n"
      "buffer_load_dwordx4 v[216:219], v28, s[20:23], s81 offen offset:2048 sc1\n"
      "buffer_load_dwordx4 v[220:223], v28, s[20:23], s81 offen offset:3072 sc1\n"
      "ds_read_b64_tr_b16 v[196:197], v5 offset:49152\n"
      "ds_read_b64_tr_b16 v[198:199], v5 offset:50176\n"
      "s_waitcnt vmcnt(3) lgkmcnt(0)\n"
      "v_mfma_f32_32x32x16_bf16 v[176:191], v[208:211], v[196:199], v[176:191]\n"
      "ds_read_b64_tr_b16 v[192:193], v4 offset:49152\n"
      "ds_read_b64_tr_b16 v[194:195], v4 offset:50176\n"
      "s_waitcnt lgkmcnt(0)\n"
      "v_mfma_f32_32x32x16_bf16 v[160:175], v[208:211], v[192:195], v[160:175]\n"
      "ds_read_b64_tr_b16 v[196:197], v5 offset:51200\n"
      "ds_read_b64_tr_b16 v[198:199], v5 offset:52224\n"
      "s_waitcnt vmcnt(2) lgkmcnt(0)\n"
      "v_mfma_f32_32x32x16_bf16 v[176:191], v[212:215], v[196:199], v[176:191]\n"
      "ds_read_b64_tr_b16 v[192:193], v4 offset:51200\n"
      "ds_read_b64_tr_b16 v[194:195], v4 offset:52224\n"
      "s_waitcnt lgkmcnt(0)\n"
      "v_mfma_f32_32x32x16_bf16 v[160:175], v[212:215], v[192:195], v[160:175]\n"
      "s_waitcnt vmcnt(1)\n"
      "v_mfma_f32_32x32x16_bf16 v[176:191], v[216:219], v[116:119], v[176:191]\n"
      "v_mfma_f32_32x32x16_bf16 v[160:175], v[216:219], v[112:115], v[160:175]\n"
      "s_waitcnt vmcnt(0)\n"
      "v_mfma_f32_32x32x16_bf16 v[176:191], v[220:223], v[124:127], v[176:191]\n"
      "v_mfma_f32_32x32x16_bf16 v[160:175], v[220:223], v[120:123], v[160:175]\n"
      "s_nop 10\n"
      "ds_write_b128 v9, v[184:187]\n"
      "v_lshlrev_b32 v192, 16, v22\n"
      "v_and_b32 v193, 0xffff0000, v22\n"
      "v_lshlrev_b32 v194, 16, v23\n"
      "v_and_b32 v195, 0xffff0000, v23\n"
      "v_lshlrev_b32 v196, 16, v24\n"
      "v_and_b32 v197, 0xffff0000, v24\n"
      "v_lshlrev_b32 v198, 16, v25\n"
      "v_and_b32 v199, 0xffff0000, v25\n"
      "v_pk_add_f32 v[192:193], v[192:193], v[26:27] op_sel_hi:[1,0]\n"
      "v_pk_add_f32 v[194:195], v[194:195], v[26:27] op_sel_hi:[1,0]\n"
      "v_pk_add_f32 v[196:197], v[196:197], v[26:27] op_sel_hi:[1,0]\n"
      "v_pk_add_f32 v[198:199], v[198:199], v[26:27] op_sel_hi:[1,0]\n"
      "v_mov_b32 v200, 0\n"
      "v_mov_b32 v201, 0\n"
      "v_mov_b32 v202, 0\n"
      "v_mov_b32 v203, 0\n"
      "v_mov_b32 v204, 0\n"
      "v_mov_b32 v205, 0\n"
      "v_mov_b32 v206, 0\n"
      "v_mov_b32 v207, 0\n"
      "s_waitcnt vmcnt(0) lgkmcnt(0)\n"
      "s_nop 15\n"
      "s_nop 7\n"
      "s_branch .Lb2_ok0\n"
      ".Lpk_345:\n"
      "s_mov_b32 s98, s80\n"
      ".Lpk_345l:\n"
      "s_add_u32 s80, s86, s10\n"
      "buffer_load_dwordx4 v[144:147], v28, s[20:23], s80 offen sc1\n"
      "s_waitcnt vmcnt(9)\n"
      "buffer_load_dwordx4 v[148:151], v28, s[20:23], s80 offen offset:1024 sc1\n"
      "s_waitcnt vmcnt(9)\n"
      "buffer_load_dwordx4 v[152:155], v28, s[20:23], s80 offen offset:2048 sc1\n"
      "s_waitcnt vmcnt(9)\n"
      "buffer_load_dwordx4 v[156:159], v28, s[20:23], s80 offen offset:3072 sc1\n"
      "s_waitcnt vmcnt(0)\n"
      "v_cmp_eq_u32 vcc, -1, v144\n"
      "s_sub_u32 s79, s79, 1\n"
      "s_cmp_lt_i32 s79, 0\n"
      "s_cbranch_scc1 .Lpk_347\n"
      "s_cbranch_vccnz .Lpk_345l\n"
      ".Lpk_347:\n"
      "s_mov_b32 s80, s98\n"
      "s_branch .Lpr_346\n"
      ".Lpk_348:\n"
      "s_mov_b32 s98, s80\n"
      ".Lpk_348l:\n"
      "s_add_u32 s80, s86, s10\n"
      "buffer_load_dwordx4 v[148:151], v28, s[20:23], s80 offen offset:1024 sc1\n"
      "s_waitcnt vmcnt(9)\n"
      "buffer_load_dwordx4 v[152:155], v28, s[20:23], s80 offen offset:2048 sc1\n"
      "s_waitcnt vmcnt(9)\n"
      "buffer_load_dwordx4 v[156:159], v28, s[20:23], s80 offen offset:3072 sc1\n"
      "s_waitcnt vmcnt(0)\n"
      "v_cmp_eq_u32 vcc, -1, v148\n"
      "s_sub_u32 s79, s79, 1\n"
      "s_cmp_lt_i32 s79, 0\n"
      "s_cbranch_scc1 .Lpk_350\n"
      "s_cbranch_vccnz .Lpk_348l\n"
      ".Lpk_350:\n"
      "s_mov_b32 s80, s98\n"
      "s_branch .Lpr_349\n"
      ".Lpk_351:\n"
      "s_mov_b32 s98, s80\n"
      ".Lpk_351l:\n"
      "s_add_u32 s80, s86, s10\n"
      "buffer_load_dwordx4 v[152:155], v28, s[20:23], s80 offen offset:2048 sc1\n"
      "s_waitcnt vmcnt(10)\n"
      "buffer_load_dwordx4 v[156:159], v28, s[20:23], s80 offen offset:3072 sc1\n"
      "s_waitcnt vmcnt(0)\n"
      "v_cmp_eq_u32 vcc, -1, v152\n"
      "s_sub_u32 s79, s79, 1\n"
      "s_cmp_lt_i32 s79, 0\n"
      "s_cbranch_scc1 .Lpk_353\n"
      "s_cbranch_vccnz .Lpk_351l\n"
      ".Lpk_353:\n"
      "s_mov_b32 s80, s98\n"
      "s_branch .Lpr_352\n"
      ".Lpk_354:\n"
      "s_mov_b32 s98, s80\n"
      ".Lpk_354l:\n"
      "s_add_u32 s80, s86, s10\n"
      "buffer_load_dwordx4 v[156:159], v28, s[20:23], s80 offen offset:3072 sc1\n"
      "s_waitcnt vmcnt(0)\n"
      "v_cmp_eq_u32 vcc, -1, v156\n"
      "s_sub_u32 s79, s79, 1\n"
      "s_cmp_lt_i32 s79, 0\n"
      "s_cbranch_scc1 .Lpk_356\n"
      "s_cbranch_vccnz .Lpk_354l\n"
      ".Lpk_356:\n"
      "s_mov_b32 s80, s98\n"
      "s_branch .Lpr_355\n"
      ".Lpk_357:\n"
      "s_mov_b32 s98, s80\n"
      ".Lpk_357l:\n"
      "s_add_u32 s80, s86, s11\n"
      "buffer_load_dwordx4 v[12:15], v28, s[20:23], s80 offen sc1\n"
      "s_waitcnt vmcnt(10)\n"
      "buffer_load_dwordx4 v[244:247], v28, s[20:23], s80 offen offset:1024 sc1\n"
      "s_waitcnt vmcnt(10)\n"
      "buffer_load_dwordx4 v[248:251], v28, s[20:23], s80 offen offset:2048 sc1\n"
      "s_waitcnt vmcnt(10)\n"
      "buffer_load_dwordx4 v[252:255], v28, s[20:23], s80 offen offset:3072 sc1\n"
      "s_waitcnt vmcnt(0)\n"
      "v_cmp_eq_u32 vcc, -1, v12\n"
      "s_sub_u32 s79, s79, 1\n"
      "s_cmp_lt_i32 s79, 0\n"
      "s_cbranch_scc1 .Lpk_359\n"
      "s_cbranch_vccnz .Lpk_357l\n"
      ".Lpk_359:\n"
      "s_mov_b32 s80, s98\n"
      "s_branch .Lpr_358\n"
      ".Lpk_360:\n"
      "s_mov_b32 s98, s80\n"
      ".Lpk_360l:\n"
      "s_add_u32 s80, s86, s11\n"
      "buffer_load_dwordx4 v[244:247], v28, s[20:23], s80 offen offset:1024 sc1\n"
      "s_waitcnt vmcnt(10)\n"
      "buffer_load_dwordx4 v[248:251], v28, s[20:23], s80 offen offset:2048 sc1\n"
      "s_waitcnt vmcnt(10)\n"
      "buffer_load_dwordx4 v[252:255], v28, s[20:23], s80 offen offset:3072 sc1\n"
      "s_waitcnt vmcnt(0)\n"
      "v_cmp_eq_u32 vcc, -1, v244\n"
      "s_sub_u32 s79, s79, 1\n"
      "s_cmp_lt_i32 s79, 0\n"
      "s_cbranch_scc1 .Lpk_362\n"
      "s_cbranch_vccnz .Lpk_360l\n"
      ".Lpk_362:\n"
      "s_mov_b32 s80, s98\n"
      "s_branch .Lpr_361\n"
      ".Lpk_363:\n"
      "s_mov_b32 s98, s80\n"
      ".Lpk_363l:\n"
      "s_add_u32 s80, s86, s11\n"
      "buffer_load_dwordx4 v[248:251], v28, s[20:23], s80 offen offset:2048 sc1\n"
      "s_waitcnt vmcnt(10)\n"
      "buffer_load_dwordx4 v[252:255], v28, s[20:23], s80 offen offset:3072 sc1\n"
      "s_waitcnt vmcnt(0)\n"
      "v_cmp_eq_u32 vcc, -1, v248\n"
      "s_sub_u32 s79, s79, 1\n"
      "s_cmp_lt_i32 s79, 0\n"
      "s_cbranch_scc1 .Lpk_365\n"
      "s_cbranch_vccnz .Lpk_363l\n"
      ".Lpk_365:\n"
      "s_mov_b32 s80, s98\n"
      "s_branch .Lpr_364\n"
      ".Lpk_366:\n"
      "s_mov_b32 s98, s80\n"
      ".Lpk_366l:\n"
      "s_add_u32 s80, s86, s11\n"
      "buffer_load_dwordx4 v[252:255], v28, s[20:23], s80 offen offset:3072 sc1\n"
      "s_waitcnt vmcnt(0)\n"
      "v_cmp_eq_u32 vcc, -1, v252\n"
      "s_sub_u32 s79, s79, 1\n"
      "s_cmp_lt_i32 s79, 0\n"
      "s_cbranch_scc1 .Lpk_368\n"
      "s_cbranch_vccnz .Lpk_366l\n"
      ".Lpk_368:\n"
      "s_mov_b32 s80, s98\n"
      "s_branch .Lpr_367\n"
      ".Lpk_369:\n"
      "s_mov_b32 s98, s80\n"
      ".Lpk_369l:\n"
      "s_add_u32 s80, s86, s12\n"
      "buffer_load_dwordx4 v[128:131], v28, s[20:23], s80 offen sc1\n"
      "s_waitcnt vmcnt(10)\n"
      "buffer_load_dwordx4 v[132:135], v28, s[20:23], s80 offen offset:1024 sc1\n"
      "s_waitcnt vmcnt(10)\n"
      "buffer_load_dwordx4 v[136:139], v28, s[20:23], s80 offen offset:2048 sc1\n"
      "s_waitcnt vmcnt(10)\n"
      "buffer_load_dwordx4 v[140:143], v28, s[20:23], s80 offen offset:3072 sc1\n"
      "s_waitcnt vmcnt(0)\n"
      "v_cmp_eq_u32 vcc, -1, v128\n"
      "s_sub_u32 s79, s79, 1\n"
      "s_cmp_lt_i32 s79, 0\n"
      "s_cbranch_scc1 .Lpk_371\n"
      "s_cbranch_vccnz .Lpk_369l\n"
      ".Lpk_371:\n"
      "s_mov_b32 s80, s98\n"
      "s_branch .Lpr_370\n"
      ".Lpk_372:\n"
      "s_mov_b32 s98, s80\n"
      ".Lpk_372l:\n"
      "s_add_u32 s80, s86, s12\n"
      "buffer_load_dwordx4 v[132:135], v28, s[20:23], s80 offen offset:1024 sc1\n"
      "s_waitcnt vmcnt(10)\n"
      "buffer_load_dwordx4 v[136:139], v28, s[20:23], s80 offen offset:2048 sc1\n"
      "s_waitcnt vmcnt(10)\n"
      "buffer_load_dwordx4 v[140:143], v28, s[20:23], s80 offen offset:3072 sc1\n"
      "s_waitcnt vmcnt(0)\n"
      "v_cmp_eq_u32 vcc, -1, v132\n"
      "s_sub_u32 s79, s79, 1\n"
      "s_cmp_lt_i32 s79, 0\n"
      "s_cbranch_scc1 .Lpk_374\n"
      "s_cbranch_vccnz .Lpk_372l\n"
      ".Lpk_374:\n"
      "s_mov_b32 s80, s98\n"
      "s_branch .Lpr_373\n"
      ".Lpk_375:\n"
      "s_mov_b32 s98, s80\n"
      ".Lpk_375l:\n"
      "s_add_u32 s80, s86, s12\n"
      "buffer_load_dwordx4 v[136:139], v28, s[20:23], s80 offen offset:2048 sc1\n"
      "s_waitcnt vmcnt(10)\n"
      "buffer_load_dwordx4 v[140:143], v28, s[20:23], s80 offen offset:3072 sc1\n"
      "s_waitcnt vmcnt(0)\n"
      "v_cmp_eq_u32 vcc, -1, v136\n"
      "s_sub_u32 s79, s79, 1\n"
      "s_cmp_lt_i32 s79, 0\n"
      "s_cbranch_scc1 .Lpk_377\n"
      "s_cbranch_vccnz .Lpk_375l\n"
      ".Lpk_377:\n"
      "s_mov_b32 s80, s98\n"
      "s_branch .Lpr_376\n"
      ".Lpk_378:\n"
      "s_mov_b32 s98, s80\n"
      ".Lpk_378l:\n"
      "s_add_u32 s80, s86, s12\n"
      "buffer_load_dwordx4 v[140:143], v28, s[20:23], s80 offen offset:3072 sc1\n"
      "s_waitcnt vmcnt(0)\n"
      "v_cmp_eq_u32 vcc, -1, v140\n"
      "s_sub_u32 s79, s79, 1\n"
      "s_cmp_lt_i32 s79, 0\n"
      "s_cbranch_scc1 .Lpk_380\n"
      "s_cbranch_vccnz .Lpk_378l\n"
      ".Lpk_380:\n"
      "s_mov_b32 s80, s98\n"
      "s_branch .Lpr_379\n"
      ".Lpk_381:\n"
      "s_mov_b32 s98, s80\n"
      ".Lpk_381l:\n"
      "s_add_u32 s80, s86, s13\n"
      "buffer_load_dwordx4 v[144:147], v28, s[20:23], s80 offen sc1\n"
      "s_waitcnt vmcnt(9)\n"
      "buffer_load_dwordx4 v[148:151], v28, s[20:23], s80 offen offset:1024 sc1\n"
      "s_waitcnt vmcnt(9)\n"
      "buffer_load_dwordx4 v[152:155], v28, s[20:23], s80 offen offset:2048 sc1\n"
      "s_waitcnt vmcnt(9)\n"
      "buffer_load_dwordx4 v[156:159], v28, s[20:23], s80 offen offset:3072 sc1\n"
      "s_waitcnt vmcnt(0)\n"
      "v_cmp_eq_u32 vcc, -1, v144\n"
      "s_sub_u32 s79, s79, 1\n"
      "s_cmp_lt_i32 s79, 0\n"
      "s_cbranch_scc1 .Lpk_383\n"
      "s_cbranch_vccnz .Lpk_381l\n"
      ".Lpk_383:\n"
      "s_mov_b32 s80, s98\n"
      "s_branch .Lpr_382\n"
      ".Lpk_384:\n"
      "s_mov_b32 s98, s80\n"
      ".Lpk_384l:\n"
      "s_add_u32 s80, s86, s13\n"
      "buffer_load_dwordx4 v[148:151], v28, s[20:23], s80 offen offset:1024 sc1\n"
      "s_waitcnt vmcnt(9)\n"
      "buffer_load_dwordx4 v[152:155], v28, s[20:23], s80 offen offset:2048 sc1\n"
      "s_waitcnt vmcnt(9)\n"
      "buffer_load_dwordx4 v[156:159], v28, s[20:23], s80 offen offset:3072 sc1\n"
      "s_waitcnt vmcnt(0)\n"
      "v_cmp_eq_u32 vcc, -1, v148\n"
      "s_sub_u32 s79, s79, 1\n"
      "s_cmp_lt_i32 s79, 0\n"
      "s_cbranch_scc1 .Lpk_386\n"
      "s_cbranch_vccnz .Lpk_384l\n"
      ".Lpk_386:\n"
      "s_mov_b32 s80, s98\n"
      "s_branch .Lpr_385\n"
      ".Lpk_387:\n"
      "s_mov_b32 s98, s80\n"
      ".Lpk_387l:\n"
      "s_add_u32 s80, s86, s13\n"
      "buffer_load_dwordx4 v[152:155], v28, s[20:23], s80 offen offset:2048 sc1\n"
      "s_waitcnt vmcnt(10)\n"
      "buffer_load_dwordx4 v[156:159], v28, s[20:23], s80 offen offset:3072 sc1\n"
      "s_waitcnt vmcnt(0)\n"
      "v_cmp_eq_u32 vcc, -1, v152\n"
      "s_sub_u32 s79, s79, 1\n"
      "s_cmp_lt_i32 s79, 0\n"
      "s_cbranch_scc1 .Lpk_389\n"
      "s_cbranch_vccnz .Lpk_387l\n"
      ".Lpk_389:\n"
      "s_mov_b32 s80, s98\n"
      "s_branch .Lpr_388\n"
      ".Lpk_390:\n"
      "s_mov_b32 s98, s80\n"
      ".Lpk_390l:\n"
      "s_add_u32 s80, s86, s13\n"
      "buffer_load_dwordx4 v[156:159], v28, s[20:23], s80 offen offset:3072 sc1\n"
      "s_waitcnt vmcnt(0)\n"
      "v_cmp_eq_u32 vcc, -1, v156\n"
      "s_sub_u32 s79, s79, 1\n"
      "s_cmp_lt_i32 s79, 0\n"
      "s_cbranch_scc1 .Lpk_392\n"
      "s_cbranch_vccnz .Lpk_390l\n"
      ".Lpk_392:\n"
      "s_mov_b32 s80, s98\n"
      "s_branch .Lpr_391\n"
      ".Lpk_393:\n"
      "s_mov_b32 s98, s80\n"
      ".Lpk_393l:\n"
      "s_add_u32 s80, s86, s19\n"
      "buffer_load_dwordx4 v[12:15], v28, s[20:23], s80 offen sc1\n"
      "s_waitcnt vmcnt(10)\n"
      "buffer_load_dwordx4 v[244:247], v28, s[20:23], s80 offen offset:1024 sc1\n"
      "s_waitcnt vmcnt(10)\n"
      "buffer_load_dwordx4 v[248:251], v28, s[20:23], s80 offen offset:2048 sc1\n"
      "s_waitcnt vmcnt(10)\n"
      "buffer_load_dwordx4 v[252:255], v28, s[20:23], s80 offen offset:3072 sc1\n"
      "s_waitcnt vmcnt(0)\n"
      "v_cmp_eq_u32 vcc, -1, v12\n"
      "s_sub_u32 s79, s79, 1\n"
      "s_cmp_lt_i32 s79, 0\n"
      "s_cbranch_scc1 .Lpk_395\n"
      "s_cbranch_vccnz .Lpk_393l\n"
      ".Lpk_395:\n"
      "s_mov_b32 s80, s98\n"
      "s_branch .Lpr_394\n"
      ".Lpk_396:\n"
      "s_mov_b32 s98, s80\n"
      ".Lpk_396l:\n"
      "s_add_u32 s80, s86, s19\n"
      "buffer_load_dwordx4 v[244:247], v28, s[20:23], s80 offen offset:1024 sc1\n"
      "s_waitcnt vmcnt(10)\n"
      "buffer_load_dwordx4 v[248:251], v28, s[20:23], s80 offen offset:2048 sc1\n"
      "s_waitcnt vmcnt(10)\n"
      "buffer_load_dwordx4 v[252:255], v28, s[20:23], s80 offen offset:3072 sc1\n"
      "s_waitcnt vmcnt(0)\n"
      "v_cmp_eq_u32 vcc, -1, v244\n"
      "s_sub_u32 s79, s79, 1\n"
      "s_cmp_lt_i32 s79, 0\n"
      "s_cbranch_scc1 .Lpk_398\n"
      "s_cbranch_vccnz .Lpk_396l\n"
      ".Lpk_398:\n"
      "s_mov_b32 s80, s98\n"
      "s_branch .Lpr_397\n"
      ".Lpk_399:\n"
      "s_mov_b32 s98, s80\n"
      ".Lpk_399l:\n"
      "s_add_u32 s80, s86, s19\n"
      "buffer_load_dwordx4 v[248:251], v28, s[20:23], s80 offen offset:2048 sc1\n"
      "s_waitcnt vmcnt(10)\n"
      "buffer_load_dwordx4 v[252:255], v28, s[20:23], s80 offen offset:3072 sc1\n"
      "s_waitcnt vmcnt(0)\n"
      "v_cmp_eq_u32 vcc, -1, v248\n"
      "s_sub_u32 s79, s79, 1\n"
      "s_cmp_lt_i32 s79, 0\n"
      "s_cbranch_scc1 .Lpk_401\n"
      "s_cbranch_vccnz .Lpk_399l\n"
      ".Lpk_401:\n"
      "s_mov_b32 s80, s98\n"
      "s_branch .Lpr_400\n"
      ".Lpk_402:\n"
      "s_mov_b32 s98, s80\n"
      ".Lpk_402l:\n"
      "s_add_u32 s80, s86, s19\n"
      "buffer_load_dwordx4 v[252:255], v28, s[20:23], s80 offen offset:3072 sc1\n"
      "s_waitcnt vmcnt(0)\n"
      "v_cmp_eq_u32 vcc, -1, v252\n"
      "s_sub_u32 s79, s79, 1\n"
      "s_cmp_lt_i32 s79, 0\n"
      "s_cbranch_scc1 .Lpk_404\n"
      "s_cbranch_vccnz .Lpk_402l\n"
      ".Lpk_404:\n"
      "s_mov_b32 s80, s98\n"
      "s_branch .Lpr_403\n"
      ".Lpk_405:\n"
      "s_mov_b32 s98, s80\n"
      ".Lpk_405l:\n"
      "s_add_u32 s80, s86, s99\n"
      "buffer_load_dwordx4 v[128:131], v28, s[20:23], s80 offen sc1\n"
      "buffer_load_dwordx4 v[132:135], v28, s[20:23], s80 offen offset:1024 sc1\n"
      "s_waitcnt vmcnt(8)\n"
      "buffer_load_dwordx4 v[136:139], v28, s[20:23], s80 offen offset:2048 sc1\n"
      "s_waitcnt vmcnt(8)\n"
      "buffer_load_dwordx4 v[140:143], v28, s[20:23], s80 offen offset:3072 sc1\n"
      "s_waitcnt vmcnt(0)\n"
      "v_cmp_eq_u32 vcc, -1, v128\n"
      "s_sub_u32 s79, s79, 1\n"
      "s_cmp_lt_i32 s79, 0\n"
      "s_cbranch_scc1 .Lpk_407\n"
      "s_cbranch_vccnz .Lpk_405l\n"
      ".Lpk_407:\n"
      "s_mov_b32 s80, s98\n"
      "s_branch .Lpr_406\n"
      ".Lpk_408:\n"
      "s_mov_b32 s98, s80\n"
      ".Lpk_408l:\n"
      "s_add_u32 s80, s86, s99\n"
      "buffer_load_dwordx4 v[132:135], v28, s[20:23], s80 offen offset:1024 sc1\n"
      "buffer_load_dwordx4 v[136:139], v28, s[20:23], s80 offen offset:2048 sc1\n"
      "s_waitcnt vmcnt(7)\n"
      "buffer_load_dwordx4 v[140:143], v28, s[20:23], s80 offen offset:3072 sc1\n"
      "s_waitcnt vmcnt(0)\n"
      "v_cmp_eq_u32 vcc, -1, v132\n"
      "s_sub_u32 s79, s79, 1\n"
      "s_cmp_lt_i32 s79, 0\n"
      "s_cbranch_scc1 .Lpk_410\n"
      "s_cbranch_vccnz .Lpk_408l\n"
      ".Lpk_410:\n"
      "s_mov_b32 s80, s98\n"
      "s_branch .Lpr_409\n"
      ".Lpk_411:\n"
      "s_mov_b32 s98, s80\n"
      ".Lpk_411l:\n"
      "s_add_u32 s80, s86, s99\n"
      "buffer_load_dwordx4 v[136:139], v28, s[20:23], s80 offen offset:2048 sc1\n"
      "buffer_load_dwordx4 v[140:143], v28, s[20:23], s80 offen offset:3072 sc1\n"
      "s_waitcnt vmcnt(0)\n"
      "v_cmp_eq_u32 vcc, -1, v136\n"
      "s_sub_u32 s79, s79, 1\n"
      "s_cmp_lt_i32 s79, 0\n"
      "s_cbranch_scc1 .Lpk_413\n"
      "s_cbranch_vccnz .Lpk_411l\n"
      ".Lpk_413:\n"
      "s_mov_b32 s80, s98\n"
      "s_branch .Lpr_412\n"
      ".Lpk_414:\n"
      "s_mov_b32 s98, s80\n"
      ".Lpk_414l:\n"
      "s_add_u32 s80, s86, s99\n"
      "buffer_load_dwordx4 v[140:143], v28, s[20:23], s80 offen offset:3072 sc1\n"
      "s_waitcnt vmcnt(0)\n"
      "v_cmp_eq_u32 vcc, -1, v140\n"
      "s_sub_u32 s79, s79, 1\n"
      "s_cmp_lt_i32 s79, 0\n"
      "s_cbranch_scc1 .Lpk_416\n"
      "s_cbranch_vccnz .Lpk_414l\n"
      ".Lpk_416:\n"
      "s_mov_b32 s80, s98\n"
      "s_branch .Lpr_415\n"
      ".Lpk_417:\n"
      "s_mov_b32 s98, s80\n"
      ".Lpk_417l:\n"
      "s_add_u32 s80, s86, s16\n"
      "buffer_load_dwordx4 v[144:147], v28, s[20:23], s80 offen sc1\n"
      "buffer_load_dwordx4 v[148:151], v28, s[20:23], s80 offen offset:1024 sc1\n"
      "s_waitcnt vmcnt(3)\n"
      "buffer_load_dwordx4 v[152:155], v28, s[20:23], s80 offen offset:2048 sc1\n"
      "s_waitcnt vmcnt(3)\n"
      "buffer_load_dwordx4 v[156:159], v28, s[20:23], s80 offen offset:3072 sc1\n"
      "s_waitcnt vmcnt(0)\n"
      "v_cmp_eq_u32 vcc, -1, v144\n"
      "s_sub_u32 s79, s79, 1\n"
      "s_cmp_lt_i32 s79, 0\n"
      "s_cbranch_scc1 .Lpk_419\n"
      "s_cbranch_vccnz .Lpk_417l\n"
      ".Lpk_419:\n"
      "s_mov_b32 s80, s98\n"
      "s_branch .Lpr_418\n"
      ".Lpk_420:\n"
      "s_mov_b32 s98, s80\n"
      ".Lpk_420l:\n"
      "s_add_u32 s80, s86, s16\n"
      "buffer_load_dwordx4 v[148:151], v28, s[20:23], s80 offen offset:1024 sc1\n"
      "buffer_load_dwordx4 v[152:155], v28, s[20:23], s80 offen offset:2048 sc1\n"
      "s_waitcnt vmcnt(2)\n"
      "buffer_load_dwordx4 v[156:159], v28, s[20:23], s80 offen offset:3072 sc1\n"
      "s_waitcnt vmcnt(0)\n"
      "v_cmp_eq_u32 vcc, -1, v148\n"
      "s_sub_u32 s79, s79, 1\n"
      "s_cmp_lt_i32 s79, 0\n"
      "s_cbranch_scc1 .Lpk_422\n"
      "s_cbranch_vccnz .Lpk_420l\n"
      ".Lpk_422:\n"
      "s_mov_b32 s80, s98\n"
      "s_branch .Lpr_421\n"
      ".Lpk_423:\n"
      "s_mov_b32 s98, s80\n"
      ".Lpk_423l:\n"
      "s_add_u32 s80, s86, s16\n"
      "buffer_load_dwordx4 v[152:155], v28, s[20:23], s80 offen offset:2048 sc1\n"
      "buffer_load_dwordx4 v[156:159], v28, s[20:23], s80 offen offset:3072 sc1\n"
      "s_waitcnt vmcnt(0)\n"
      "v_cmp_eq_u32 vcc, -1, v152\n"
      "s_sub_u32 s79, s79, 1\n"
      "s_cmp_lt_i32 s79, 0\n"
      "s_cbranch_scc1 .Lpk_425\n"
      "s_cbranch_vccnz .Lpk_423l\n"
      ".Lpk_425:\n"
      "s_mov_b32 s80, s98\n"
      "s_branch .Lpr_424\n"
      ".Lpk_426:\n"
      "s_mov_b32 s98, s80\n"
      ".Lpk_426l:\n"
      "s_add_u32 s80, s86, s16\n"
      "buffer_load_dwordx4 v[156:159], v28, s[20:23], s80 offen offset:3072 sc1\n"
      "s_waitcnt vmcnt(0)\n"
      "v_cmp_eq_u32 vcc, -1, v156\n"
      "s_sub_u32 s79, s79, 1\n"
      "s_cmp_lt_i32 s79, 0\n"
      "s_cbranch_scc1 .Lpk_428\n"
      "s_cbranch_vccnz .Lpk_426l\n"
      ".Lpk_428:\n"
      "s_mov_b32 s80, s98\n"
      "s_branch .Lpr_427\n"
      ".Lpk_432:\n"
      "s_mov_b32 s98, s80\n"
      ".Lpk_432l:\n"
      "s_add_u32 s80, s86, s13\n"
      "buffer_load_dwordx4 v[152:155], v28, s[20:23], s80 offen offset:2048 sc1\n"
      "s_waitcnt vmcnt(10)\n"
      "buffer_load_dwordx4 v[156:159], v28, s[20:23], s80 offen offset:3072 sc1\n"
      "s_waitcnt vmcnt(0)\n"
      "v_cmp_eq_u32 vcc, -1, v152\n"
      "s_sub_u32 s79, s79, 1\n"
      "s_cmp_lt_i32 s79, 0\n"
      "s_cbranch_scc1 .Lpk_434\n"
      "s_cbranch_vccnz .Lpk_432l\n"
      ".Lpk_434:\n"
      "s_mov_b32 s80, s98\n"
      "s_branch .Lpr_433\n"
      ".Lpk_435:\n"
      "s_mov_b32 s98, s80\n"
      ".Lpk_435l:\n"
      "s_add_u32 s80, s86, s13\n"
      "buffer_load_dwordx4 v[156:159], v28, s[20:23], s80 offen offset:3072 sc1\n"
      "s_waitcnt vmcnt(0)\n"
      "v_cmp_eq_u32 vcc, -1, v156\n"
      "s_sub_u32 s79, s79, 1\n"
      "s_cmp_lt_i32 s79, 0\n"
      "s_cbranch_scc1 .Lpk_437\n"
      "s_cbranch_vccnz .Lpk_435l\n"
      ".Lpk_437:\n"
      "s_mov_b32 s80, s98\n"
      "s_branch .Lpr_436\n"
      ".Lpk_438:\n"
      "s_mov_b32 s98, s80\n"
      ".Lpk_438l:\n"
      "s_add_u32 s80, s86, s19\n"
      "buffer_load_dwordx4 v[12:15], v28, s[20:23], s80 offen sc1\n"
      "s_waitcnt vmcnt(12)\n"
      "buffer_load_dwordx4 v[244:247], v28, s[20:23], s80 offen offset:1024 sc1\n"
      "s_waitcnt vmcnt(12)\n"
      "buffer_load_dwordx4 v[248:251], v28, s[20:23], s80 offen offset:2048 sc1\n"
      "s_waitcnt vmcnt(12)\n"
      "buffer_load_dwordx4 v[252:255], v28, s[20:23], s80 offen offset:3072 sc1\n"
      "s_waitcnt vmcnt(0)\n"
      "v_cmp_eq_u32 vcc, -1, v12\n"
      "s_sub_u32 s79, s79, 1\n"
      "s_cmp_lt_i32 s79, 0\n"
      "s_cbranch_scc1 .Lpk_440\n"
      "s_cbranch_vccnz .Lpk_438l\n"
      ".Lpk_440:\n"
      "s_mov_b32 s80, s98\n"
      "s_branch .Lpr_439\n"
      ".Lpk_441:\n"
      "s_mov_b32 s98, s80\n"
      ".Lpk_441l:\n"
      "s_add_u32 s80, s86, s19\n"
      "buffer_load_dwordx4 v[244:247], v28, s[20:23], s80 offen offset:1024 sc1\n"
      "s_waitcnt vmcnt(13)\n"
      "buffer_load_dwordx4 v[248:251], v28, s[20:23], s80 offen offset:2048 sc1\n"
      "s_waitcnt vmcnt(13)\n"
      "buffer_load_dwordx4 v[252:255], v28, s[20:23], s80 offen offset:3072 sc1\n"
      "s_waitcnt vmcnt(0)\n"
      "v_cmp_eq_u32 vcc, -1, v244\n"
      "s_sub_u32 s79, s79, 1\n"
      "s_cmp_lt_i32 s79, 0\n"
      "s_cbranch_scc1 .Lpk_443\n"
      "s_cbranch_vccnz .Lpk_441l\n"
      ".Lpk_443:\n"
      "s_mov_b32 s80, s98\n"
      "s_branch .Lpr_442\n"
      ".Lpk_444:\n"
      "s_mov_b32 s98, s80\n"
      ".Lpk_444l:\n"
      "s_add_u32 s80, s86, s19\n"
      "buffer_load_dwordx4 v[248:251], v28, s[20:23], s80 offen offset:2048 sc1\n"
      "s_waitcnt vmcnt(14)\n"
      "buffer_load_dwordx4 v[252:255], v28, s[20:23], s80 offen offset:3072 sc1\n"
      "s_waitcnt vmcnt(0)\n"
      "v_cmp_eq_u32 vcc, -1, v248\n"
      "s_sub_u32 s79, s79, 1\n"
      "s_cmp_lt_i32 s79, 0\n"
      "s_cbranch_scc1 .Lpk_446\n"
      "s_cbranch_vccnz .Lpk_444l\n"
      ".Lpk_446:\n"
      "s_mov_b32 s80, s98\n"
      "s_branch .Lpr_445\n"
      ".Lpk_447:\n"
      "s_mov_b32 s98, s80\n"
      ".Lpk_447l:\n"
      "s_add_u32 s80, s86, s19\n"
      "buffer_load_dwordx4 v[252:255], v28, s[20:23], s80 offen offset:3072 sc1\n"
      "s_waitcnt vmcnt(0)\n"
      "v_cmp_eq_u32 vcc, -1, v252\n"
      "s_sub_u32 s79, s79, 1\n"
      "s_cmp_lt_i32 s79, 0\n"
      "s_cbranch_scc1 .Lpk_449\n"
      "s_cbranch_vccnz .Lpk_447l\n"
      ".Lpk_449:\n"
      "s_mov_b32 s80, s98\n"
      "s_branch .Lpr_448\n"
      ".Lpk_450:\n"
      "s_mov_b32 s98, s80\n"
      ".Lpk_450l:\n"
      "s_add_u32 s80, s86, s99\n"
      "buffer_load_dwordx4 v[128:131], v28, s[20:23], s80 offen sc1\n"
      "buffer_load_dwordx4 v[132:135], v28, s[20:23], s80 offen offset:1024 sc1\n"
      "s_waitcnt vmcnt(14)\n"
      "buffer_load_dwordx4 v[136:139], v28, s[20:23], s80 offen offset:2048 sc1\n"
      "s_waitcnt vmcnt(14)\n"
      "buffer_load_dwordx4 v[140:143], v28, s[20:23], s80 offen offset:3072 sc1\n"
      "s_waitcnt vmcnt(0)\n"
      "v_cmp_eq_u32 vcc, -1, v128\n"
      "s_sub_u32 s79, s79, 1\n"
      "s_cmp_lt_i32 s79, 0\n"
      "s_cbranch_scc1 .Lpk_452\n"
      "s_cbranch_vccnz .Lpk_450l\n"
      ".Lpk_452:\n"
      "s_mov_b32 s80, s98\n"
      "s_branch .Lpr_451\n"
      ".Lpk_453:\n"
      "s_mov_b32 s98, s80\n"
      ".Lpk_453l:\n"
      "s_add_u32 s80, s86, s99\n"
      "buffer_load_dwordx4 v[132:135], v28, s[20:23], s80 offen offset:1024 sc1\n"
      "buffer_load_dwordx4 v[136:139], v28, s[20:23], s80 offen offset:2048 sc1\n"
      "s_waitcnt vmcnt(13)\n"
      "buffer_load_dwordx4 v[140:143], v28, s[20:23], s80 offen offset:3072 sc1\n"
      "s_waitcnt vmcnt(0)\n"
      "v_cmp_eq_u32 vcc, -1, v132\n"
      "s_sub_u32 s79, s79, 1\n"
      "s_cmp_lt_i32 s79, 0\n"
      "s_cbranch_scc1 .Lpk_455\n"
      "s_cbranch_vccnz .Lpk_453l\n"
      ".Lpk_455:\n"
      "s_mov_b32 s80, s98\n"
      "s_branch .Lpr_454\n"
      ".Lpk_456:\n"
      "s_mov_b32 s98, s80\n"
      ".Lpk_456l:\n"
      "s_add_u32 s80, s86, s99\n"
      "buffer_load_dwordx4 v[136:139], v28, s[20:23], s80 offen offset:2048 sc1\n"
      "buffer_load_dwordx4 v[140:143], v28, s[20:23], s80 offen offset:3072 sc1\n"
      "s_waitcnt vmcnt(0)\n"
      "v_cmp_eq_u32 vcc, -1, v136\n"
      "s_sub_u32 s79, s79, 1\n"
      "s_cmp_lt_i32 s79, 0\n"
      "s_cbranch_scc1 .Lpk_458\n"
      "s_cbranch_vccnz .Lpk_456l\n"
      ".Lpk_458:\n"
      "s_mov_b32 s80, s98\n"
      "s_branch .Lpr_457\n"
      ".Lpk_459:\n"
      "s_mov_b32 s98, s80\n"
      ".Lpk_459l:\n"
      "s_add_u32 s80, s86, s99\n"
      "buffer_load_dwordx4 v[140:143], v28, s[20:23], s80 offen offset:3072 sc1\n"
      "s_waitcnt vmcnt(0)\n"
      "v_cmp_eq_u32 vcc, -1, v140\n"
      "s_sub_u32 s79, s79, 1\n"
      "s_cmp_lt_i32 s79, 0\n"
      "s_cbranch_scc1 .Lpk_461\n"
      "s_cbranch_vccnz .Lpk_459l\n"
      ".Lpk_461:\n"
      "s_mov_b32 s80, s98\n"
      "s_branch .Lpr_460\n"
      ".Lpk_462:\n"
      "s_mov_b32 s98, s80\n"
      ".Lpk_462l:\n"
      "s_add_u32 s80, s86, s16\n"
      "buffer_load_dwordx4 v[144:147], v28, s[20:23], s80 offen sc1\n"
      "buffer_load_dwordx4 v[148:151], v28, s[20:23], s80 offen offset:1024 sc1\n"
      "s_waitcnt vmcnt(9)\n"
      "buffer_load_dwordx4 v[152:155], v28, s[20:23], s80 offen offset:2048 sc1\n"
      "s_waitcnt vmcnt(8)\n"
      "buffer_load_dwordx4 v[156:159], v28, s[20:23], s80 offen offset:3072 sc1\n"
      "s_waitcnt vmcnt(0)\n"
      "v_cmp_eq_u32 vcc, -1, v144\n"
      "s_sub_u32 s79, s79, 1\n"
      "s_cmp_lt_i32 s79, 0\n"
      "s_cbranch_scc1 .Lpk_464\n"
      "s_cbranch_vccnz .Lpk_462l\n"
      ".Lpk_464:\n"
      "s_mov_b32 s80, s98\n"
      "s_branch .Lpr_463\n"
      ".Lpk_465:\n"
      "s_mov_b32 s98, s80\n"
      ".Lpk_465l:\n"
      "s_add_u32 s80, s86, s16\n"
      "buffer_load_dwordx4 v[148:151], v28, s[20:23], s80 offen offset:1024 sc1\n"
      "buffer_load_dwordx4 v[152:155], v28, s[20:23], s80 offen offset:2048 sc1\n"
      "s_waitcnt vmcnt(7)\n"
      "buffer_load_dwordx4 v[156:159], v28, s[20:23], s80 offen offset:3072 sc1\n"
      "s_waitcnt vmcnt(0)\n"
      "v_cmp_eq_u32 vcc, -1, v148\n"
      "s_sub_u32 s79, s79, 1\n"
      "s_cmp_lt_i32 s79, 0\n"
      "s_cbranch_scc1 .Lpk_467\n"
      "s_cbranch_vccnz .Lpk_465l\n"
      ".Lpk_467:\n"
      "s_mov_b32 s80, s98\n"
      "s_branch .Lpr_466\n"
      ".Lpk_468:\n"
      "s_mov_b32 s98, s80\n"
      ".Lpk_468l:\n"
      "s_add_u32 s80, s86, s16\n"
      "buffer_load_dwordx4 v[152:155], v28, s[20:23], s80 offen offset:2048 sc1\n"
      "buffer_load_dwordx4 v[156:159], v28, s[20:23], s80 offen offset:3072 sc1\n"
      "s_waitcnt vmcnt(0)\n"
      "v_cmp_eq_u32 vcc, -1, v152\n"
      "s_sub_u32 s79, s79, 1\n"
      "s_cmp_lt_i32 s79, 0\n"
      "s_cbranch_scc1 .Lpk_470\n"
      "s_cbranch_vccnz .Lpk_468l\n"
      ".Lpk_470:\n"
      "s_mov_b32 s80, s98\n"
      "s_branch .Lpr_469\n"
      ".Lpk_471:\n"
      "s_mov_b32 s98, s80\n"
      ".Lpk_471l:\n"
      "s_add_u32 s80, s86, s16\n"
      "buffer_load_dwordx4 v[156:159], v28, s[20:23], s80 offen offset:3072 sc1\n"
      "s_waitcnt vmcnt(0)\n"
      "v_cmp_eq_u32 vcc, -1, v156\n"
      "s_sub_u32 s79, s79, 1\n"
      "s_cmp_lt_i32 s79, 0\n"
      "s_cbranch_scc1 .Lpk_473\n"
      "s_cbranch_vccnz .Lpk_471l\n"
      ".Lpk_473:\n"
      "s_mov_b32 s80, s98\n"
      "s_branch .Lpr_472\n"
      ".Lb2_fix1:\n"
      "s_add_u32 s81, s69, 0x1c000\n"
      "s_add_u32 s82, s81, 0x7000\n"
      ".Lvf_474:\n"
      "buffer_load_dwordx4 v[176:179], v1, s[20:23], s81 offen sc1\n"
      "buffer_load_dwordx4 v[180:183], v1, s[20:23], s81 offen offset:1024 sc1\n"
      "s_sub_u32 s79, s79, 1\n"
      "s_cmp_lt_i32 s79, 0\n"
      "s_cbranch_scc1 .Lvf_475\n"
      "s_waitcnt vmcnt(1)\n"
      "v_max_u32 v184, v176, v177\n"
      "v_max3_u32 v184, v184, v178, v179\n"
      "s_waitcnt vmcnt(0)\n"
      "v_max3_u32 v184, v184, v180, v181\n"
      "v_max3_u32 v184, v184, v182, v183\n"
      "v_cmp_eq_u32 vcc, -1, v184\n"
      "s_nop 0\n"
      "s_cbranch_vccnz .Lvf_474\n"
      "s_add_u32 s81, s81, 0x800\n"
      "s_cmp_lt_u32 s81, s82\n"
      "s_cbranch_scc1 .Lvf_474\n"
      ".Lvf_475:\n"
      "s_nop 2\n"
      "v_lshlrev_b32 v192, 16, v22\n"
      "v_and_b32 v193, 0xffff0000, v22\n"
      "v_lshlrev_b32 v194, 16, v23\n"
      "v_and_b32 v195, 0xffff0000, v23\n"
      "v_lshlrev_b32 v196, 16, v24\n"
      "v_and_b32 v197, 0xffff0000, v24\n"
      "v_lshlrev_b32 v198, 16, v25\n"
      "v_and_b32 v199, 0xffff0000, v25\n"
      "v_pk_add_f32 v[192:193], v[192:193], v[26:27] op_sel_hi:[1,0]\n"
      "v_pk_add_f32 v[194:195], v[194:195], v[26:27] op_sel_hi:[1,0]\n"
      "v_pk_add_f32 v[196:197], v[196:197], v[26:27] op_sel_hi:[1,0]\n"
      "v_pk_add_f32 v[198:199], v[198:199], v[26:27] op_sel_hi:[1,0]\n"
      "v_mov_b32 v200, 0\n"
      "v_mov_b32 v201, 0\n"
      "v_mov_b32 v202, 0\n"
      "v_mov_b32 v203, 0\n"
      "v_mov_b32 v204, 0\n"
      "v_mov_b32 v205, 0\n"
      "v_mov_b32 v206, 0\n"
      "v_mov_b32 v207, 0\n"
      "s_mul_i32 s78, s19, 64\n"
      "s_add_u32 s78, s78, s75\n"
      "s_mov_b32 m0, s76\n"
      "s_nop 0\n"
      "buffer_load_dwordx4 v6, s[40:43], s78 offen lds\n"
      "s_add_u32 s78, s78, 0x7c00\n"
      "buffer_load_dwordx4 v6, s[40:43], s78 offen offset:1024 lds\n"
      "s_add_u32 s78, s78, 0x7c00\n"
      "buffer_load_dwordx4 v6, s[40:43], s78 offen offset:2048 lds\n"
      "s_add_u32 s78, s78, 0x7c00\n"
      "buffer_load_dwordx4 v6, s[40:43], s78 offen offset:3072 lds\n"
      "s_mul_i32 s78, s13, 64\n"
      "s_add_u32 s78, s78, s75\n"
      "s_add_u32 m0, s76, 0x4000\n"
      "s_add_u32 s78, s78, 0x20000\n"
      "buffer_load_dwordx4 v6, s[40:43], s78 offen lds\n"
      "s_add_u32 s78, s78, 0x7c00\n"
      "buffer_load_dwordx4 v6, s[40:43], s78 offen offset:1024 lds\n"
      "s_add_u32 s78, s78, 0x7c00\n"
      "buffer_load_dwordx4 v6, s[40:43], s78 offen offset:2048 lds\n"
      "s_add_u32 s78, s78, 0x7c00\n"
      "buffer_load_dwordx4 v6, s[40:43], s78 offen offset:3072 lds\n"
      "s_mul_i32 s78, s12, 64\n"
      "s_add_u32 s78, s78, s75\n"
      "s_add_u32 m0, s76, 0x8000\n"
      "s_add_u32 s78, s78, 0x20000\n"
      "buffer_load_dwordx4 v6, s[40:43], s78 offen lds\n"
      "s_add_u32 s78, s78, 0x7c00\n"
      "buffer_load_dwordx4 v6, s[40:43], s78 offen offset:1024 lds\n"
      "s_add_u32 s78, s78, 0x7c00\n"
      "buffer_load_dwordx4 v6, s[40:43], s78 offen offset:2048 lds\n"
      "s_add_u32 s78, s78, 0x7c00\n"
      "buffer_load_dwordx4 v6, s[40:43], s78 offen offset:3072 lds\n"
      "s_mul_i32 s78, s10, 64\n"
      "s_add_u32 s78, s78, s75\n"
      "s_add_u32 m0, s76, 0xc000\n"
      "s_nop 0\n"
      "buffer_load_dwordx4 v6, s[40:43], s78 offen lds\n"
      "s_add_u32 s78, s78, 0x7c00\n"
      "buffer_load_dwordx4 v6, s[40:43], s78 offen offset:1024 lds\n"
      "s_add_u32 s78, s78, 0x7c00\n"
      "buffer_load_dwordx4 v6, s[40:43], s78 offen offset:2048 lds\n"
      "s_add_u32 s78, s78, 0x7c00\n"
      "buffer_load_dwordx4 v6, s[40:43], s78 offen offset:3072 lds\n"
      "s_mul_i32 s78, s11, 64\n"
      "s_add_u32 s78, s78, s75\n"
      "s_add_u32 m0, s76, 0x10000\n"
      "s_nop 0\n"
      "buffer_load_dwordx4 v6, s[40:43], s78 offen lds\n"
      "s_add_u32 s78, s78, 0x7c00\n"
      "buffer_load_dwordx4 v6, s[40:43], s78 offen offset:1024 lds\n"
      "s_add_u32 s78, s78, 0x7c00\n"
      "buffer_load_dwordx4 v6, s[40:43], s78 offen offset:2048 lds\n"
      "s_add_u32 s78, s78, 0x7c00\n"
      "buffer_load_dwordx4 v6, s[40:43], s78 offen offset:3072 lds\n"
      "s_mul_i32 s78, s12, 64\n"
      "s_add_u32 s78, s78, s75\n"
      "s_add_u32 m0, s76, 0x14000\n"
      "s_nop 0\n"
      "buffer_load_dwordx4 v6, s[40:43], s78 offen lds\n"
      "s_add_u32 s78, s78, 0x7c00\n"
      "buffer_load_dwordx4 v6, s[40:43], s78 offen offset:1024 lds\n"
      "s_add_u32 s78, s78, 0x7c00\n"
      "buffer_load_dwordx4 v6, s[40:43], s78 offen offset:2048 lds\n"
      "s_add_u32 s78, s78, 0x7c00\n"
      "buffer_load_dwordx4 v6, s[40:43], s78 offen offset:3072 lds\n"
      "s_waitcnt vmcnt(0)\n"
      "s_add_u32 s81, s69, s10\n"
      "s_add_u32 s81, s81, 0x1c000\n"
      "buffer_load_dwordx4 v[176:179], v28, s[20:23], s81 offen sc1\n"
      "buffer_load_dwordx4 v[180:183], v28, s[20:23], s81 offen offset:1024 sc1\n"
      "buffer_load_dwordx4 v[184:187], v28, s[20:23], s81 offen offset:2048 sc1\n"
      "buffer_load_dwordx4 v[188:191], v28, s[20:23], s81 offen offset:3072 sc1\n"
      "ds_read_b64_tr_b16 v[164:165], v3 offset:49152\n"
      "ds_read_b64_tr_b16 v[166:167], v3 offset:50176\n"
      "s_waitcnt vmcnt(3) lgkmcnt(0)\n"
      "v_mfma_f32_32x32x16_bf16 v[208:223], v[176:179], v[164:167], 0\n"
      "ds_read_b64_tr_b16 v[160:161], v2 offset:49152\n"
      "ds_read_b64_tr_b16 v[162:163], v2 offset:50176\n"
      "s_waitcnt lgkmcnt(0)\n"
      "v_mfma_f32_32x32x16_bf16 v[192:207], v[176:179], v[160:163], v[192:207]\n"
      "ds_read_b64_tr_b16 v[164:165], v3 offset:51200\n"
      "ds_read_b64_tr_b16 v[166:167], v3 offset:52224\n"
      "s_waitcnt vmcnt(2) lgkmcnt(0)\n"
      "v_mfma_f32_32x32x16_bf16 v[208:223], v[180:183], v[164:167], v[208:223]\n"
      "ds_read_b64_tr_b16 v[160:161], v2 offset:51200\n"
      "ds_read_b64_tr_b16 v[162:163], v2 offset:52224\n"
      "s_waitcnt lgkmcnt(0)\n"
      "v_mfma_f32_32x32x16_bf16 v[192:207], v[180:183], v[160:163], v[192:207]\n"
      "s_waitcnt vmcnt(1)\n"
      "v_mfma_f32_32x32x16_bf16 v[208:223], v[184:187], v[36:39], v[208:223]\n"
      "v_mfma_f32_32x32x16_bf16 v[192:207], v[184:187], v[32:35], v[192:207]\n"
      "s_waitcnt vmcnt(0)\n"
      "v_mfma_f32_32x32x16_bf16 v[208:223], v[188:191], v[44:47], v[208:223]\n"
      "v_mfma_f32_32x32x16_bf16 v[192:207], v[188:191], v[40:43], v[192:207]\n"
      "s_add_u32 s81, s69, s11\n"
      "s_add_u32 s81, s81, 0x1c000\n"
      "buffer_load_dwordx4 v[176:179], v28, s[20:23], s81 offen sc1\n"
      "buffer_load_dwordx4 v[180:183], v28, s[20:23], s81 offen offset:1024 sc1\n"
      "buffer_load_dwordx4 v[184:187], v28, s[20:23], s81 offen offset:2048 sc1\n"
      "buffer_load_dwordx4 v[188:191], v28, s[20:23], s81 offen offset:3072 sc1\n"
      "ds_read_b64_tr_b16 v[164:165], v5 offset:0\n"
      "ds_read_b64_tr_b16 v[166:167], v5 offset:1024\n"
      "s_waitcnt vmcnt(3) lgkmcnt(0)\n"
      "v_mfma_f32_32x32x16_bf16 v[208:223], v[176:179], v[164:167], v[208:223]\n"
      "ds_read_b64_tr_b16 v[160:161], v4 offset:0\n"
      "ds_read_b64_tr_b16 v[162:163], v4 offset:1024\n"
      "s_waitcnt lgkmcnt(0)\n"
      "v_mfma_f32_32x32x16_bf16 v[192:207], v[176:179], v[160:163], v[192:207]\n"
      "ds_read_b64_tr_b16 v[164:165], v5 offset:2048\n"
      "ds_read_b64_tr_b16 v[166:167], v5 offset:3072\n"
      "s_waitcnt vmcnt(2) lgkmcnt(0)\n"
      "v_mfma_f32_32x32x16_bf16 v[208:223], v[180:183], v[164:167], v[208:223]\n"
      "ds_read_b64_tr_b16 v[160:161], v4 offset:2048\n"
      "ds_read_b64_tr_b16 v[162:163], v4 offset:3072\n"
      "s_waitcnt lgkmcnt(0)\n"
      "v_mfma_f32_32x32x16_bf16 v[192:207], v[180:183], v[160:163], v[192:207]\n"
      "s_waitcnt vmcnt(1)\n"
      "v_mfma_f32_32x32x16_bf16 v[208:223], v[184:187], v[52:55], v[208:223]\n"
      "v_mfma_f32_32x32x16_bf16 v[192:207], v[184:187], v[48:51], v[192:207]\n"
      "s_waitcnt vmcnt(0)\n"
      "v_mfma_f32_32x32x16_bf16 v[208:223], v[188:191], v[60:63], v[208:223]\n"
      "v_mfma_f32_32x32x16_bf16 v[192:207], v[188:191], v[56:59], v[192:207]\n"
      "s_add_u32 s81, s69, s12\n"
      "s_add_u32 s81, s81, 0x1c000\n"
      "buffer_load_dwordx4 v[176:179], v28, s[20:23], s81 offen sc1\n"
      "buffer_load_dwordx4 v[180:183], v28, s[20:23], s81 offen offset:1024 sc1\n"
      "buffer_load_dwordx4 v[184:187], v28, s[20:23], s81 offen offset:2048 sc1\n"
      "buffer_load_dwordx4 v[188:191], v28, s[20:23], s81 offen offset:3072 sc1\n"
      "ds_read_b64_tr_b16 v[164:165], v5 offset:16384\n"
      "ds_read_b64_tr_b16 v[166:167], v5 offset:17408\n"
      "s_waitcnt vmcnt(3) lgkmcnt(0)\n"
      "v_mfma_f32_32x32x16_bf16 v[208:223], v[176:179], v[164:167], v[208:223]\n"
      "ds_read_b64_tr_b16 v[160:161], v4 offset:16384\n"
      "ds_read_b64_tr_b16 v[162:163], v4 offset:17408\n"
      "s_waitcnt lgkmcnt(0)\n"
      "v_mfma_f32_32x32x16_bf16 v[192:207], v[176:179], v[160:163], v[192:207]\n"
      "ds_read_b64_tr_b16 v[164:165], v5 offset:18432\n"
      "ds_read_b64_tr_b16 v[166:167], v5 offset:19456\n"
      "s_waitcnt vmcnt(2) lgkmcnt(0)\n"
      "v_mfma_f32_32x32x16_bf16 v[208:223], v[180:183], v[164:167], v[208:223]\n"
      "ds_read_b64_tr_b16 v[160:161], v4 offset:18432\n"
      "ds_read_b64_tr_b16 v[162:163], v4 offset:19456\n"
      "s_waitcnt lgkmcnt(0)\n"
      "v_mfma_f32_32x32x16_bf16 v[192:207], v[180:183], v[160:163], v[192:207]\n"
      "ds_read_b64_tr_b16 v[164:165], v3 offset:32768\n"
      "ds_read_b64_tr_b16 v[166:167], v3 offset:33792\n"
      "s_waitcnt vmcnt(1) lgkmcnt(0)\n"
      "v_mfma_f32_32x32x16_bf16 v[208:223], v[184:187], v[164:167], v[208:223]\n"
      "ds_read_b64_tr_b16 v[160:161], v2 offset:32768\n"
      "ds_read_b64_tr_b16 v[162:163], v2 offset:33792\n"
      "s_waitcnt lgkmcnt(0)\n"
      "v_mfma_f32_32x32x16_bf16 v[192:207], v[184:187], v[160:163], v[192:207]\n"
      "ds_read_b64_tr_b16 v[164:165], v3 offset:34816\n"
      "ds_read_b64_tr_b16 v[166:167], v3 offset:35840\n"
      "s_waitcnt vmcnt(0) lgkmcnt(0)\n"
      "v_mfma_f32_32x32x16_bf16 v[208:223], v[188:191], v[164:167], v[208:223]\n"
      "ds_read_b64_tr_b16 v[160:161], v2 offset:34816\n"
      "ds_read_b64_tr_b16 v[162:163], v2 offset:35840\n"
      "s_waitcnt lgkmcnt(0)\n"
      "v_mfma_f32_32x32x16_bf16 v[192:207], v[188:191], v[160:163], v[192:207]\n"
      "s_add_u32 s81, s69, s13\n"
      "s_add_u32 s81, s81, 0x1c000\n"
      "buffer_load_dwordx4 v[176:179], v28, s[20:23], s81 offen sc1\n"
      "buffer_load_dwordx4 v[180:183], v28, s[20:23], s81 offen offset:1024 sc1\n"
      "buffer_load_dwordx4 v[184:187], v28, s[20:23], s81 offen offset:2048 sc1\n"
      "buffer_load_dwordx4 v[188:191], v28, s[20:23], s81 offen offset:3072 sc1\n"
      "s_waitcnt vmcnt(3)\n"
      "v_mfma_f32_32x32x16_bf16 v[208:223], v[176:179], v[68:71], v[208:223]\n"
      "v_mfma_f32_32x32x16_bf16 v[192:207], v[176:179], v[64:67], v[192:207]\n"
      "s_waitcnt vmcnt(2)\n"
      "v_mfma_f32_32x32x16_bf16 v[208:223], v[180:183], v[76:79], v[208:223]\n"
      "v_mfma_f32_32x32x16_bf16 v[192:207], v[180:183], v[72:75], v[192:207]\n"
      "ds_read_b64_tr_b16 v[164:165], v3 offset:16384\n"
      "ds_read_b64_tr_b16 v[166:167], v3 offset:17408\n"
      "s_waitcnt vmcnt(1) lgkmcnt(0)\n"
      "v_mfma_f32_32x32x16_bf16 v[208:223], v[184:187], v[164:167], v[208:223]\n"
      "ds_read_b64_tr_b16 v[160:161], v2 offset:16384\n"
      "ds_read_b64_tr_b16 v[162:163], v2 offset:17408\n"
      "s_waitcnt lgkmcnt(0)\n"
      "v_mfma_f32_32x32x16_bf16 v[192:207], v[184:187], v[160:163], v[192:207]\n"
      "ds_read_b64_tr_b16 v[164:165], v3 offset:18432\n"
      "ds_read_b64_tr_b16 v[166:167], v3 offset:19456\n"
      "s_waitcnt vmcnt(0) lgkmcnt(0)\n"
      "v_mfma_f32_32x32x16_bf16 v[208:223], v[188:191], v[164:167], v[208:223]\n"
      "ds_read_b64_tr_b16 v[160:161], v2 offset:18432\n"
      "ds_read_b64_tr_b16 v[162:163], v2 offset:19456\n"
      "s_waitcnt lgkmcnt(0)\n"
      "v_mfma_f32_32x32x16_bf16 v[192:207], v[188:191], v[160:163], v[192:207]\n"
      "s_add_u32 s81, s69, s19\n"
      "s_add_u32 s81, s81, 0x1c000\n"
      "buffer_load_dwordx4 v[176:179], v28, s[20:23], s81 offen sc1\n"
      "buffer_load_dwordx4 v[180:183], v28, s[20:23], s81 offen offset:1024 sc1\n"
      "buffer_load_dwordx4 v[184:187], v28, s[20:23], s81 offen offset:2048 sc1\n"
      "buffer_load_dwordx4 v[188:191], v28, s[20:23], s81 offen offset:3072 sc1\n"
      "ds_read_b64_tr_b16 v[164:165], v3 offset:0\n"
      "ds_read_b64_tr_b16 v[166:167], v3 offset:1024\n"
      "s_waitcnt vmcnt(3) lgkmcnt(0)\n"
      "v_mfma_f32_32x32x16_bf16 v[208:223], v[176:179], v[164:167], v[208:223]\n"
      "ds_read_b64_tr_b16 v[160:161], v2 offset:0\n"
      "ds_read_b64_tr_b16 v[162:163], v2 offset:1024\n"
      "s_waitcnt lgkmcnt(0)\n"
      "v_mfma_f32_32x32x16_bf16 v[192:207], v[176:179], v[160:163], v[192:207]\n"
      "ds_read_b64_tr_b16 v[164:165], v3 offset:2048\n"
      "ds_read_b64_tr_b16 v[166:167], v3 offset:3072\n"
      "s_waitcnt vmcnt(2) lgkmcnt(0)\n"
      "v_mfma_f32_32x32x16_bf16 v[208:223], v[180:183], v[164:167], v[208:223]\n"
      "ds_read_b64_tr_b16 v[160:161], v2 offset:2048\n"
      "ds_read_b64_tr_b16 v[162:163], v2 offset:3072\n"
      "s_waitcnt lgkmcnt(0)\n"
      "v_mfma_f32_32x32x16_bf16 v[192:207], v[180:183], v[160:163], v[192:207]\n"
      "s_waitcnt vmcnt(1)\n"
      "v_mfma_f32_32x32x16_bf16 v[208:223], v[184:187], v[84:87], v[208:223]\n"
      "v_mfma_f32_32x32x16_bf16 v[192:207], v[184:187], v[80:83], v[192:207]\n"
      "s_waitcnt vmcnt(0)\n"
      "v_mfma_f32_32x32x16_bf16 v[208:223], v[188:191], v[92:95], v[208:223]\n"
      "v_mfma_f32_32x32x16_bf16 v[192:207], v[188:191], v[88:91], v[192:207]\n"
      "s_add_u32 s81, s69, s99\n"
      "s_add_u32 s81, s81, 0x1c000\n"
      "buffer_load_dwordx4 v[176:179], v28, s[20:23], s81 offen sc1\n"
      "buffer_load_dwordx4 v[180:183], v28, s[20:23], s81 offen offset:1024 sc1\n"
      "buffer_load_dwordx4 v[184:187], v28, s[20:23], s81 offen offset:2048 sc1\n"
      "buffer_load_dwordx4 v[188:191], v28, s[20:23], s81 offen offset:3072 sc1\n"
      "ds_read_b64_tr_b16 v[164:165], v5 offset:32768\n"
      "ds_read_b64_tr_b16 v[166:167], v5 offset:33792\n"
      "s_waitcnt vmcnt(3) lgkmcnt(0)\n"
      "v_mfma_f32_32x32x16_bf16 v[208:223], v[176:179], v[164:167], v[208:223]\n"
      "ds_read_b64_tr_b16 v[160:161], v4 offset:32768\n"
      "ds_read_b64_tr_b16 v[162:163], v4 offset:33792\n"
      "s_waitcnt lgkmcnt(0)\n"
      "v_mfma_f32_32x32x16_bf16 v[192:207], v[176:179], v[160:163], v[192:207]\n"
      "ds_read_b64_tr_b16 v[164:165], v5 offset:34816\n"
      "ds_read_b64_tr_b16 v[166:167], v5 offset:35840\n"
      "s_waitcnt vmcnt(2) lgkmcnt(0)\n"
      "v_mfma_f32_32x32x16_bf16 v[208:223], v[180:183], v[164:167], v[208:223]\n"
      "ds_read_b64_tr_b16 v[160:161], v4 offset:34816\n"
      "ds_read_b64_tr_b16 v[162:163], v4 offset:35840\n"
      "s_waitcnt lgkmcnt(0)\n"
      "v_mfma_f32_32x32x16_bf16 v[192:207], v[180:183], v[160:163], v[192:207]\n"
      "s_waitcnt vmcnt(1)\n"
      "v_mfma_f32_32x32x16_bf16 v[208:223], v[184:187], v[100:103], v[208:223]\n"
      "v_mfma_f32_32x32x16_bf16 v[192:207], v[184:187], v[96:99], v[192:207]\n"
      "s_waitcnt vmcnt(0)\n"
      "v_mfma_f32_32x32x16_bf16 v[208:223], v[188:191], v[108:111], v[208:223]\n"
      "v_mfma_f32_32x32x16_bf16 v[192:207], v[188:191], v[104:107], v[192:207]\n"
      "s_add_u32 s81, s69, s16\n"
      "s_add_u32 s81, s81, 0x1c000\n"
      "buffer_load_dwordx4 v[176:179], v28, s[20:23], s81 offen sc1\n"
      "buffer_load_dwordx4 v[180:183], v28, s[20:23], s81 offen offset:1024 sc1\n"
      "buffer_load_dwordx4 v[184:187], v28, s[20:23], s81 offen offset:2048 sc1\n"
      "buffer_load_dwordx4 v[188:191], v28, s[20:23], s81 offen offset:3072 sc1\n"
      "ds_read_b64_tr_b16 v[164:165], v5 offset:49152\n"
      "ds_read_b64_tr_b16 v[166:167], v5 offset:50176\n"
      "s_waitcnt vmcnt(3) lgkmcnt(0)\n"
      "v_mfma_f32_32x32x16_bf16 v[208:223], v[176:179], v[164:167], v[208:223]\n"
      "ds_read_b64_tr_b16 v[160:161], v4 offset:49152\n"
      "ds_read_b64_tr_b16 v[162:163], v4 offset:50176\n"
      "s_waitcnt lgkmcnt(0)\n"
      "v_mfma_f32_32x32x16_bf16 v[192:207], v[176:179], v[160:163], v[192:207]\n"
      "ds_read_b64_tr_b16 v[164:165], v5 offset:51200\n"
      "ds_read_b64_tr_b16 v[166:167], v5 offset:52224\n"
      "s_waitcnt vmcnt(2) lgkmcnt(0)\n"
      "v_mfma_f32_32x32x16_bf16 v[208:223], v[180:183], v[164:167], v[208:223]\n"
      "ds_read_b64_tr_b16 v[160:161], v4 offset:51200\n"
      "ds_read_b64_tr_b16 v[162:163], v4 offset:52224\n"
      "s_waitcnt lgkmcnt(0)\n"
      "v_mfma_f32_32x32x16_bf16 v[192:207], v[180:183], v[160:163], v[192:207]\n"
      "s_waitcnt vmcnt(1)\n"
      "v_mfma_f32_32x32x16_bf16 v[208:223], v[184:187], v[116:119], v[208:223]\n"
      "v_mfma_f32_32x32x16_bf16 v[192:207], v[184:187], v[112:115], v[192:207]\n"
      "s_waitcnt vmcnt(0)\n"
      "v_mfma_f32_32x32x16_bf16 v[208:223], v[188:191], v[124:127], v[208:223]\n"
      "v_mfma_f32_32x32x16_bf16 v[192:207], v[188:191], v[120:123], v[192:207]\n"
      "s_cmp_lg_u32 s53, 0\n"
      "s_cbranch_scc1 .Lb2_fix1b\n"
      "s_add_u32 m0, s76, 0xc000\n"
      "s_add_u32 s78, s85, 0xc000\n"
      "buffer_load_dwordx4 v1, s[48:51], s78 offen sc1 lds\n"
      "buffer_load_dwordx4 v1, s[48:51], s78 offen offset:1024 sc1 lds\n"
      "buffer_load_dwordx4 v1, s[48:51], s78 offen offset:2048 sc1 lds\n"
      "buffer_load_dwordx4 v1, s[48:51], s78 offen offset:3072 sc1 lds\n"
      "s_add_u32 m0, s76, 0x10000\n"
      "s_add_u32 s78, s85, 0x10000\n"
      "buffer_load_dwordx4 v1, s[48:51], s78 offen sc1 lds\n"
      "buffer_load_dwordx4 v1, s[48:51], s78 offen offset:1024 sc1 lds\n"
      "buffer_load_dwordx4 v1, s[48:51], s78 offen offset:2048 sc1 lds\n"
      "buffer_load_dwordx4 v1, s[48:51], s78 offen offset:3072 sc1 lds\n"
      "s_add_u32 m0, s76, 0x14000\n"
      "s_add_u32 s78, s85, 0x14000\n"
      "buffer_load_dwordx4 v1, s[48:51], s78 offen sc1 lds\n"
      "buffer_load_dwordx4 v1, s[48:51], s78 offen offset:1024 sc1 lds\n"
      "buffer_load_dwordx4 v1, s[48:51], s78 offen offset:2048 sc1 lds\n"
      "buffer_load_dwordx4 v1, s[48:51], s78 offen offset:3072 sc1 lds\n"
      "s_add_u32 m0, s76, 0x8000\n"
      "s_add_u32 s78, s85, 0x8000\n"
      "buffer_load_dwordx4 v1, s[48:51], s78 offen sc1 lds\n"
      "buffer_load_dwordx4 v1, s[48:51], s78 offen offset:1024 sc1 lds\n"
      ".Lb2_fix1b:\n"
      "s_nop 8\n"
      "ds_write_b128 v9, v[216:219]\n"
      "ds_write_b128 v9, v[220:223] offset:1024\n"
      "ds_write_b128 v8, v[208:211]\n"
      "ds_write_b128 v8, v[212:215] offset:1024\n"
      "s_waitcnt vmcnt(0) lgkmcnt(0)\n"
      "s_nop 15\n"
      "s_nop 7\n"
      "s_branch .Lb2_ok1\n"
      ".Lgu_89:\n"
      "s_waitcnt vmcnt(0) lgkmcnt(0)\n"
      "s_nop 15\n"
      "s_nop 7\n"
      "s_branch .La1_ok\n"
      ".Lgu_146:\n"
      "s_waitcnt vmcnt(0) lgkmcnt(0)\n"
      "s_nop 15\n"
      "s_nop 7\n"
      "s_branch .La2_ok\n"
      ".Lhf_429:\n"
      "s_mov_b64 s[94:95], vcc\n"
      "s_movk_i32 s81, 0x800\n"
      "v_and_b32 v164, 0x1f0, v1\n"
      ".Lhf_430:\n"
      "buffer_load_dword v165, v164, s[20:23], s81 offen sc1\n"
      "s_waitcnt vmcnt(0)\n"
      "v_cmp_ne_u32 vcc, s56, v165\n"
      "s_sub_u32 s79, s79, 1\n"
      "s_cmp_lt_i32 s79, 0\n"
      "s_cbranch_scc1 .Lhf_431\n"
      "s_cbranch_vccnz .Lhf_430\n"
      ".Lhf_431:\n"
      "s_mov_b64 vcc, s[94:95]\n"
      "s_waitcnt vmcnt(0) lgkmcnt(0)\n"
      "s_nop 15\n"
      "s_nop 7\n"
      "s_branch .Lhf_429r\n"
      ".Lcode_end:\n"
      :
      : "s"(__builtin_amdgcn_kernarg_segment_ptr()), "s"(blockIdx.x), "v"(threadIdx.x)
      : "v1", "v2", "v3", "v4", "v5", "v6", "v7", "v8", "v9", "v10", "v11", "v12", "v13", "v14", "v15", "v16", "v17", "v18", "v19", "v20", "v21", "v22", "v23", "v24", "v25", "v26", "v27", "v28", "v29", "v30", "v31", "v32", "v33", "v34", "v35", "v36", "v37", "v38", "v39", "v40", "v41", "v42", "v43", "v44", "v45", "v46", "v47", "v48", "v49", "v50", "v51", "v52", "v53", "v54", "v55", "v56", "v57", "v58", "v59", "v60", "v61", "v62", "v63", "v64", "v65", "v66", "v67", "v68", "v69", "v70", "v71", "v72", "v73", "v74", "v75", "v76", "v77", "v78", "v79", "v80", "v81", "v82", "v83", "v84", "v85", "v86", "v87", "v88", "v89", "v90", "v91", "v92", "v93", "v94", "v95", "v96", "v97", "v98", "v99", "v100", "v101", "v102", "v103", "v104", "v105", "v106", "v107", "v108", "v109", "v110", "v111", "v112", "v113", "v114", "v115", "v116", "v117", "v118", "v119", "v120", "v121", "v122", "v123", "v124", "v125", "v126", "v127", "v128", "v129", "v130", "v131", "v132", "v133", "v134", "v135", "v136", "v137", "v138", "v139", "v140", "v141", "v142", "v143", "v144", "v145", "v146", "v147", "v148", "v149", "v150", "v151", "v152", "v153", "v154", "v155", "v156", "v157", "v158", "v159", "v160", "v161", "v162", "v163", "v164", "v165", "v166", "v167", "v168", "v169", "v170", "v171", "v172", "v173", "v174", "v175", "v176", "v177", "v178", "v179", "v180", "v181", "v182", "v183", "v184", "v185", "v186", "v187", "v188", "v189", "v190", "v191", "v192", "v193", "v194", "v195", "v196", "v197", "v198", "v199", "v200", "v201", "v202", "v203", "v204", "v205", "v206", "v207", "v208", "v209", "v210", "v211", "v212", "v213", "v214", "v215", "v216", "v217", "v218", "v219", "v220", "v221", "v222", "v223", "v224", "v225", "v226", "v227", "v228", "v229", "v230", "v231", "v232", "v233", "v234", "v235", "v236", "v237", "v238", "v239", "v240", "v241", "v242", "v243", "v244", "v245", "v246", "v247", "v248", "v249", "v250", "v251", "v252", "v253", "v254", "v255", "a0", "a1", "a2", "a3", "a4", "a5", "a6", "a7", "a8", "a9", "a10", "a11", "a12", "a13", "a14", "a15", "a16", "a17", "a18", "a19", "a20", "a21", "a22", "a23", "a24", "a25", "a26", "a27", "a28", "a29", "a30", "a31", "a32", "a33", "a34", "a35", "a36", "a37", "a38", "a39", "a40", "a41", "a42", "a43", "a44", "a45", "a46", "a47", "a48", "a49", "a50", "a51", "a52", "a53", "a54", "a55", "a56", "a57", "a58", "a59", "a60", "a61", "a62", "a63", "a64", "a65", "a66", "a67", "a68", "a69", "a70", "a71", "a72", "a73", "a74", "a75", "a76", "a77", "a78", "a79", "a80", "a81", "a82", "a83", "a84", "a85", "a86", "a87", "a88", "a89", "a90", "a91", "a92", "a93", "a94", "a95", "a96", "a97", "a98", "a99", "a100", "a101", "a102", "a103", "a104", "a105", "a106", "a107", "a108", "a109", "a110", "a111", "a112", "a113", "a114", "a115", "a116", "a117", "a118", "a119", "a120", "a121", "a122", "a123", "a124", "a125", "a126", "a127", "a128", "a129", "a130", "a131", "a132", "a133", "a134", "a135", "a136", "a137", "a138", "a139", "a140", "a141", "a142", "a143", "a144", "a145", "a146", "a147", "a148", "a149", "a150", "a151", "a152", "a153", "a154", "a155", "a156", "a157", "a158", "a159", "a160", "a161", "a162", "a163", "a164", "a165", "a166", "a167", "a168", "a169", "a170", "a171", "a172", "a173", "a174", "a175", "a176", "a177", "a178", "a179", "a180", "a181", "a182", "a183", "a184", "a185", "a186", "a187", "a188", "a189", "a190", "a191", "a192", "a193", "a194", "a195", "a196", "a197", "a198", "a199", "a200", "a201", "a202", "a203", "a204", "a205", "a206", "a207", "a208", "a209", "a210", "a211", "a212", "a213", "a214", "a215", "a216", "a217", "a218", "a219", "a220", "a221", "a222", "a223", "a224", "a225", "a226", "a227", "a228", "a229", "a230", "a231", "a232", "a233", "a234", "a235", "a236", "a237", "a238", "a239", "a240", "a241", "a242", "a243", "a244", "a245", "a246", "a247", "a248", "a249", "a250", "a251", "a252", "a253", "a254", "a255", "s16", "s17", "s18", "s19", "s20", "s21", "s22", "s23", "s24", "s25", "s26", "s27", "s28", "s29", "s30", "s31", "s32", "s33", "s34", "s35", "s36", "s37", "s38", "s39", "s40", "s41", "s42", "s43", "s44", "s45", "s46", "s47", "s48", "s49", "s50", "s51", "s52", "s53", "s54", "s55", "s56", "s57", "s58", "s59", "s60", "s61", "s62", "s63", "s64", "s65", "s66", "s67", "s68", "s69", "s70", "s71", "s72", "s73", "s74", "s75", "s76", "s77", "s78", "s79", "s80", "s81", "s82", "s83", "s84", "s85", "s86", "s87", "s88", "s89", "s90", "s91", "s92", "s93", "s94", "s95", "s96", "s97", "s98", "s99", "vcc", "scc", "memory");
}

extern "C" void kernel_launch(void *const *d_in, const int *in_sizes, int n_in, void *d_out, int out_size, void *d_ws,
                              size_t ws_size, hipStream_t stream) {
  Params p;
  p.x = d_in[0];
  p.wqkv = d_in[1];
  p.wo = d_in[2];
  p.w1 = d_in[4];
  p.b1 = d_in[5];
  p.w2 = d_in[6];
  p.b2 = d_in[7];
  p.out = d_out;
  p.ws = (char *)d_ws + 0;
  (void)hipFuncSetAttribute((const void *)fused_kernel, hipFuncAttributeMaxDynamicSharedMemorySize, LDS_BYTES);
  fused_kernel<<<256, 256, LDS_BYTES, stream>>>(p);
}
```
